```python
import torch

def _softmax(x, axis=-1):
    return torch.softmax(x, dim=axis)
def _gelu(x):
    return torch.nn.functional.gelu(x, approximate="tanh")
def _silu(x):
    return torch.nn.functional.silu(x)
def _sigmoid(x):
    return torch.sigmoid(x)
def _layernorm(x, axis=-1, eps=1e-5):
    m = x.mean(dim=axis, keepdim=True)
    v = x.var(dim=axis, keepdim=True, unbiased=False)
    return (x - m) / torch.sqrt(v + eps)
def _scatter_add(tgt, idx, src, axis=0):
    return tgt.clone().index_add_(axis, idx, src)
def _segment_sum(src, seg, n):
    out = torch.zeros((n,) + src.shape[1:], dtype=src.dtype)
    return out.index_add_(0, seg, src)
def _pad(x, pw):
    flat = tuple(v for pair in reversed(pw) for v in pair)
    return torch.nn.functional.pad(x, flat)

kernel_name = 'recurrent_scan__sliding_block_cached__moe_arch_mod__prompt'

def setup_inputs():

    _w = (lambda a: (lambda t: t.to(torch.bfloat16) if t.is_floating_point() else t)(torch.as_tensor(a)))
    def _A_setup():
        B, Sq, D = 1, 2048, 448
        Hq, Hkv, Sk, Dh = 16, 8, 2048, 256
        assert Hq % Hkv == 0, (Hq, Hkv)
        return {
            "x": _w((lambda *s: torch.randn(*s, dtype=torch.bfloat16) * 0.05)(B, Sq, D)),
            "k_cache": _w((lambda *s: torch.randn(*s, dtype=torch.bfloat16) * 0.8)(B, Hkv, Sk, Dh)),
            "v_cache": _w((lambda *s: torch.randn(*s, dtype=torch.bfloat16) * 0.2)(B, Hkv, Sk, Dh)),
            "wq": _w((lambda *s: torch.randn(*s, dtype=torch.bfloat16) * 0.8)(D, Hq * Dh) * (D ** -0.5)),
            "wo": _w((lambda *s: torch.randn(*s, dtype=torch.bfloat16) * 0.2)(Hq * Dh, D) * ((Hq * Dh) ** -0.5)),
        }
    _A_in = _A_setup()

    def _B_setup():
        B, S, D, F, C = 1, 2048, 448, 1792, 2
        return {
            "x": _w((lambda *s: torch.randn(*s, dtype=torch.bfloat16) * 0.05)(B, S, D)),
            "w_r": _w((lambda *s: torch.randn(*s, dtype=torch.bfloat16) * 0.2)(D)),
            "w_gate": _w((lambda *s: torch.randn(*s, dtype=torch.bfloat16) * 0.1)(D, F)),
            "w_up": _w((lambda *s: torch.randn(*s, dtype=torch.bfloat16) * 0.1)(D, F)),
            "w_down": _w((lambda *s: torch.randn(*s, dtype=torch.bfloat16) * 0.1)(F, D)),
        }
    _B_in = _B_setup()

    return {
        "A_x": _A_in["x"],
        "A_k_cache": _A_in["k_cache"],
        "A_v_cache": _A_in["v_cache"],
        "A_wq": _A_in["wq"],
        "A_wo": _A_in["wo"],
        "B_x": _B_in["x"],
        "B_w_r": _B_in["w_r"],
        "B_w_gate": _B_in["w_gate"],
        "B_w_up": _B_in["w_up"],
        "B_w_down": _B_in["w_down"],
    }

def reference(A_x, A_k_cache, A_v_cache, A_wq, A_wo, B_x, B_w_r, B_w_gate, B_w_up, B_w_down):
    def _A_ref(x, k_cache, v_cache, wq, wo):
        B, Sq, D = x.shape
        Hq, Dh = 16, 256
        Hkv, Sk = k_cache.shape[1], k_cache.shape[2]
        W = min(4096, max(256, Sk // 4))
        rep = Hq // Hkv
        q = (lambda x, *d: x.permute(*d))((x @ wq).reshape(B, Sq, Hq, Dh), 0, 2, 1, 3)
        k = (lambda x, n, axis: torch.repeat_interleave(x, n, dim=axis))(k_cache[:, :, Sk - W:, :], rep, axis=1)
        v = (lambda x, n, axis: torch.repeat_interleave(x, n, dim=axis))(v_cache[:, :, Sk - W:, :], rep, axis=1)
        scores = torch.einsum("bhqd,bhkd->bhqk", q, k) * (Dh ** -0.5)
        p = _softmax(scores, axis=-1)
        o = torch.einsum("bhqk,bhkd->bhqd", p, v)
        return (lambda x, *d: x.permute(*d))(o, 0, 2, 1, 3).reshape(B, Sq, Hq * Dh) @ wo

    def _B_ref(x, w_r, w_gate, w_up, w_down):
        B, S, D = x.shape
        C = 2
        r = x @ w_r
        idx = torch.argsort(-r, axis=-1)[:, :C]
        idx_e = (lambda x, n, axis: torch.repeat_interleave(x, n, dim=axis))(idx.reshape(B, C, 1), D, axis=2)
        x_sel = (lambda x, i, axis: torch.take_along_dim(x, i, dim=axis))(x, idx_e, axis=1)
        g = _silu(x_sel @ w_gate)
        u = x_sel @ w_up
        y = (g * u) @ w_down
        r_sel = _sigmoid((lambda x, i, axis: torch.take_along_dim(x, i, dim=axis))(r, idx, axis=1))
        y = y * r_sel.reshape(B, C, 1)
        b_off = (torch.arange(B) * S).reshape(B, 1)
        flat_idx = (idx + b_off).reshape(B * C)
        zero = (x * 0.0).reshape(B * S, D)
        upd = _scatter_add(zero, flat_idx, y.reshape(B * C, D), axis=0)
        return x + upd.reshape(B, S, D)

    x = A_x
    xn = _layernorm(x, axis=-1)
    s = _A_ref(xn, A_k_cache, A_v_cache, A_wq, A_wo)
    g = _silu(_B_ref(xn, B_w_r, B_w_gate, B_w_up, B_w_down))
    return x + s * g
```

```cpp
#include <hip/hip_runtime.h>
#include <hip/hip_bf16.h>

typedef float f32x4 __attribute__((ext_vector_type(4)));
typedef float f32x2 __attribute__((ext_vector_type(2)));
typedef unsigned u32x4 __attribute__((ext_vector_type(4)));
typedef unsigned u32x2 __attribute__((ext_vector_type(2)));
typedef __bf16 bf16x2 __attribute__((ext_vector_type(2)));
typedef __bf16 bf16x4 __attribute__((ext_vector_type(4)));
typedef __bf16 bf16x8 __attribute__((ext_vector_type(8)));
typedef unsigned short u16;
typedef __amdgpu_buffer_rsrc_t srd_t;
#define DI __device__ __forceinline__
#define LAS __attribute__((address_space(3)))

constexpr int DM = 448, FF = 1792;
constexpr unsigned SLOT = 17376, RING = 8 * SLOT;
constexpr unsigned TAG_OFF = RING, GU_OFF = TAG_OFF + 64, WD_OFF = GU_OFF + 14336, ST_OFF = 6 * SLOT + 4 * 14336 + 1024, LDS_BYTES = ST_OFF + 1024;
static_assert(WD_OFF + 7168 <= ST_OFF && GU_OFF + 16384 + 2048 <= ST_OFF && LDS_BYTES <= 163840, "LDS map");
constexpr unsigned BIAS = 4096;
constexpr unsigned PART_WG = 57344;
constexpr unsigned CTL = 256u * PART_WG;
constexpr unsigned C_EPOCH = 0, C_RC = 0x1000, C_YF = 0x2000, C_Y2 = 0x3000, C_PF = 0x8000, C_YP = 0x28000, CTL_BYTES = C_YP + 224 * 3584;
constexpr unsigned SPIN_CAP = 1u << 18;
constexpr int SC1 = 16;
constexpr int NSIDE = 30;
#define HPRIO 0
#define GPER 5
#define PFG 2
#define GST 2
#define PFG_LOADS const srd_t sg = mk_srd((j & 1) ? wu : wg, DM * FF * 2); \
    const unsigned o_ = ((j >> 1) * 28 + (lane >> 2)) * (FF * 2) + (((bid & 7) * 448) & ~127u) + (lane & 3) * 128; \
    pfa = __builtin_amdgcn_raw_buffer_load_b32(sg, o_, 0, 0); pfb = __builtin_amdgcn_raw_buffer_load_b32(sg, lane < 48 ? o_ : 0x40000000u, 16 * FF * 2, 0);
#define ROT ((j >> 1) + 3 * (j & 1))
#define L2AT 16
#define LVA 3

DI float rbf(float x) { return (float)(__bf16)x; }
DI float lo16(unsigned u) { return __uint_as_float(u << 16); }
DI float hi16(unsigned u) { return __uint_as_float(u & 0xffff0000u); }
DI unsigned pack2(float a, float b) { f32x2 v = {a, b}; return __builtin_bit_cast(unsigned, __builtin_convertvector(v, bf16x2)); }
DI void bar() { asm volatile("s_barrier" ::: "memory"); }
DI void bars(int n) { for (int i = 0; i < n; ++i) bar(); }
DI void drain_vm() { asm volatile("s_waitcnt vmcnt(0)" ::: "memory"); }
DI void drain_lds() { asm volatile("s_waitcnt lgkmcnt(0)" ::: "memory"); }
DI srd_t mk_srd(const void* p, unsigned bytes) { return __builtin_amdgcn_make_buffer_rsrc((void*)p, (short)0, (int)bytes, 0x00020000); }
DI unsigned lane_id() { unsigned l; asm volatile("v_mbcnt_lo_u32_b32 %0, -1, 0\n\tv_mbcnt_hi_u32_b32 %0, -1, %0" : "=v"(l)); return l; }
DI u32x2 ld8(srd_t s, unsigned off) { asm volatile("" ::: "memory"); return __builtin_amdgcn_raw_buffer_load_b64(s, off, 0, SC1); }
DI void st8(srd_t s, unsigned off, unsigned a, unsigned b) { u32x2 v = {a, b}; __builtin_amdgcn_raw_buffer_store_b64(v, s, off, 0, SC1); }
DI float sigm(float v) { return __builtin_amdgcn_rcpf(1.f + __builtin_amdgcn_exp2f(-1.44269504f * v)); }
template <int CTRL> DI unsigned dpp(unsigned x) { return (unsigned)__builtin_amdgcn_mov_dpp((int)x, CTRL, 0xf, 0xf, true); }
DI unsigned umax(unsigned a, unsigned b) { return a > b ? a : b; }
DI unsigned wave_max(unsigned v) {
  v = umax(v, dpp<0xB1>(v)); v = umax(v, dpp<0x4E>(v)); v = umax(v, dpp<0x141>(v)); v = umax(v, dpp<0x140>(v));
  auto a = __builtin_amdgcn_permlane16_swap(v, v, false, false); v = umax(a[0], a[1]);
  auto b = __builtin_amdgcn_permlane32_swap(v, v, false, false); return umax(b[0], b[1]);
}
DI float xrow_sum(float v) {
  auto a = __builtin_amdgcn_permlane16_swap(__float_as_uint(v), __float_as_uint(v), false, false); v = __uint_as_float(a[0]) + __uint_as_float(a[1]);
  auto b = __builtin_amdgcn_permlane32_swap(__float_as_uint(v), __float_as_uint(v), false, false); return __uint_as_float(b[0]) + __uint_as_float(b[1]);
}
DI float fadd1(float a, float b) { float r; asm("v_add_f32 %0, %1, %2" : "=v"(r) : "v"(a), "v"(b)); return r; }
DI float fmul1(float a, float b) { float r; asm("s_nop 0\n\tv_mul_f32 %0, %1, %2" : "=v"(r) : "v"(a), "v"(b)); return r; }
DI float ffma1(float a, float b, float c) { float r; asm("v_fma_f32 %0, %1, %2, %3" : "=v"(r) : "v"(a), "v"(b), "v"(c)); return r; }
#define PIN(x) asm volatile("" : "+v"(x))

struct Ctx {
  srd_t ctl;
  LAS char* lds;
  unsigned t0, t1;
};
DI unsigned watch(const Ctx& c, unsigned lane, unsigned rb, u32x2& f) {
  unsigned ok = (unsigned)__ballot(f[0] == c.t0 && f[1] == c.t1), m = 0;
  f = ld8(c.ctl, lane < 16 ? C_PF + (rb * 8 + (lane & 7)) * 256 + (lane >> 3) * 128 : 0x40000000u);
  ok &= ok >> 1;
  #pragma unroll
  for (int i = 0; i < 8; ++i) m |= ((ok >> (2 * i)) & 1) << i;
  *(volatile LAS unsigned*)(c.lds + TAG_OFF + 32) = m;
  return m;
}

__global__ __launch_bounds__(512, 2) void k_main(const u16* __restrict__ x, const u16* __restrict__ kc, const u16* __restrict__ vc, const u16* __restrict__ wq,
                                                  const u16* __restrict__ wo, const u16* __restrict__ wr, const u16* __restrict__ wg, const u16* __restrict__ wu,
                                                  const u16* __restrict__ wd, char* __restrict__ ws, u16* __restrict__ out) {
  __shared__ __attribute__((aligned(16))) char smem[LDS_BYTES];
  asm volatile(".Lcode_begin:" :: "s"((LAS char*)smem), "s"(x), "s"(kc), "s"(vc), "s"(wq), "s"(wo), "s"(wr), "s"(wg), "s"(wu), "s"(wd), "s"(ws), "s"(out) : "memory");
  const unsigned wid = __builtin_amdgcn_readfirstlane(threadIdx.x >> 6);
  const unsigned bid = blockIdx.x, j = bid >> 3;
  const unsigned kv = 2 * ((bid & 7) >> 1) + (j & 1), rb = 16 * (bid & 1) + (j >> 1);
  const char* xb = (const char*)x + (size_t)rb * 64 * 896;
  const bool pub = kv == (rb & 7);
  const unsigned rot = pub ? 0 : (ROT) % 7;
  Ctx c;
  u32x2 pf0;
  c.lds = (LAS char*)smem;
  const srd_t sw = mk_srd((const char*)wq + kv * 1024, 447 * 8192 + 1024);
  unsigned la[5];
  asm volatile(
  "v_mbcnt_lo_u32_b32 v80, -1, 0\n"
  "v_mbcnt_hi_u32_b32 v80, -1, v80\n"
  "v_lshlrev_b32 v88, 4, v80\n"
  "buffer_load_dwordx4 v[56:59], v88, %7, 0 offen\n"
  "v_lshrrev_b32 v86, 3, v80\n"
  "v_and_b32 v89, 0x70, v88\n"
  "v_mul_u32_u24 v81, 896, v86\n"
  "s_mul_i32 s60, %12, 7168\n"
  "v_add3_u32 v81, v81, v89, s60\n"
  "s_lshl_b32 s61, %9, 7\n"
  "buffer_load_dwordx4 v[0:3], v81, %5, s61 offen \n"
  "s_addk_i32 s61, 128\n"
  "s_cmpk_eq_u32 s61, 896\n"
  "s_cselect_b32 s61, 0, s61\n"
  "buffer_load_dwordx4 v[4:7], v81, %5, s61 offen \n"
  "s_addk_i32 s61, 128\n"
  "s_cmpk_eq_u32 s61, 896\n"
  "s_cselect_b32 s61, 0, s61\n"
  "buffer_load_dwordx4 v[8:11], v81, %5, s61 offen \n"
  "s_addk_i32 s61, 128\n"
  "s_cmpk_eq_u32 s61, 896\n"
  "s_cselect_b32 s61, 0, s61\n"
  "buffer_load_dwordx4 v[12:15], v81, %5, s61 offen \n"
  "s_addk_i32 s61, 128\n"
  "s_cmpk_eq_u32 s61, 896\n"
  "s_cselect_b32 s61, 0, s61\n"
  "buffer_load_dwordx4 v[16:19], v81, %5, s61 offen \n"
  "s_addk_i32 s61, 128\n"
  "s_cmpk_eq_u32 s61, 896\n"
  "s_cselect_b32 s61, 0, s61\n"
  "buffer_load_dwordx4 v[20:23], v81, %5, s61 offen \n"
  "s_addk_i32 s61, 128\n"
  "s_cmpk_eq_u32 s61, 896\n"
  "s_cselect_b32 s61, 0, s61\n"
  "buffer_load_dwordx4 v[24:27], v81, %5, s61 offen \n"
  "s_cmp_lt_u32 %8, 4\n"
  "s_cbranch_scc1 .Lln_old\n"
  "s_setprio 1\n"
  ".Lln_old:\n"
  "v_lshrrev_b32 v85, 5, v80\n"
  "v_lshlrev_b32 v83, 15, v85\n"
  "v_and_b32 v84, 0x1f0, v88\n"
  "v_add_u32 v92, v84, v83\n"
  "v_add_u32 v93, 7104, v92\n"
  "v_add_u32 v94, 14208, v92\n"
  "v_add_u32 v95, 21312, v92\n"
  "s_and_b32 s60, %8, 3\n"
  "s_lshr_b32 s61, %8, 2\n"
  "s_mul_i32 s62, s60, 4360\n"
  "s_mul_i32 s63, s61, 17376\n"
  "s_add_u32 s63, s63, s62\n"
  "s_lshl_b32 s60, s60, 16\n"
  "s_lshl_b32 s61, s61, 9\n"
  "s_add_u32 s60, s60, s61\n"
  "s_lshl_b32 s61, %9, 19\n"
  "s_mov_b32 m0, s63\n"
  "s_add_u32 s64, s60, s61\n"
  "s_getpc_b64 s[66:67]\n"
  ".Lpc_ln:\n"
  "s_add_u32 s66, s66, .Lcode_begin-.Lpc_ln\n"
  "s_addc_u32 s67, s67, -1\n"
  "s_lshl_b32 s60, %11, 3\n"
  "s_add_u32 s60, s60, %8\n"
  "s_lshl_b32 s60, s60, 6\n"
  "s_min_u32 s61, s60, .Lcode_end-.Lcode_begin\n"
  "s_load_dword s65, s[66:67], s61\n"
  "s_addk_i32 s60, 16384\n"
  "s_min_u32 s61, s60, .Lcode_end-.Lcode_begin\n"
  "s_load_dword s65, s[66:67], s61\n"
  "s_addk_i32 s60, 16384\n"
  "s_min_u32 s61, s60, .Lcode_end-.Lcode_begin\n"
  "s_load_dword s65, s[66:67], s61\n"
  "s_addk_i32 s60, 16384\n"
  "s_min_u32 s61, s60, .Lcode_end-.Lcode_begin\n"
  "s_load_dword s65, s[66:67], s61\n"
  "s_addk_i32 s60, 16384\n"
  "v_lshrrev_b32 v82, 4, v80\n"
  "v_and_b32 v84, 3, v80\n"
  "v_bfe_u32 v83, v80, 2, 3\n"
  "v_mul_u32_u24 v83, 1056, v83\n"
  "v_lshl_add_u32 v83, v84, 3, v83\n"
  "v_lshl_add_u32 v245, v85, 9, v83\n"
  "v_add_u32 v245, 69504, v245\n"
  "v_and_b32 v81, 7, v80\n"
  "v_mul_u32_u24 v81, 1056, v81\n"
  "v_lshl_add_u32 v81, v82, 4, v81\n"
  "v_bfe_u32 v83, v80, 3, 1\n"
  "v_lshl_add_u32 v244, v83, 9, v81\n"
  "v_add_u32 v244, 69504, v244\n"
  "v_bfe_u32 v81, v80, 2, 2\n"
  "v_mul_u32_u24 v83, 1088, v81\n"
  "v_lshl_add_u32 v83, v84, 4, v83\n"
  "v_mul_u32_u24 v246, 4360, v82\n"
  "s_and_b32 s60, %8, 1\n"
  "s_mul_i32 s61, s60, 17376\n"
  "v_add3_u32 v246, v246, v83, s61\n"
  "v_add_u32 v247, 69504, v246\n"
  "v_sub_u32 v81, 0, v81\n"
  "v_xor_b32 v81, v81, v82\n"
  "v_and_b32 v81, 3, v81\n"
  "v_and_b32 v83, 15, v80\n"
  "v_lshl_add_u32 v81, v83, 2, v81\n"
  "s_lshr_b32 s61, %8, 1\n"
  "s_mul_i32 s62, s61, 28672\n"
  "s_add_u32 s62, s62, 104256\n"
  "v_lshl_add_u32 v248, v81, 4, s62\n"
  "s_lshl_b32 s62, s60, 1\n"
  "v_add_u32 v81, s62, v85\n"
  "v_sub_u32 v81, 0, v81\n"
  "v_xor_b32 v81, v81, v80\n"
  "v_and_b32 v81, 3, v81\n"
  "v_lshl_add_u32 v81, v86, 2, v81\n"
  "v_and_b32 v83, 4, v80\n"
  "v_lshl_add_u32 v81, v83, 4, v81\n"
  "s_lshr_b32 s61, %12, 1\n"
  "s_mul_i32 s62, s61, 14336\n"
  "s_lshl_b32 s60, s60, 9\n"
  "s_add_u32 s62, s62, s60\n"
  "s_add_u32 s62, s62, 104256\n"
  "v_lshl_add_u32 v87, v81, 4, s62\n"
  "s_lshl_b32 s60, %12, 7\n"
  "s_add_u32 s60, s60, 162624\n"
  "v_lshl_add_u32 v86, v86, 4, s60\n"
  "v_add_u32 v88, 161600, v88\n"
  "v_add_u32 v89, 161600, v89\n"
  "v_mov_b32 v90, 0x3f803f80\n"
  "v_mov_b32 v76, 0\n"
  "v_mov_b32 v77, 0\n"
  "v_mov_b32 v78, 0\n"
  "v_mov_b32 v79, 0\n"
  "v_mov_b32 v98, 0\n"
  "v_mov_b32 v99, 0\n"
  "s_mov_b32 s60, 0xffff0000\n"
  "s_waitcnt vmcnt(6)\n"
  "v_dot2c_f32_bf16 v77, v90, v0\n"
  "v_dot2c_f32_bf16 v79, v0, v0\n"
  "v_dot2c_f32_bf16 v76, v90, v1\n"
  "v_dot2c_f32_bf16 v78, v1, v1\n"
  "v_dot2c_f32_bf16 v77, v90, v2\n"
  "v_dot2c_f32_bf16 v79, v2, v2\n"
  "v_dot2c_f32_bf16 v76, v90, v3\n"
  "v_dot2c_f32_bf16 v78, v3, v3\n"
  "v_lshlrev_b32 v104, 16, v0\n"
  "v_and_b32 v105, s60, v0\n"
  "v_lshlrev_b32 v106, 16, v1\n"
  "v_and_b32 v107, s60, v1\n"
  "v_lshlrev_b32 v108, 16, v2\n"
  "v_and_b32 v109, s60, v2\n"
  "v_lshlrev_b32 v110, 16, v3\n"
  "v_and_b32 v111, s60, v3\n"
  "s_waitcnt vmcnt(5)\n"
  "v_dot2c_f32_bf16 v77, v90, v4\n"
  "v_dot2c_f32_bf16 v79, v4, v4\n"
  "v_dot2c_f32_bf16 v76, v90, v5\n"
  "v_dot2c_f32_bf16 v78, v5, v5\n"
  "v_dot2c_f32_bf16 v77, v90, v6\n"
  "v_dot2c_f32_bf16 v79, v6, v6\n"
  "v_dot2c_f32_bf16 v76, v90, v7\n"
  "v_dot2c_f32_bf16 v78, v7, v7\n"
  "v_lshlrev_b32 v112, 16, v4\n"
  "v_and_b32 v113, s60, v4\n"
  "v_lshlrev_b32 v114, 16, v5\n"
  "v_and_b32 v115, s60, v5\n"
  "v_lshlrev_b32 v116, 16, v6\n"
  "v_and_b32 v117, s60, v6\n"
  "v_lshlrev_b32 v118, 16, v7\n"
  "v_and_b32 v119, s60, v7\n"
  "s_waitcnt vmcnt(4)\n"
  "v_dot2c_f32_bf16 v77, v90, v8\n"
  "v_dot2c_f32_bf16 v79, v8, v8\n"
  "v_dot2c_f32_bf16 v76, v90, v9\n"
  "v_dot2c_f32_bf16 v78, v9, v9\n"
  "v_dot2c_f32_bf16 v77, v90, v10\n"
  "v_dot2c_f32_bf16 v79, v10, v10\n"
  "v_dot2c_f32_bf16 v76, v90, v11\n"
  "v_dot2c_f32_bf16 v78, v11, v11\n"
  "v_lshlrev_b32 v120, 16, v8\n"
  "v_and_b32 v121, s60, v8\n"
  "v_lshlrev_b32 v122, 16, v9\n"
  "v_and_b32 v123, s60, v9\n"
  "v_lshlrev_b32 v124, 16, v10\n"
  "v_and_b32 v125, s60, v10\n"
  "v_lshlrev_b32 v126, 16, v11\n"
  "v_and_b32 v127, s60, v11\n"
  "s_waitcnt vmcnt(3)\n"
  "buffer_load_dwordx4 v92, %6, s64 offen offset:0 lds\n"
  "v_dot2c_f32_bf16 v77, v90, v12\n"
  "v_dot2c_f32_bf16 v79, v12, v12\n"
  "v_dot2c_f32_bf16 v76, v90, v13\n"
  "v_dot2c_f32_bf16 v78, v13, v13\n"
  "v_dot2c_f32_bf16 v77, v90, v14\n"
  "v_dot2c_f32_bf16 v79, v14, v14\n"
  "v_dot2c_f32_bf16 v76, v90, v15\n"
  "v_dot2c_f32_bf16 v78, v15, v15\n"
  "v_lshlrev_b32 v128, 16, v12\n"
  "v_and_b32 v129, s60, v12\n"
  "v_lshlrev_b32 v130, 16, v13\n"
  "v_and_b32 v131, s60, v13\n"
  "v_lshlrev_b32 v132, 16, v14\n"
  "v_and_b32 v133, s60, v14\n"
  "v_lshlrev_b32 v134, 16, v15\n"
  "v_and_b32 v135, s60, v15\n"
  "s_waitcnt vmcnt(3)\n"
  "buffer_load_dwordx4 v93, %6, s64 offen offset:1088 lds\n"
  "v_dot2c_f32_bf16 v77, v90, v16\n"
  "v_dot2c_f32_bf16 v79, v16, v16\n"
  "v_dot2c_f32_bf16 v76, v90, v17\n"
  "v_dot2c_f32_bf16 v78, v17, v17\n"
  "v_dot2c_f32_bf16 v77, v90, v18\n"
  "v_dot2c_f32_bf16 v79, v18, v18\n"
  "v_dot2c_f32_bf16 v76, v90, v19\n"
  "v_dot2c_f32_bf16 v78, v19, v19\n"
  "v_lshlrev_b32 v136, 16, v16\n"
  "v_and_b32 v137, s60, v16\n"
  "v_lshlrev_b32 v138, 16, v17\n"
  "v_and_b32 v139, s60, v17\n"
  "v_lshlrev_b32 v140, 16, v18\n"
  "v_and_b32 v141, s60, v18\n"
  "v_lshlrev_b32 v142, 16, v19\n"
  "v_and_b32 v143, s60, v19\n"
  "s_waitcnt vmcnt(3)\n"
  "buffer_load_dwordx4 v94, %6, s64 offen offset:2176 lds\n"
  "v_dot2c_f32_bf16 v77, v90, v20\n"
  "v_dot2c_f32_bf16 v79, v20, v20\n"
  "v_dot2c_f32_bf16 v76, v90, v21\n"
  "v_dot2c_f32_bf16 v78, v21, v21\n"
  "v_dot2c_f32_bf16 v77, v90, v22\n"
  "v_dot2c_f32_bf16 v79, v22, v22\n"
  "v_dot2c_f32_bf16 v76, v90, v23\n"
  "v_dot2c_f32_bf16 v78, v23, v23\n"
  "v_lshlrev_b32 v144, 16, v20\n"
  "v_and_b32 v145, s60, v20\n"
  "v_lshlrev_b32 v146, 16, v21\n"
  "v_and_b32 v147, s60, v21\n"
  "v_lshlrev_b32 v148, 16, v22\n"
  "v_and_b32 v149, s60, v22\n"
  "v_lshlrev_b32 v150, 16, v23\n"
  "v_and_b32 v151, s60, v23\n"
  "s_waitcnt vmcnt(3)\n"
  "buffer_load_dwordx4 v95, %6, s64 offen offset:3264 lds\n"
  "v_dot2c_f32_bf16 v77, v90, v24\n"
  "v_dot2c_f32_bf16 v79, v24, v24\n"
  "v_dot2c_f32_bf16 v76, v90, v25\n"
  "v_dot2c_f32_bf16 v78, v25, v25\n"
  "v_dot2c_f32_bf16 v77, v90, v26\n"
  "v_dot2c_f32_bf16 v79, v26, v26\n"
  "v_dot2c_f32_bf16 v76, v90, v27\n"
  "v_dot2c_f32_bf16 v78, v27, v27\n"
  "s_add_u32 m0, s63, 34752\n"
  "s_add_u32 s64, s64, 262144\n"
  "s_cmp_eq_u32 %10, 0\n"
  "s_cbranch_scc0 .Lno_wr\n"
  "ds_write_b128 v88, v[56:59]\n"
  "ds_read_b128 v[28:31], v89 offset:0\n"
  "ds_read_b128 v[32:35], v89 offset:128\n"
  "ds_read_b128 v[36:39], v89 offset:256\n"
  "ds_read_b128 v[40:43], v89 offset:384\n"
  "ds_read_b128 v[44:47], v89 offset:512\n"
  "ds_read_b128 v[48:51], v89 offset:640\n"
  "ds_read_b128 v[52:55], v89 offset:768\n"
  ".Lno_wr:\n"
  "v_add_f32 v76, v76, v77\n"
  "v_add_f32 v78, v78, v79\n"
  "s_nop 0\n"
  "v_add_f32_dpp v76, v76, v76 quad_perm:[1,0,3,2] row_mask:0xf bank_mask:0xf\n"
  "v_add_f32_dpp v78, v78, v78 quad_perm:[1,0,3,2] row_mask:0xf bank_mask:0xf\n"
  "s_nop 0\n"
  "v_add_f32_dpp v76, v76, v76 quad_perm:[2,3,0,1] row_mask:0xf bank_mask:0xf\n"
  "v_add_f32_dpp v78, v78, v78 quad_perm:[2,3,0,1] row_mask:0xf bank_mask:0xf\n"
  "s_nop 0\n"
  "v_add_f32_dpp v76, v76, v76 row_half_mirror row_mask:0xf bank_mask:0xf\n"
  "v_add_f32_dpp v78, v78, v78 row_half_mirror row_mask:0xf bank_mask:0xf\n"
  "v_mul_f32 v76, 0x3b124925, v76\n"
  "v_mul_f32 v78, 0x3b124925, v78\n"
  "v_fma_f32 v78, -v76, v76, v78\n"
  "v_max_f32 v78, 0, v78\n"
  "v_cvt_pk_bf16_f32 v78, v76, v78\n"
  "v_lshlrev_b32 v96, 16, v78\n"
  "v_and_b32 v78, 0xffff0000, v78\n"
  "v_add_f32 v78, 0x3727c5ac, v78\n"
  "v_cvt_pk_bf16_f32 v78, v78, v78\n"
  "v_lshlrev_b32 v78, 16, v78\n"
  "v_sqrt_f32 v78, v78\n"
  "s_nop 0\n"
  "v_cvt_pk_bf16_f32 v78, v78, v78\n"
  "v_lshlrev_b32 v97, 16, v78\n"
  "v_rcp_f32 v100, v97\n"
  "s_nop 0\n"
  "v_mul_f32 v100, 0x3db8aa3b, v100\n"
  "v_mul_f32 v102, v100, v96\n"
  "v_pk_fma_f32 v[104:105], v[104:105], v[100:101], v[102:103] op_sel_hi:[1,0,0] neg_lo:[0,0,1] neg_hi:[0,0,1]\n"
  "v_pk_fma_f32 v[106:107], v[106:107], v[100:101], v[102:103] op_sel_hi:[1,0,0] neg_lo:[0,0,1] neg_hi:[0,0,1]\n"
  "v_pk_fma_f32 v[108:109], v[108:109], v[100:101], v[102:103] op_sel_hi:[1,0,0] neg_lo:[0,0,1] neg_hi:[0,0,1]\n"
  "v_pk_fma_f32 v[110:111], v[110:111], v[100:101], v[102:103] op_sel_hi:[1,0,0] neg_lo:[0,0,1] neg_hi:[0,0,1]\n"
  "v_cvt_pk_bf16_f32 v0, v104, v105\n"
  "v_cvt_pk_bf16_f32 v1, v106, v107\n"
  "v_cvt_pk_bf16_f32 v2, v108, v109\n"
  "v_cvt_pk_bf16_f32 v3, v110, v111\n"
  "ds_write_b128 v87, v[0:3] offset:0\n"
  "v_pk_fma_f32 v[112:113], v[112:113], v[100:101], v[102:103] op_sel_hi:[1,0,0] neg_lo:[0,0,1] neg_hi:[0,0,1]\n"
  "v_pk_fma_f32 v[114:115], v[114:115], v[100:101], v[102:103] op_sel_hi:[1,0,0] neg_lo:[0,0,1] neg_hi:[0,0,1]\n"
  "v_pk_fma_f32 v[116:117], v[116:117], v[100:101], v[102:103] op_sel_hi:[1,0,0] neg_lo:[0,0,1] neg_hi:[0,0,1]\n"
  "v_pk_fma_f32 v[118:119], v[118:119], v[100:101], v[102:103] op_sel_hi:[1,0,0] neg_lo:[0,0,1] neg_hi:[0,0,1]\n"
  "v_cvt_pk_bf16_f32 v4, v112, v113\n"
  "v_cvt_pk_bf16_f32 v5, v114, v115\n"
  "v_cvt_pk_bf16_f32 v6, v116, v117\n"
  "v_cvt_pk_bf16_f32 v7, v118, v119\n"
  "ds_write_b128 v87, v[4:7] offset:2048\n"
  "v_pk_fma_f32 v[120:121], v[120:121], v[100:101], v[102:103] op_sel_hi:[1,0,0] neg_lo:[0,0,1] neg_hi:[0,0,1]\n"
  "v_pk_fma_f32 v[122:123], v[122:123], v[100:101], v[102:103] op_sel_hi:[1,0,0] neg_lo:[0,0,1] neg_hi:[0,0,1]\n"
  "v_pk_fma_f32 v[124:125], v[124:125], v[100:101], v[102:103] op_sel_hi:[1,0,0] neg_lo:[0,0,1] neg_hi:[0,0,1]\n"
  "v_pk_fma_f32 v[126:127], v[126:127], v[100:101], v[102:103] op_sel_hi:[1,0,0] neg_lo:[0,0,1] neg_hi:[0,0,1]\n"
  "v_cvt_pk_bf16_f32 v8, v120, v121\n"
  "v_cvt_pk_bf16_f32 v9, v122, v123\n"
  "v_cvt_pk_bf16_f32 v10, v124, v125\n"
  "v_cvt_pk_bf16_f32 v11, v126, v127\n"
  "ds_write_b128 v87, v[8:11] offset:4096\n"
  "v_pk_fma_f32 v[128:129], v[128:129], v[100:101], v[102:103] op_sel_hi:[1,0,0] neg_lo:[0,0,1] neg_hi:[0,0,1]\n"
  "v_pk_fma_f32 v[130:131], v[130:131], v[100:101], v[102:103] op_sel_hi:[1,0,0] neg_lo:[0,0,1] neg_hi:[0,0,1]\n"
  "v_pk_fma_f32 v[132:133], v[132:133], v[100:101], v[102:103] op_sel_hi:[1,0,0] neg_lo:[0,0,1] neg_hi:[0,0,1]\n"
  "v_pk_fma_f32 v[134:135], v[134:135], v[100:101], v[102:103] op_sel_hi:[1,0,0] neg_lo:[0,0,1] neg_hi:[0,0,1]\n"
  "v_cvt_pk_bf16_f32 v12, v128, v129\n"
  "v_cvt_pk_bf16_f32 v13, v130, v131\n"
  "v_cvt_pk_bf16_f32 v14, v132, v133\n"
  "v_cvt_pk_bf16_f32 v15, v134, v135\n"
  "ds_write_b128 v87, v[12:15] offset:6144\n"
  "v_pk_fma_f32 v[136:137], v[136:137], v[100:101], v[102:103] op_sel_hi:[1,0,0] neg_lo:[0,0,1] neg_hi:[0,0,1]\n"
  "v_pk_fma_f32 v[138:139], v[138:139], v[100:101], v[102:103] op_sel_hi:[1,0,0] neg_lo:[0,0,1] neg_hi:[0,0,1]\n"
  "v_pk_fma_f32 v[140:141], v[140:141], v[100:101], v[102:103] op_sel_hi:[1,0,0] neg_lo:[0,0,1] neg_hi:[0,0,1]\n"
  "v_pk_fma_f32 v[142:143], v[142:143], v[100:101], v[102:103] op_sel_hi:[1,0,0] neg_lo:[0,0,1] neg_hi:[0,0,1]\n"
  "v_cvt_pk_bf16_f32 v16, v136, v137\n"
  "v_cvt_pk_bf16_f32 v17, v138, v139\n"
  "v_cvt_pk_bf16_f32 v18, v140, v141\n"
  "v_cvt_pk_bf16_f32 v19, v142, v143\n"
  "ds_write_b128 v87, v[16:19] offset:8192\n"
  "v_pk_fma_f32 v[144:145], v[144:145], v[100:101], v[102:103] op_sel_hi:[1,0,0] neg_lo:[0,0,1] neg_hi:[0,0,1]\n"
  "v_pk_fma_f32 v[146:147], v[146:147], v[100:101], v[102:103] op_sel_hi:[1,0,0] neg_lo:[0,0,1] neg_hi:[0,0,1]\n"
  "v_pk_fma_f32 v[148:149], v[148:149], v[100:101], v[102:103] op_sel_hi:[1,0,0] neg_lo:[0,0,1] neg_hi:[0,0,1]\n"
  "v_pk_fma_f32 v[150:151], v[150:151], v[100:101], v[102:103] op_sel_hi:[1,0,0] neg_lo:[0,0,1] neg_hi:[0,0,1]\n"
  "v_cvt_pk_bf16_f32 v20, v144, v145\n"
  "v_cvt_pk_bf16_f32 v21, v146, v147\n"
  "v_cvt_pk_bf16_f32 v22, v148, v149\n"
  "v_cvt_pk_bf16_f32 v23, v150, v151\n"
  "ds_write_b128 v87, v[20:23] offset:10240\n"
  "v_lshlrev_b32 v152, 16, v24\n"
  "v_and_b32 v153, s60, v24\n"
  "v_lshlrev_b32 v154, 16, v25\n"
  "v_and_b32 v155, s60, v25\n"
  "v_lshlrev_b32 v156, 16, v26\n"
  "v_and_b32 v157, s60, v26\n"
  "v_lshlrev_b32 v158, 16, v27\n"
  "v_and_b32 v159, s60, v27\n"
  "v_pk_fma_f32 v[152:153], v[152:153], v[100:101], v[102:103] op_sel_hi:[1,0,0] neg_lo:[0,0,1] neg_hi:[0,0,1]\n"
  "v_pk_fma_f32 v[154:155], v[154:155], v[100:101], v[102:103] op_sel_hi:[1,0,0] neg_lo:[0,0,1] neg_hi:[0,0,1]\n"
  "v_pk_fma_f32 v[156:157], v[156:157], v[100:101], v[102:103] op_sel_hi:[1,0,0] neg_lo:[0,0,1] neg_hi:[0,0,1]\n"
  "v_pk_fma_f32 v[158:159], v[158:159], v[100:101], v[102:103] op_sel_hi:[1,0,0] neg_lo:[0,0,1] neg_hi:[0,0,1]\n"
  "v_cvt_pk_bf16_f32 v24, v152, v153\n"
  "v_cvt_pk_bf16_f32 v25, v154, v155\n"
  "v_cvt_pk_bf16_f32 v26, v156, v157\n"
  "v_cvt_pk_bf16_f32 v27, v158, v159\n"
  "ds_write_b128 v87, v[24:27] offset:12288\n"
  "buffer_load_dwordx4 v92, %6, s64 offen offset:0 lds\n"
  "buffer_load_dwordx4 v93, %6, s64 offen offset:1088 lds\n"
  "buffer_load_dwordx4 v94, %6, s64 offen offset:2176 lds\n"
  "buffer_load_dwordx4 v95, %6, s64 offen offset:3264 lds\n"
  "s_cmp_eq_u32 %10, 0\n"
  "s_cbranch_scc0 .Lno_router\n"
  "s_waitcnt lgkmcnt(0)\n"
  "v_dot2c_f32_bf16 v98, v0, v28\n"
  "v_dot2c_f32_bf16 v99, v1, v29\n"
  "v_dot2c_f32_bf16 v98, v2, v30\n"
  "v_dot2c_f32_bf16 v99, v3, v31\n"
  "v_dot2c_f32_bf16 v98, v4, v32\n"
  "v_dot2c_f32_bf16 v99, v5, v33\n"
  "v_dot2c_f32_bf16 v98, v6, v34\n"
  "v_dot2c_f32_bf16 v99, v7, v35\n"
  "v_dot2c_f32_bf16 v98, v8, v36\n"
  "v_dot2c_f32_bf16 v99, v9, v37\n"
  "v_dot2c_f32_bf16 v98, v10, v38\n"
  "v_dot2c_f32_bf16 v99, v11, v39\n"
  "v_dot2c_f32_bf16 v98, v12, v40\n"
  "v_dot2c_f32_bf16 v99, v13, v41\n"
  "v_dot2c_f32_bf16 v98, v14, v42\n"
  "v_dot2c_f32_bf16 v99, v15, v43\n"
  "v_dot2c_f32_bf16 v98, v16, v44\n"
  "v_dot2c_f32_bf16 v99, v17, v45\n"
  "v_dot2c_f32_bf16 v98, v18, v46\n"
  "v_dot2c_f32_bf16 v99, v19, v47\n"
  "v_dot2c_f32_bf16 v98, v20, v48\n"
  "v_dot2c_f32_bf16 v99, v21, v49\n"
  "v_dot2c_f32_bf16 v98, v22, v50\n"
  "v_dot2c_f32_bf16 v99, v23, v51\n"
  "v_dot2c_f32_bf16 v98, v24, v52\n"
  "v_dot2c_f32_bf16 v99, v25, v53\n"
  "v_dot2c_f32_bf16 v98, v26, v54\n"
  "v_dot2c_f32_bf16 v99, v27, v55\n"
  "s_nop 2\n"
  "v_add_f32 v98, v98, v99\n"
  "s_nop 1\n"
  "v_add_f32_dpp v98, v98, v98 quad_perm:[1,0,3,2] row_mask:0xf bank_mask:0xf\n"
  "s_nop 1\n"
  "v_add_f32_dpp v98, v98, v98 quad_perm:[2,3,0,1] row_mask:0xf bank_mask:0xf\n"
  "s_nop 1\n"
  "v_add_f32_dpp v98, v98, v98 row_half_mirror row_mask:0xf bank_mask:0xf\n"
  ".Lno_router:\n"
  "ds_write_b128 v86, v[96:99] offset:0\n"
  "s_waitcnt lgkmcnt(0)\n"
    : "={v244}"(la[0]), "={v245}"(la[1]), "={v246}"(la[2]), "={v247}"(la[3]), "={v248}"(la[4])
    : "s"(mk_srd(xb, 64 * 896)), "s"(sw), "s"(mk_srd(wr, pub ? 896 : 0)), "s"(wid), "s"(rot), "s"(kv ^ (rb & 7)), "s"(j), "s"(wid) : "v0","v1","v2","v3","v4","v5","v6","v7","v8","v9","v10","v11","v12","v13","v14","v15","v16","v17","v18","v19","v20","v21","v22","v23","v24","v25","v26","v27","v28","v29","v30","v31","v32","v33","v34","v35","v36","v37","v38","v39","v40","v41","v42","v43","v44","v45","v46","v47","v48","v49","v50","v51","v52","v53","v54","v55","v56","v57","v58","v59","v60","v61","v62","v63","v64","v65","v66","v67","v68","v69","v70","v71","v72","v73","v74","v75","v76","v77","v78","v79","v80","v81","v82","v83","v84","v85","v86","v87","v88","v89","v90","v91","v92","v93","v94","v95","v96","v97","v98","v99","v100","v101","v102","v103","v104","v105","v106","v107","v108","v109","v110","v111","v112","v113","v114","v115","v116","v117","v118","v119","v120","v121","v122","v123","v124","v125","v126","v127","v128","v129","v130","v131","v132","v133","v134","v135","v136","v137","v138","v139","v140","v141","v142","v143","v144","v145","v146","v147","v148","v149","v150","v151","v152","v153","v154","v155","v156","v157","v158","v159","s60","s61","s62","s63","s64","s65","s66","s67","s68","s69","s70","s71","s72","s73","s74","s75","s76","s77","s78","s79","s80","s81","v249","scc","memory");
  if (__builtin_expect(wid < 4, 1)) {
    asm volatile(
  "s_setprio 2\n"
  "s_mov_b32 s42, 0x41000000\n"
  "s_mov_b32 s41, 4294897792\n"
  "s_mov_b32 s40, 6\n"
  "s_waitcnt vmcnt(4)\n"
  "s_barrier\n"
  "ds_read_b128 v[128:131], v248 offset:0\n"
  "ds_read_b128 v[132:135], v248 offset:14336\n"
  "ds_read_b64_tr_b16 v[104:105], v246 offset:0\n"
  "ds_read_b64_tr_b16 v[106:107], v246 offset:512\n"
  "ds_read_b64_tr_b16 v[108:109], v246 offset:8\n"
  "ds_read_b64_tr_b16 v[110:111], v246 offset:520\n"
  "ds_read_b64_tr_b16 v[112:113], v246 offset:64\n"
  "ds_read_b64_tr_b16 v[114:115], v246 offset:576\n"
  "ds_read_b64_tr_b16 v[116:117], v246 offset:72\n"
  "ds_read_b64_tr_b16 v[118:119], v246 offset:584\n"
  "ds_read_b64_tr_b16 v[120:121], v246 offset:128\n"
  "ds_read_b64_tr_b16 v[122:123], v246 offset:640\n"
  "ds_read_b64_tr_b16 v[124:125], v246 offset:136\n"
  "s_waitcnt lgkmcnt(7)\n"
  "v_mfma_f32_16x16x32_bf16 v[0:3], v[104:107], v[128:131], 0\n"
  "ds_read_b64_tr_b16 v[126:127], v246 offset:648\n"
  "ds_read_b128 v[136:139], v248 offset:1024\n"
  "ds_read_b128 v[140:143], v248 offset:15360\n"
  "v_mfma_f32_16x16x32_bf16 v[4:7], v[104:107], v[132:135], 0\n"
  "ds_read_b64_tr_b16 v[104:105], v246 offset:192\n"
  "ds_read_b128 v[144:147], v248 offset:2048\n"
  "ds_read_b128 v[148:151], v248 offset:16384\n"
  "v_mfma_f32_16x16x32_bf16 v[8:11], v[108:111], v[128:131], 0\n"
  "ds_read_b64_tr_b16 v[106:107], v246 offset:704\n"
  "ds_read_b128 v[152:155], v248 offset:3072\n"
  "v_mfma_f32_16x16x32_bf16 v[12:15], v[108:111], v[132:135], 0\n"
  "s_waitcnt lgkmcnt(11)\n"
  "ds_read_b64_tr_b16 v[108:109], v246 offset:200\n"
  "ds_read_b128 v[156:159], v248 offset:17408\n"
  "ds_read_b128 v[160:163], v248 offset:4096\n"
  "v_mfma_f32_16x16x32_bf16 v[16:19], v[112:115], v[128:131], 0\n"
  "ds_read_b64_tr_b16 v[110:111], v246 offset:712\n"
  "s_waitcnt lgkmcnt(11)\n"
  "ds_read_b128 v[164:167], v248 offset:18432\n"
  "ds_read_b128 v[168:171], v248 offset:5120\n"
  "v_mfma_f32_16x16x32_bf16 v[20:23], v[112:115], v[132:135], 0\n"
  "ds_read_b64_tr_b16 v[112:113], v246 offset:256\n"
  "ds_read_b128 v[172:175], v248 offset:19456\n"
  "v_mfma_f32_16x16x32_bf16 v[24:27], v[116:119], v[128:131], 0\n"
  "s_waitcnt lgkmcnt(11)\n"
  "ds_read_b64_tr_b16 v[114:115], v246 offset:768\n"
  "ds_read_b128 v[176:179], v248 offset:6144\n"
  "ds_read_b128 v[180:183], v248 offset:20480\n"
  "v_mfma_f32_16x16x32_bf16 v[28:31], v[116:119], v[132:135], 0\n"
  "ds_read_b64_tr_b16 v[116:117], v246 offset:264\n"
  "s_waitcnt lgkmcnt(11)\n"
  "ds_read_b128 v[184:187], v248 offset:7168\n"
  "v_mfma_f32_16x16x32_bf16 v[32:35], v[120:123], v[128:131], 0\n"
  "ds_read_b64_tr_b16 v[118:119], v246 offset:776\n"
  "ds_read_b128 v[188:191], v248 offset:21504\n"
  "ds_read_b128 v[192:195], v248 offset:8192\n"
  "v_mfma_f32_16x16x32_bf16 v[36:39], v[120:123], v[132:135], 0\n"
  "s_waitcnt lgkmcnt(11)\n"
  "ds_read_b64_tr_b16 v[120:121], v246 offset:320\n"
  "ds_read_b128 v[196:199], v248 offset:22528\n"
  "ds_read_b128 v[200:203], v248 offset:9216\n"
  "v_mfma_f32_16x16x32_bf16 v[40:43], v[124:127], v[128:131], 0\n"
  "ds_read_b64_tr_b16 v[122:123], v246 offset:832\n"
  "s_waitcnt lgkmcnt(11)\n"
  "ds_read_b128 v[204:207], v248 offset:23552\n"
  "v_mfma_f32_16x16x32_bf16 v[44:47], v[124:127], v[132:135], 0\n"
  "ds_read_b64_tr_b16 v[124:125], v246 offset:328\n"
  "ds_read_b128 v[208:211], v248 offset:10240\n"
  "ds_read_b128 v[212:215], v248 offset:24576\n"
  "v_mfma_f32_16x16x32_bf16 v[48:51], v[104:107], v[128:131], 0\n"
  "s_waitcnt lgkmcnt(11)\n"
  "ds_read_b64_tr_b16 v[126:127], v246 offset:840\n"
  "ds_read_b128 v[216:219], v248 offset:11264\n"
  "ds_read_b128 v[220:223], v248 offset:25600\n"
  "v_mfma_f32_16x16x32_bf16 v[52:55], v[104:107], v[132:135], 0\n"
  "ds_read_b64_tr_b16 v[104:105], v246 offset:384\n"
  "s_waitcnt lgkmcnt(11)\n"
  "ds_read_b128 v[224:227], v248 offset:12288\n"
  "v_mfma_f32_16x16x32_bf16 v[56:59], v[108:111], v[128:131], 0\n"
  "ds_read_b64_tr_b16 v[106:107], v246 offset:896\n"
  "ds_read_b128 v[228:231], v248 offset:26624\n"
  "ds_read_b128 v[232:235], v248 offset:13312\n"
  "v_mfma_f32_16x16x32_bf16 v[60:63], v[108:111], v[132:135], 0\n"
  "s_waitcnt lgkmcnt(11)\n"
  "ds_read_b64_tr_b16 v[240:241], v246 offset:392\n"
  "ds_read_b128 v[236:239], v248 offset:27648\n"
  "v_mfma_f32_16x16x32_bf16 v[64:67], v[112:115], v[128:131], 0\n"
  "ds_read_b64_tr_b16 v[242:243], v246 offset:904\n"
  "v_mfma_f32_16x16x32_bf16 v[68:71], v[112:115], v[132:135], 0\n"
  "ds_read_b64_tr_b16 v[252:253], v246 offset:448\n"
  "v_mfma_f32_16x16x32_bf16 v[72:75], v[116:119], v[128:131], 0\n"
  "s_waitcnt lgkmcnt(11)\n"
  "ds_read_b64_tr_b16 v[254:255], v246 offset:960\n"
  "v_mfma_f32_16x16x32_bf16 v[76:79], v[116:119], v[132:135], 0\n"
  "ds_read_b64_tr_b16 v[248:249], v246 offset:456\n"
  "v_mfma_f32_16x16x32_bf16 v[80:83], v[120:123], v[128:131], 0\n"
  "ds_read_b64_tr_b16 v[250:251], v246 offset:968\n"
  "v_mfma_f32_16x16x32_bf16 v[84:87], v[120:123], v[132:135], 0\n"
  "v_mfma_f32_16x16x32_bf16 v[88:91], v[124:127], v[128:131], 0\n"
  "v_mfma_f32_16x16x32_bf16 v[92:95], v[124:127], v[132:135], 0\n"
  "s_waitcnt lgkmcnt(4)\n"
  "v_mfma_f32_16x16x32_bf16 v[96:99], v[104:107], v[128:131], 0\n"
  "v_mfma_f32_16x16x32_bf16 v[100:103], v[104:107], v[132:135], 0\n"
  "v_mfma_f32_16x16x32_bf16 v[104:107], v[240:243], v[128:131], 0\n"
  "v_mfma_f32_16x16x32_bf16 v[108:111], v[240:243], v[132:135], 0\n"
  "s_waitcnt vmcnt(0)\n"
  "s_barrier\n"
  "ds_read_b64_tr_b16 v[240:241], v246 offset:34752\n"
  "s_waitcnt lgkmcnt(3)\n"
  "v_mfma_f32_16x16x32_bf16 v[112:115], v[252:255], v[128:131], 0\n"
  "ds_read_b64_tr_b16 v[242:243], v246 offset:35264\n"
  "v_mfma_f32_16x16x32_bf16 v[116:119], v[252:255], v[132:135], 0\n"
  "ds_read_b64_tr_b16 v[252:253], v246 offset:34760\n"
  "s_waitcnt lgkmcnt(3)\n"
  "v_mfma_f32_16x16x32_bf16 v[120:123], v[248:251], v[128:131], 0\n"
  "ds_read_b64_tr_b16 v[254:255], v246 offset:35272\n"
  "v_mfma_f32_16x16x32_bf16 v[124:127], v[248:251], v[132:135], 0\n"
  "ds_read_b64_tr_b16 v[248:249], v246 offset:34816\n"
  "s_waitcnt lgkmcnt(3)\n"
  "v_mfma_f32_16x16x32_bf16 v[0:3], v[240:243], v[136:139], v[0:3]\n"
  "ds_read_b64_tr_b16 v[250:251], v246 offset:35328\n"
  "v_mfma_f32_16x16x32_bf16 v[4:7], v[240:243], v[140:143], v[4:7]\n"
  "ds_read_b64_tr_b16 v[240:241], v246 offset:34824\n"
  "ds_read_b64_tr_b16 v[242:243], v246 offset:35336\n"
  "s_waitcnt lgkmcnt(4)\n"
  "v_mfma_f32_16x16x32_bf16 v[8:11], v[252:255], v[136:139], v[8:11]\n"
  "ds_read_b64_tr_b16 v[128:129], v246 offset:34880\n"
  "ds_read_b64_tr_b16 v[130:131], v246 offset:35392\n"
  "v_mfma_f32_16x16x32_bf16 v[12:15], v[252:255], v[140:143], v[12:15]\n"
  "ds_read_b64_tr_b16 v[132:133], v246 offset:34888\n"
  "ds_read_b64_tr_b16 v[134:135], v246 offset:35400\n"
  "s_waitcnt lgkmcnt(6)\n"
  "v_mfma_f32_16x16x32_bf16 v[16:19], v[248:251], v[136:139], v[16:19]\n"
  "ds_read_b64_tr_b16 v[252:253], v246 offset:34944\n"
  "v_mfma_f32_16x16x32_bf16 v[20:23], v[248:251], v[140:143], v[20:23]\n"
  "ds_read_b64_tr_b16 v[254:255], v246 offset:35456\n"
  "s_waitcnt lgkmcnt(6)\n"
  "v_mfma_f32_16x16x32_bf16 v[24:27], v[240:243], v[136:139], v[24:27]\n"
  "ds_read_b64_tr_b16 v[248:249], v246 offset:34952\n"
  "v_mfma_f32_16x16x32_bf16 v[28:31], v[240:243], v[140:143], v[28:31]\n"
  "ds_read_b64_tr_b16 v[250:251], v246 offset:35464\n"
  "s_waitcnt lgkmcnt(4)\n"
  "v_mfma_f32_16x16x32_bf16 v[32:35], v[128:131], v[136:139], v[32:35]\n"
  "ds_read_b64_tr_b16 v[240:241], v246 offset:35008\n"
  "v_mfma_f32_16x16x32_bf16 v[36:39], v[128:131], v[140:143], v[36:39]\n"
  "ds_read_b64_tr_b16 v[242:243], v246 offset:35520\n"
  "v_mfma_f32_16x16x32_bf16 v[40:43], v[132:135], v[136:139], v[40:43]\n"
  "ds_read_b64_tr_b16 v[128:129], v246 offset:35016\n"
  "v_mfma_f32_16x16x32_bf16 v[44:47], v[132:135], v[140:143], v[44:47]\n"
  "ds_read_b64_tr_b16 v[130:131], v246 offset:35528\n"
  "s_waitcnt lgkmcnt(4)\n"
  "v_mfma_f32_16x16x32_bf16 v[48:51], v[252:255], v[136:139], v[48:51]\n"
  "ds_read_b64_tr_b16 v[132:133], v246 offset:35072\n"
  "v_mfma_f32_16x16x32_bf16 v[52:55], v[252:255], v[140:143], v[52:55]\n"
  "ds_read_b64_tr_b16 v[134:135], v246 offset:35584\n"
  "v_mfma_f32_16x16x32_bf16 v[56:59], v[248:251], v[136:139], v[56:59]\n"
  "ds_read_b64_tr_b16 v[252:253], v246 offset:35080\n"
  "v_mfma_f32_16x16x32_bf16 v[60:63], v[248:251], v[140:143], v[60:63]\n"
  "ds_read_b64_tr_b16 v[254:255], v246 offset:35592\n"
  "s_waitcnt lgkmcnt(4)\n"
  "v_mfma_f32_16x16x32_bf16 v[64:67], v[240:243], v[136:139], v[64:67]\n"
  "ds_read_b64_tr_b16 v[248:249], v246 offset:35136\n"
  "v_mfma_f32_16x16x32_bf16 v[68:71], v[240:243], v[140:143], v[68:71]\n"
  "ds_read_b64_tr_b16 v[250:251], v246 offset:35648\n"
  "v_mfma_f32_16x16x32_bf16 v[72:75], v[128:131], v[136:139], v[72:75]\n"
  "ds_read_b64_tr_b16 v[240:241], v246 offset:35144\n"
  "v_mfma_f32_16x16x32_bf16 v[76:79], v[128:131], v[140:143], v[76:79]\n"
  "ds_read_b64_tr_b16 v[242:243], v246 offset:35656\n"
  "s_waitcnt lgkmcnt(4)\n"
  "v_mfma_f32_16x16x32_bf16 v[80:83], v[132:135], v[136:139], v[80:83]\n"
  "ds_read_b64_tr_b16 v[128:129], v246 offset:35200\n"
  "v_mfma_f32_16x16x32_bf16 v[84:87], v[132:135], v[140:143], v[84:87]\n"
  "ds_read_b64_tr_b16 v[130:131], v246 offset:35712\n"
  "v_mfma_f32_16x16x32_bf16 v[88:91], v[252:255], v[136:139], v[88:91]\n"
  "ds_read_b64_tr_b16 v[132:133], v246 offset:35208\n"
  "v_mfma_f32_16x16x32_bf16 v[92:95], v[252:255], v[140:143], v[92:95]\n"
  "ds_read_b64_tr_b16 v[134:135], v246 offset:35720\n"
  "s_waitcnt lgkmcnt(4)\n"
  "v_mfma_f32_16x16x32_bf16 v[96:99], v[248:251], v[136:139], v[96:99]\n"
  "s_barrier\n"
  "ds_read_b64_tr_b16 v[252:253], v247 offset:0\n"
  "v_mfma_f32_16x16x32_bf16 v[100:103], v[248:251], v[140:143], v[100:103]\n"
  "ds_read_b64_tr_b16 v[254:255], v247 offset:512\n"
  "v_mfma_f32_16x16x32_bf16 v[104:107], v[240:243], v[136:139], v[104:107]\n"
  "ds_read_b64_tr_b16 v[248:249], v247 offset:8\n"
  "v_mfma_f32_16x16x32_bf16 v[108:111], v[240:243], v[140:143], v[108:111]\n"
  "ds_read_b64_tr_b16 v[250:251], v247 offset:520\n"
  "s_waitcnt lgkmcnt(4)\n"
  "v_mfma_f32_16x16x32_bf16 v[112:115], v[128:131], v[136:139], v[112:115]\n"
  "ds_read_b64_tr_b16 v[240:241], v247 offset:64\n"
  "v_mfma_f32_16x16x32_bf16 v[116:119], v[128:131], v[140:143], v[116:119]\n"
  "ds_read_b64_tr_b16 v[242:243], v247 offset:576\n"
  "v_mfma_f32_16x16x32_bf16 v[120:123], v[132:135], v[136:139], v[120:123]\n"
  "ds_read_b64_tr_b16 v[128:129], v247 offset:72\n"
  "v_mfma_f32_16x16x32_bf16 v[124:127], v[132:135], v[140:143], v[124:127]\n"
  "ds_read_b64_tr_b16 v[130:131], v247 offset:584\n"
  "ds_read_b64_tr_b16 v[136:137], v247 offset:128\n"
  "s_waitcnt lgkmcnt(5)\n"
  "v_mfma_f32_16x16x32_bf16 v[0:3], v[252:255], v[144:147], v[0:3]\n"
  "ds_read_b64_tr_b16 v[138:139], v247 offset:640\n"
  "v_mfma_f32_16x16x32_bf16 v[4:7], v[252:255], v[148:151], v[4:7]\n"
  "ds_read_b64_tr_b16 v[140:141], v247 offset:136\n"
  "ds_read_b64_tr_b16 v[142:143], v247 offset:648\n"
  "v_mfma_f32_16x16x32_bf16 v[8:11], v[248:251], v[144:147], v[8:11]\n"
  "ds_read_b64_tr_b16 v[132:133], v247 offset:192\n"
  "v_mfma_f32_16x16x32_bf16 v[12:15], v[248:251], v[148:151], v[12:15]\n"
  "ds_read_b64_tr_b16 v[134:135], v247 offset:704\n"
  "s_waitcnt lgkmcnt(6)\n"
  "v_mfma_f32_16x16x32_bf16 v[16:19], v[240:243], v[144:147], v[16:19]\n"
  "ds_read_b64_tr_b16 v[252:253], v247 offset:200\n"
  "v_mfma_f32_16x16x32_bf16 v[20:23], v[240:243], v[148:151], v[20:23]\n"
  "ds_read_b64_tr_b16 v[254:255], v247 offset:712\n"
  "v_mfma_f32_16x16x32_bf16 v[24:27], v[128:131], v[144:147], v[24:27]\n"
  "ds_read_b64_tr_b16 v[248:249], v247 offset:256\n"
  "v_mfma_f32_16x16x32_bf16 v[28:31], v[128:131], v[148:151], v[28:31]\n"
  "ds_read_b64_tr_b16 v[250:251], v247 offset:768\n"
  "s_waitcnt lgkmcnt(6)\n"
  "v_mfma_f32_16x16x32_bf16 v[32:35], v[136:139], v[144:147], v[32:35]\n"
  "ds_read_b64_tr_b16 v[240:241], v247 offset:264\n"
  "v_mfma_f32_16x16x32_bf16 v[36:39], v[136:139], v[148:151], v[36:39]\n"
  "ds_read_b64_tr_b16 v[242:243], v247 offset:776\n"
  "v_mfma_f32_16x16x32_bf16 v[40:43], v[140:143], v[144:147], v[40:43]\n"
  "ds_read_b64_tr_b16 v[128:129], v247 offset:320\n"
  "v_mfma_f32_16x16x32_bf16 v[44:47], v[140:143], v[148:151], v[44:47]\n"
  "ds_read_b64_tr_b16 v[130:131], v247 offset:832\n"
  "s_waitcnt lgkmcnt(6)\n"
  "v_mfma_f32_16x16x32_bf16 v[48:51], v[132:135], v[144:147], v[48:51]\n"
  "ds_read_b64_tr_b16 v[136:137], v247 offset:328\n"
  "v_mfma_f32_16x16x32_bf16 v[52:55], v[132:135], v[148:151], v[52:55]\n"
  "ds_read_b64_tr_b16 v[138:139], v247 offset:840\n"
  "v_mfma_f32_16x16x32_bf16 v[56:59], v[252:255], v[144:147], v[56:59]\n"
  "ds_read_b64_tr_b16 v[140:141], v247 offset:384\n"
  "v_mfma_f32_16x16x32_bf16 v[60:63], v[252:255], v[148:151], v[60:63]\n"
  "ds_read_b64_tr_b16 v[142:143], v247 offset:896\n"
  "s_waitcnt lgkmcnt(6)\n"
  "v_mfma_f32_16x16x32_bf16 v[64:67], v[248:251], v[144:147], v[64:67]\n"
  "ds_read_b64_tr_b16 v[132:133], v247 offset:392\n"
  "v_mfma_f32_16x16x32_bf16 v[68:71], v[248:251], v[148:151], v[68:71]\n"
  "ds_read_b64_tr_b16 v[134:135], v247 offset:904\n"
  "v_mfma_f32_16x16x32_bf16 v[72:75], v[240:243], v[144:147], v[72:75]\n"
  "ds_read_b64_tr_b16 v[252:253], v247 offset:448\n"
  "v_mfma_f32_16x16x32_bf16 v[76:79], v[240:243], v[148:151], v[76:79]\n"
  "ds_read_b64_tr_b16 v[254:255], v247 offset:960\n"
  "s_waitcnt lgkmcnt(6)\n"
  "v_mfma_f32_16x16x32_bf16 v[80:83], v[128:131], v[144:147], v[80:83]\n"
  "ds_read_b64_tr_b16 v[248:249], v247 offset:456\n"
  "v_mfma_f32_16x16x32_bf16 v[84:87], v[128:131], v[148:151], v[84:87]\n"
  "ds_read_b64_tr_b16 v[250:251], v247 offset:968\n"
  "v_mfma_f32_16x16x32_bf16 v[88:91], v[136:139], v[144:147], v[88:91]\n"
  "s_barrier\n"
  "ds_read_b64_tr_b16 v[240:241], v247 offset:34752\n"
  "v_mfma_f32_16x16x32_bf16 v[92:95], v[136:139], v[148:151], v[92:95]\n"
  "ds_read_b64_tr_b16 v[242:243], v247 offset:35264\n"
  "s_waitcnt lgkmcnt(6)\n"
  "v_mfma_f32_16x16x32_bf16 v[96:99], v[140:143], v[144:147], v[96:99]\n"
  "ds_read_b64_tr_b16 v[128:129], v247 offset:34760\n"
  "v_mfma_f32_16x16x32_bf16 v[100:103], v[140:143], v[148:151], v[100:103]\n"
  "ds_read_b64_tr_b16 v[130:131], v247 offset:35272\n"
  "v_mfma_f32_16x16x32_bf16 v[104:107], v[132:135], v[144:147], v[104:107]\n"
  "ds_read_b64_tr_b16 v[136:137], v247 offset:34816\n"
  "v_mfma_f32_16x16x32_bf16 v[108:111], v[132:135], v[148:151], v[108:111]\n"
  "ds_read_b64_tr_b16 v[138:139], v247 offset:35328\n"
  "s_waitcnt lgkmcnt(6)\n"
  "v_mfma_f32_16x16x32_bf16 v[112:115], v[252:255], v[144:147], v[112:115]\n"
  "ds_read_b64_tr_b16 v[140:141], v247 offset:34824\n"
  "v_mfma_f32_16x16x32_bf16 v[116:119], v[252:255], v[148:151], v[116:119]\n"
  "ds_read_b64_tr_b16 v[142:143], v247 offset:35336\n"
  "v_mfma_f32_16x16x32_bf16 v[120:123], v[248:251], v[144:147], v[120:123]\n"
  "v_mfma_f32_16x16x32_bf16 v[124:127], v[248:251], v[148:151], v[124:127]\n"
  "ds_read_b64_tr_b16 v[144:145], v247 offset:34880\n"
  "ds_read_b64_tr_b16 v[146:147], v247 offset:35392\n"
  "s_waitcnt lgkmcnt(6)\n"
  "v_mfma_f32_16x16x32_bf16 v[0:3], v[240:243], v[152:155], v[0:3]\n"
  "ds_read_b64_tr_b16 v[132:133], v247 offset:34888\n"
  "v_mfma_f32_16x16x32_bf16 v[4:7], v[240:243], v[156:159], v[4:7]\n"
  "ds_read_b64_tr_b16 v[134:135], v247 offset:35400\n"
  "v_mfma_f32_16x16x32_bf16 v[8:11], v[128:131], v[152:155], v[8:11]\n"
  "ds_read_b64_tr_b16 v[252:253], v247 offset:34944\n"
  "v_mfma_f32_16x16x32_bf16 v[12:15], v[128:131], v[156:159], v[12:15]\n"
  "ds_read_b64_tr_b16 v[254:255], v247 offset:35456\n"
  "s_waitcnt lgkmcnt(6)\n"
  "v_mfma_f32_16x16x32_bf16 v[16:19], v[136:139], v[152:155], v[16:19]\n"
  "ds_read_b64_tr_b16 v[248:249], v247 offset:34952\n"
  "v_mfma_f32_16x16x32_bf16 v[20:23], v[136:139], v[156:159], v[20:23]\n"
  "ds_read_b64_tr_b16 v[250:251], v247 offset:35464\n"
  "v_mfma_f32_16x16x32_bf16 v[24:27], v[140:143], v[152:155], v[24:27]\n"
  "ds_read_b64_tr_b16 v[240:241], v247 offset:35008\n"
  "v_mfma_f32_16x16x32_bf16 v[28:31], v[140:143], v[156:159], v[28:31]\n"
  "ds_read_b64_tr_b16 v[242:243], v247 offset:35520\n"
  "s_waitcnt lgkmcnt(6)\n"
  "v_mfma_f32_16x16x32_bf16 v[32:35], v[144:147], v[152:155], v[32:35]\n"
  "ds_read_b64_tr_b16 v[128:129], v247 offset:35016\n"
  "v_mfma_f32_16x16x32_bf16 v[36:39], v[144:147], v[156:159], v[36:39]\n"
  "ds_read_b64_tr_b16 v[130:131], v247 offset:35528\n"
  "v_mfma_f32_16x16x32_bf16 v[40:43], v[132:135], v[152:155], v[40:43]\n"
  "ds_read_b64_tr_b16 v[136:137], v247 offset:35072\n"
  "v_mfma_f32_16x16x32_bf16 v[44:47], v[132:135], v[156:159], v[44:47]\n"
  "ds_read_b64_tr_b16 v[138:139], v247 offset:35584\n"
  "s_waitcnt lgkmcnt(6)\n"
  "v_mfma_f32_16x16x32_bf16 v[48:51], v[252:255], v[152:155], v[48:51]\n"
  "ds_read_b64_tr_b16 v[140:141], v247 offset:35080\n"
  "v_mfma_f32_16x16x32_bf16 v[52:55], v[252:255], v[156:159], v[52:55]\n"
  "ds_read_b64_tr_b16 v[142:143], v247 offset:35592\n"
  "v_mfma_f32_16x16x32_bf16 v[56:59], v[248:251], v[152:155], v[56:59]\n"
  "ds_read_b64_tr_b16 v[144:145], v247 offset:35136\n"
  "v_mfma_f32_16x16x32_bf16 v[60:63], v[248:251], v[156:159], v[60:63]\n"
  "ds_read_b64_tr_b16 v[146:147], v247 offset:35648\n"
  "s_waitcnt lgkmcnt(6)\n"
  "v_mfma_f32_16x16x32_bf16 v[64:67], v[240:243], v[152:155], v[64:67]\n"
  "ds_read_b64_tr_b16 v[132:133], v247 offset:35144\n"
  "v_mfma_f32_16x16x32_bf16 v[68:71], v[240:243], v[156:159], v[68:71]\n"
  "ds_read_b64_tr_b16 v[134:135], v247 offset:35656\n"
  "v_mfma_f32_16x16x32_bf16 v[72:75], v[128:131], v[152:155], v[72:75]\n"
  "ds_read_b64_tr_b16 v[252:253], v247 offset:35200\n"
  "v_mfma_f32_16x16x32_bf16 v[76:79], v[128:131], v[156:159], v[76:79]\n"
  "ds_read_b64_tr_b16 v[254:255], v247 offset:35712\n"
  "s_waitcnt lgkmcnt(6)\n"
  "v_mfma_f32_16x16x32_bf16 v[80:83], v[136:139], v[152:155], v[80:83]\n"
  "ds_read_b64_tr_b16 v[248:249], v247 offset:35208\n"
  "v_mfma_f32_16x16x32_bf16 v[84:87], v[136:139], v[156:159], v[84:87]\n"
  "ds_read_b64_tr_b16 v[250:251], v247 offset:35720\n"
  "v_mfma_f32_16x16x32_bf16 v[88:91], v[140:143], v[152:155], v[88:91]\n"
  "s_barrier\n"
  "ds_read_b64_tr_b16 v[240:241], v246 offset:0\n"
  "v_mfma_f32_16x16x32_bf16 v[92:95], v[140:143], v[156:159], v[92:95]\n"
  "ds_read_b64_tr_b16 v[242:243], v246 offset:512\n"
  "s_waitcnt lgkmcnt(6)\n"
  "v_mfma_f32_16x16x32_bf16 v[96:99], v[144:147], v[152:155], v[96:99]\n"
  "ds_read_b64_tr_b16 v[128:129], v246 offset:8\n"
  "v_mfma_f32_16x16x32_bf16 v[100:103], v[144:147], v[156:159], v[100:103]\n"
  "ds_read_b64_tr_b16 v[130:131], v246 offset:520\n"
  "v_mfma_f32_16x16x32_bf16 v[104:107], v[132:135], v[152:155], v[104:107]\n"
  "ds_read_b64_tr_b16 v[136:137], v246 offset:64\n"
  "v_mfma_f32_16x16x32_bf16 v[108:111], v[132:135], v[156:159], v[108:111]\n"
  "ds_read_b64_tr_b16 v[138:139], v246 offset:576\n"
  "s_waitcnt lgkmcnt(6)\n"
  "v_mfma_f32_16x16x32_bf16 v[112:115], v[252:255], v[152:155], v[112:115]\n"
  "ds_read_b64_tr_b16 v[140:141], v246 offset:72\n"
  "v_mfma_f32_16x16x32_bf16 v[116:119], v[252:255], v[156:159], v[116:119]\n"
  "ds_read_b64_tr_b16 v[142:143], v246 offset:584\n"
  "v_mfma_f32_16x16x32_bf16 v[120:123], v[248:251], v[152:155], v[120:123]\n"
  "ds_read_b64_tr_b16 v[144:145], v246 offset:128\n"
  "v_mfma_f32_16x16x32_bf16 v[124:127], v[248:251], v[156:159], v[124:127]\n"
  "ds_read_b64_tr_b16 v[146:147], v246 offset:640\n"
  "s_waitcnt lgkmcnt(6)\n"
  "v_mfma_f32_16x16x32_bf16 v[0:3], v[240:243], v[160:163], v[0:3]\n"
  "ds_read_b64_tr_b16 v[132:133], v246 offset:136\n"
  "v_mfma_f32_16x16x32_bf16 v[4:7], v[240:243], v[164:167], v[4:7]\n"
  "ds_read_b64_tr_b16 v[134:135], v246 offset:648\n"
  "v_mfma_f32_16x16x32_bf16 v[8:11], v[128:131], v[160:163], v[8:11]\n"
  "ds_read_b64_tr_b16 v[252:253], v246 offset:192\n"
  "v_mfma_f32_16x16x32_bf16 v[12:15], v[128:131], v[164:167], v[12:15]\n"
  "ds_read_b64_tr_b16 v[254:255], v246 offset:704\n"
  "s_waitcnt lgkmcnt(6)\n"
  "v_mfma_f32_16x16x32_bf16 v[16:19], v[136:139], v[160:163], v[16:19]\n"
  "ds_read_b64_tr_b16 v[248:249], v246 offset:200\n"
  "v_mfma_f32_16x16x32_bf16 v[20:23], v[136:139], v[164:167], v[20:23]\n"
  "ds_read_b64_tr_b16 v[250:251], v246 offset:712\n"
  "v_mfma_f32_16x16x32_bf16 v[24:27], v[140:143], v[160:163], v[24:27]\n"
  "ds_read_b64_tr_b16 v[240:241], v246 offset:256\n"
  "v_mfma_f32_16x16x32_bf16 v[28:31], v[140:143], v[164:167], v[28:31]\n"
  "ds_read_b64_tr_b16 v[242:243], v246 offset:768\n"
  "s_waitcnt lgkmcnt(6)\n"
  "v_mfma_f32_16x16x32_bf16 v[32:35], v[144:147], v[160:163], v[32:35]\n"
  "ds_read_b64_tr_b16 v[128:129], v246 offset:264\n"
  "v_mfma_f32_16x16x32_bf16 v[36:39], v[144:147], v[164:167], v[36:39]\n"
  "ds_read_b64_tr_b16 v[130:131], v246 offset:776\n"
  "v_mfma_f32_16x16x32_bf16 v[40:43], v[132:135], v[160:163], v[40:43]\n"
  "ds_read_b64_tr_b16 v[136:137], v246 offset:320\n"
  "v_mfma_f32_16x16x32_bf16 v[44:47], v[132:135], v[164:167], v[44:47]\n"
  "ds_read_b64_tr_b16 v[138:139], v246 offset:832\n"
  "s_waitcnt lgkmcnt(6)\n"
  "v_mfma_f32_16x16x32_bf16 v[48:51], v[252:255], v[160:163], v[48:51]\n"
  "ds_read_b64_tr_b16 v[140:141], v246 offset:328\n"
  "v_mfma_f32_16x16x32_bf16 v[52:55], v[252:255], v[164:167], v[52:55]\n"
  "ds_read_b64_tr_b16 v[142:143], v246 offset:840\n"
  "v_mfma_f32_16x16x32_bf16 v[56:59], v[248:251], v[160:163], v[56:59]\n"
  "ds_read_b64_tr_b16 v[144:145], v246 offset:384\n"
  "v_mfma_f32_16x16x32_bf16 v[60:63], v[248:251], v[164:167], v[60:63]\n"
  "ds_read_b64_tr_b16 v[146:147], v246 offset:896\n"
  "s_waitcnt lgkmcnt(6)\n"
  "v_mfma_f32_16x16x32_bf16 v[64:67], v[240:243], v[160:163], v[64:67]\n"
  "ds_read_b64_tr_b16 v[132:133], v246 offset:392\n"
  "v_mfma_f32_16x16x32_bf16 v[68:71], v[240:243], v[164:167], v[68:71]\n"
  "ds_read_b64_tr_b16 v[134:135], v246 offset:904\n"
  "v_mfma_f32_16x16x32_bf16 v[72:75], v[128:131], v[160:163], v[72:75]\n"
  "ds_read_b64_tr_b16 v[252:253], v246 offset:448\n"
  "v_mfma_f32_16x16x32_bf16 v[76:79], v[128:131], v[164:167], v[76:79]\n"
  "ds_read_b64_tr_b16 v[254:255], v246 offset:960\n"
  "s_waitcnt lgkmcnt(6)\n"
  "v_mfma_f32_16x16x32_bf16 v[80:83], v[136:139], v[160:163], v[80:83]\n"
  "ds_read_b64_tr_b16 v[248:249], v246 offset:456\n"
  "v_mfma_f32_16x16x32_bf16 v[84:87], v[136:139], v[164:167], v[84:87]\n"
  "ds_read_b64_tr_b16 v[250:251], v246 offset:968\n"
  "v_mfma_f32_16x16x32_bf16 v[88:91], v[140:143], v[160:163], v[88:91]\n"
  "s_barrier\n"
  "ds_read_b64_tr_b16 v[240:241], v246 offset:34752\n"
  "v_mfma_f32_16x16x32_bf16 v[92:95], v[140:143], v[164:167], v[92:95]\n"
  "ds_read_b64_tr_b16 v[242:243], v246 offset:35264\n"
  "s_waitcnt lgkmcnt(6)\n"
  "v_mfma_f32_16x16x32_bf16 v[96:99], v[144:147], v[160:163], v[96:99]\n"
  "ds_read_b64_tr_b16 v[128:129], v246 offset:34760\n"
  "v_mfma_f32_16x16x32_bf16 v[100:103], v[144:147], v[164:167], v[100:103]\n"
  "ds_read_b64_tr_b16 v[130:131], v246 offset:35272\n"
  "v_mfma_f32_16x16x32_bf16 v[104:107], v[132:135], v[160:163], v[104:107]\n"
  "ds_read_b64_tr_b16 v[136:137], v246 offset:34816\n"
  "v_mfma_f32_16x16x32_bf16 v[108:111], v[132:135], v[164:167], v[108:111]\n"
  "ds_read_b64_tr_b16 v[138:139], v246 offset:35328\n"
  "s_waitcnt lgkmcnt(6)\n"
  "v_mfma_f32_16x16x32_bf16 v[112:115], v[252:255], v[160:163], v[112:115]\n"
  "ds_read_b64_tr_b16 v[140:141], v246 offset:34824\n"
  "v_mfma_f32_16x16x32_bf16 v[116:119], v[252:255], v[164:167], v[116:119]\n"
  "ds_read_b64_tr_b16 v[142:143], v246 offset:35336\n"
  "v_mfma_f32_16x16x32_bf16 v[120:123], v[248:251], v[160:163], v[120:123]\n"
  "ds_read_b64_tr_b16 v[144:145], v246 offset:34880\n"
  "v_mfma_f32_16x16x32_bf16 v[124:127], v[248:251], v[164:167], v[124:127]\n"
  "ds_read_b64_tr_b16 v[146:147], v246 offset:35392\n"
  "s_waitcnt lgkmcnt(6)\n"
  "v_mfma_f32_16x16x32_bf16 v[0:3], v[240:243], v[168:171], v[0:3]\n"
  "ds_read_b64_tr_b16 v[132:133], v246 offset:34888\n"
  "v_mfma_f32_16x16x32_bf16 v[4:7], v[240:243], v[172:175], v[4:7]\n"
  "ds_read_b64_tr_b16 v[134:135], v246 offset:35400\n"
  "v_mfma_f32_16x16x32_bf16 v[8:11], v[128:131], v[168:171], v[8:11]\n"
  "ds_read_b64_tr_b16 v[252:253], v246 offset:34944\n"
  "v_mfma_f32_16x16x32_bf16 v[12:15], v[128:131], v[172:175], v[12:15]\n"
  "ds_read_b64_tr_b16 v[254:255], v246 offset:35456\n"
  "s_waitcnt lgkmcnt(6)\n"
  "v_mfma_f32_16x16x32_bf16 v[16:19], v[136:139], v[168:171], v[16:19]\n"
  "ds_read_b64_tr_b16 v[248:249], v246 offset:34952\n"
  "v_mfma_f32_16x16x32_bf16 v[20:23], v[136:139], v[172:175], v[20:23]\n"
  "ds_read_b64_tr_b16 v[250:251], v246 offset:35464\n"
  "v_mfma_f32_16x16x32_bf16 v[24:27], v[140:143], v[168:171], v[24:27]\n"
  "ds_read_b64_tr_b16 v[240:241], v246 offset:35008\n"
  "v_mfma_f32_16x16x32_bf16 v[28:31], v[140:143], v[172:175], v[28:31]\n"
  "ds_read_b64_tr_b16 v[242:243], v246 offset:35520\n"
  "s_waitcnt lgkmcnt(6)\n"
  "v_mfma_f32_16x16x32_bf16 v[32:35], v[144:147], v[168:171], v[32:35]\n"
  "ds_read_b64_tr_b16 v[128:129], v246 offset:35016\n"
  "v_mfma_f32_16x16x32_bf16 v[36:39], v[144:147], v[172:175], v[36:39]\n"
  "ds_read_b64_tr_b16 v[130:131], v246 offset:35528\n"
  "v_mfma_f32_16x16x32_bf16 v[40:43], v[132:135], v[168:171], v[40:43]\n"
  "ds_read_b64_tr_b16 v[136:137], v246 offset:35072\n"
  "v_mfma_f32_16x16x32_bf16 v[44:47], v[132:135], v[172:175], v[44:47]\n"
  "ds_read_b64_tr_b16 v[138:139], v246 offset:35584\n"
  "s_waitcnt lgkmcnt(6)\n"
  "v_mfma_f32_16x16x32_bf16 v[48:51], v[252:255], v[168:171], v[48:51]\n"
  "ds_read_b64_tr_b16 v[140:141], v246 offset:35080\n"
  "v_mfma_f32_16x16x32_bf16 v[52:55], v[252:255], v[172:175], v[52:55]\n"
  "ds_read_b64_tr_b16 v[142:143], v246 offset:35592\n"
  "v_mfma_f32_16x16x32_bf16 v[56:59], v[248:251], v[168:171], v[56:59]\n"
  "ds_read_b64_tr_b16 v[144:145], v246 offset:35136\n"
  "v_mfma_f32_16x16x32_bf16 v[60:63], v[248:251], v[172:175], v[60:63]\n"
  "ds_read_b64_tr_b16 v[146:147], v246 offset:35648\n"
  "s_waitcnt lgkmcnt(6)\n"
  "v_mfma_f32_16x16x32_bf16 v[64:67], v[240:243], v[168:171], v[64:67]\n"
  "ds_read_b64_tr_b16 v[132:133], v246 offset:35144\n"
  "v_mfma_f32_16x16x32_bf16 v[68:71], v[240:243], v[172:175], v[68:71]\n"
  "ds_read_b64_tr_b16 v[134:135], v246 offset:35656\n"
  "v_mfma_f32_16x16x32_bf16 v[72:75], v[128:131], v[168:171], v[72:75]\n"
  "ds_read_b64_tr_b16 v[252:253], v246 offset:35200\n"
  "v_mfma_f32_16x16x32_bf16 v[76:79], v[128:131], v[172:175], v[76:79]\n"
  "ds_read_b64_tr_b16 v[254:255], v246 offset:35712\n"
  "s_waitcnt lgkmcnt(6)\n"
  "v_mfma_f32_16x16x32_bf16 v[80:83], v[136:139], v[168:171], v[80:83]\n"
  "ds_read_b64_tr_b16 v[248:249], v246 offset:35208\n"
  "v_mfma_f32_16x16x32_bf16 v[84:87], v[136:139], v[172:175], v[84:87]\n"
  "ds_read_b64_tr_b16 v[250:251], v246 offset:35720\n"
  "v_mfma_f32_16x16x32_bf16 v[88:91], v[140:143], v[168:171], v[88:91]\n"
  "s_barrier\n"
  "ds_read_b64_tr_b16 v[240:241], v247 offset:0\n"
  "v_mfma_f32_16x16x32_bf16 v[92:95], v[140:143], v[172:175], v[92:95]\n"
  "ds_read_b64_tr_b16 v[242:243], v247 offset:512\n"
  "s_waitcnt lgkmcnt(6)\n"
  "v_mfma_f32_16x16x32_bf16 v[96:99], v[144:147], v[168:171], v[96:99]\n"
  "ds_read_b64_tr_b16 v[128:129], v247 offset:8\n"
  "v_mfma_f32_16x16x32_bf16 v[100:103], v[144:147], v[172:175], v[100:103]\n"
  "ds_read_b64_tr_b16 v[130:131], v247 offset:520\n"
  "v_mfma_f32_16x16x32_bf16 v[104:107], v[132:135], v[168:171], v[104:107]\n"
  "ds_read_b64_tr_b16 v[136:137], v247 offset:64\n"
  "v_mfma_f32_16x16x32_bf16 v[108:111], v[132:135], v[172:175], v[108:111]\n"
  "ds_read_b64_tr_b16 v[138:139], v247 offset:576\n"
  "s_waitcnt lgkmcnt(6)\n"
  "v_mfma_f32_16x16x32_bf16 v[112:115], v[252:255], v[168:171], v[112:115]\n"
  "ds_read_b64_tr_b16 v[140:141], v247 offset:72\n"
  "v_mfma_f32_16x16x32_bf16 v[116:119], v[252:255], v[172:175], v[116:119]\n"
  "ds_read_b64_tr_b16 v[142:143], v247 offset:584\n"
  "v_mfma_f32_16x16x32_bf16 v[120:123], v[248:251], v[168:171], v[120:123]\n"
  "ds_read_b64_tr_b16 v[144:145], v247 offset:128\n"
  "v_mfma_f32_16x16x32_bf16 v[124:127], v[248:251], v[172:175], v[124:127]\n"
  "ds_read_b64_tr_b16 v[146:147], v247 offset:640\n"
  "s_waitcnt lgkmcnt(6)\n"
  "v_mfma_f32_16x16x32_bf16 v[0:3], v[240:243], v[176:179], v[0:3]\n"
  "ds_read_b64_tr_b16 v[132:133], v247 offset:136\n"
  "v_mfma_f32_16x16x32_bf16 v[4:7], v[240:243], v[180:183], v[4:7]\n"
  "ds_read_b64_tr_b16 v[134:135], v247 offset:648\n"
  "v_mfma_f32_16x16x32_bf16 v[8:11], v[128:131], v[176:179], v[8:11]\n"
  "ds_read_b64_tr_b16 v[252:253], v247 offset:192\n"
  "v_mfma_f32_16x16x32_bf16 v[12:15], v[128:131], v[180:183], v[12:15]\n"
  "ds_read_b64_tr_b16 v[254:255], v247 offset:704\n"
  "s_waitcnt lgkmcnt(6)\n"
  "v_mfma_f32_16x16x32_bf16 v[16:19], v[136:139], v[176:179], v[16:19]\n"
  "ds_read_b64_tr_b16 v[248:249], v247 offset:200\n"
  "v_mfma_f32_16x16x32_bf16 v[20:23], v[136:139], v[180:183], v[20:23]\n"
  "ds_read_b64_tr_b16 v[250:251], v247 offset:712\n"
  "v_mfma_f32_16x16x32_bf16 v[24:27], v[140:143], v[176:179], v[24:27]\n"
  "ds_read_b64_tr_b16 v[240:241], v247 offset:256\n"
  "v_mfma_f32_16x16x32_bf16 v[28:31], v[140:143], v[180:183], v[28:31]\n"
  "ds_read_b64_tr_b16 v[242:243], v247 offset:768\n"
  "s_waitcnt lgkmcnt(6)\n"
  "v_mfma_f32_16x16x32_bf16 v[32:35], v[144:147], v[176:179], v[32:35]\n"
  "ds_read_b64_tr_b16 v[128:129], v247 offset:264\n"
  "v_mfma_f32_16x16x32_bf16 v[36:39], v[144:147], v[180:183], v[36:39]\n"
  "ds_read_b64_tr_b16 v[130:131], v247 offset:776\n"
  "v_mfma_f32_16x16x32_bf16 v[40:43], v[132:135], v[176:179], v[40:43]\n"
  "ds_read_b64_tr_b16 v[136:137], v247 offset:320\n"
  "v_mfma_f32_16x16x32_bf16 v[44:47], v[132:135], v[180:183], v[44:47]\n"
  "ds_read_b64_tr_b16 v[138:139], v247 offset:832\n"
  "s_waitcnt lgkmcnt(6)\n"
  "v_mfma_f32_16x16x32_bf16 v[48:51], v[252:255], v[176:179], v[48:51]\n"
  "ds_read_b64_tr_b16 v[140:141], v247 offset:328\n"
  "v_mfma_f32_16x16x32_bf16 v[52:55], v[252:255], v[180:183], v[52:55]\n"
  "ds_read_b64_tr_b16 v[142:143], v247 offset:840\n"
  "v_mfma_f32_16x16x32_bf16 v[56:59], v[248:251], v[176:179], v[56:59]\n"
  "ds_read_b64_tr_b16 v[144:145], v247 offset:384\n"
  "v_mfma_f32_16x16x32_bf16 v[60:63], v[248:251], v[180:183], v[60:63]\n"
  "ds_read_b64_tr_b16 v[146:147], v247 offset:896\n"
  "s_waitcnt lgkmcnt(6)\n"
  "v_mfma_f32_16x16x32_bf16 v[64:67], v[240:243], v[176:179], v[64:67]\n"
  "ds_read_b64_tr_b16 v[132:133], v247 offset:392\n"
  "v_mfma_f32_16x16x32_bf16 v[68:71], v[240:243], v[180:183], v[68:71]\n"
  "ds_read_b64_tr_b16 v[134:135], v247 offset:904\n"
  "v_mfma_f32_16x16x32_bf16 v[72:75], v[128:131], v[176:179], v[72:75]\n"
  "ds_read_b64_tr_b16 v[252:253], v247 offset:448\n"
  "v_mfma_f32_16x16x32_bf16 v[76:79], v[128:131], v[180:183], v[76:79]\n"
  "ds_read_b64_tr_b16 v[254:255], v247 offset:960\n"
  "s_waitcnt lgkmcnt(6)\n"
  "v_mfma_f32_16x16x32_bf16 v[80:83], v[136:139], v[176:179], v[80:83]\n"
  "ds_read_b64_tr_b16 v[248:249], v247 offset:456\n"
  "v_mfma_f32_16x16x32_bf16 v[84:87], v[136:139], v[180:183], v[84:87]\n"
  "ds_read_b64_tr_b16 v[250:251], v247 offset:968\n"
  "v_mfma_f32_16x16x32_bf16 v[88:91], v[140:143], v[176:179], v[88:91]\n"
  "s_barrier\n"
  "ds_read_b64_tr_b16 v[240:241], v247 offset:34752\n"
  "v_mfma_f32_16x16x32_bf16 v[92:95], v[140:143], v[180:183], v[92:95]\n"
  "ds_read_b64_tr_b16 v[242:243], v247 offset:35264\n"
  "s_waitcnt lgkmcnt(6)\n"
  "v_mfma_f32_16x16x32_bf16 v[96:99], v[144:147], v[176:179], v[96:99]\n"
  "ds_read_b64_tr_b16 v[128:129], v247 offset:34760\n"
  "v_mfma_f32_16x16x32_bf16 v[100:103], v[144:147], v[180:183], v[100:103]\n"
  "ds_read_b64_tr_b16 v[130:131], v247 offset:35272\n"
  "v_mfma_f32_16x16x32_bf16 v[104:107], v[132:135], v[176:179], v[104:107]\n"
  "ds_read_b64_tr_b16 v[136:137], v247 offset:34816\n"
  "v_mfma_f32_16x16x32_bf16 v[108:111], v[132:135], v[180:183], v[108:111]\n"
  "ds_read_b64_tr_b16 v[138:139], v247 offset:35328\n"
  "s_waitcnt lgkmcnt(6)\n"
  "v_mfma_f32_16x16x32_bf16 v[112:115], v[252:255], v[176:179], v[112:115]\n"
  "ds_read_b64_tr_b16 v[140:141], v247 offset:34824\n"
  "v_mfma_f32_16x16x32_bf16 v[116:119], v[252:255], v[180:183], v[116:119]\n"
  "ds_read_b64_tr_b16 v[142:143], v247 offset:35336\n"
  "v_mfma_f32_16x16x32_bf16 v[120:123], v[248:251], v[176:179], v[120:123]\n"
  "ds_read_b64_tr_b16 v[144:145], v247 offset:34880\n"
  "v_mfma_f32_16x16x32_bf16 v[124:127], v[248:251], v[180:183], v[124:127]\n"
  "ds_read_b64_tr_b16 v[146:147], v247 offset:35392\n"
  "s_waitcnt lgkmcnt(6)\n"
  "v_mfma_f32_16x16x32_bf16 v[0:3], v[240:243], v[184:187], v[0:3]\n"
  "ds_read_b64_tr_b16 v[132:133], v247 offset:34888\n"
  "v_mfma_f32_16x16x32_bf16 v[4:7], v[240:243], v[188:191], v[4:7]\n"
  "ds_read_b64_tr_b16 v[134:135], v247 offset:35400\n"
  "v_mfma_f32_16x16x32_bf16 v[8:11], v[128:131], v[184:187], v[8:11]\n"
  "ds_read_b64_tr_b16 v[252:253], v247 offset:34944\n"
  "v_mfma_f32_16x16x32_bf16 v[12:15], v[128:131], v[188:191], v[12:15]\n"
  "ds_read_b64_tr_b16 v[254:255], v247 offset:35456\n"
  "s_waitcnt lgkmcnt(6)\n"
  "v_mfma_f32_16x16x32_bf16 v[16:19], v[136:139], v[184:187], v[16:19]\n"
  "ds_read_b64_tr_b16 v[248:249], v247 offset:34952\n"
  "v_mfma_f32_16x16x32_bf16 v[20:23], v[136:139], v[188:191], v[20:23]\n"
  "ds_read_b64_tr_b16 v[250:251], v247 offset:35464\n"
  "v_mfma_f32_16x16x32_bf16 v[24:27], v[140:143], v[184:187], v[24:27]\n"
  "ds_read_b64_tr_b16 v[240:241], v247 offset:35008\n"
  "v_mfma_f32_16x16x32_bf16 v[28:31], v[140:143], v[188:191], v[28:31]\n"
  "ds_read_b64_tr_b16 v[242:243], v247 offset:35520\n"
  "s_waitcnt lgkmcnt(6)\n"
  "v_mfma_f32_16x16x32_bf16 v[32:35], v[144:147], v[184:187], v[32:35]\n"
  "ds_read_b64_tr_b16 v[128:129], v247 offset:35016\n"
  "v_mfma_f32_16x16x32_bf16 v[36:39], v[144:147], v[188:191], v[36:39]\n"
  "ds_read_b64_tr_b16 v[130:131], v247 offset:35528\n"
  "v_mfma_f32_16x16x32_bf16 v[40:43], v[132:135], v[184:187], v[40:43]\n"
  "ds_read_b64_tr_b16 v[136:137], v247 offset:35072\n"
  "v_mfma_f32_16x16x32_bf16 v[44:47], v[132:135], v[188:191], v[44:47]\n"
  "ds_read_b64_tr_b16 v[138:139], v247 offset:35584\n"
  "s_waitcnt lgkmcnt(6)\n"
  "v_mfma_f32_16x16x32_bf16 v[48:51], v[252:255], v[184:187], v[48:51]\n"
  "ds_read_b64_tr_b16 v[140:141], v247 offset:35080\n"
  "v_mfma_f32_16x16x32_bf16 v[52:55], v[252:255], v[188:191], v[52:55]\n"
  "ds_read_b64_tr_b16 v[142:143], v247 offset:35592\n"
  "v_mfma_f32_16x16x32_bf16 v[56:59], v[248:251], v[184:187], v[56:59]\n"
  "ds_read_b64_tr_b16 v[144:145], v247 offset:35136\n"
  "v_mfma_f32_16x16x32_bf16 v[60:63], v[248:251], v[188:191], v[60:63]\n"
  "ds_read_b64_tr_b16 v[146:147], v247 offset:35648\n"
  "s_waitcnt lgkmcnt(6)\n"
  "v_mfma_f32_16x16x32_bf16 v[64:67], v[240:243], v[184:187], v[64:67]\n"
  "ds_read_b64_tr_b16 v[132:133], v247 offset:35144\n"
  "v_mfma_f32_16x16x32_bf16 v[68:71], v[240:243], v[188:191], v[68:71]\n"
  "ds_read_b64_tr_b16 v[134:135], v247 offset:35656\n"
  "v_mfma_f32_16x16x32_bf16 v[72:75], v[128:131], v[184:187], v[72:75]\n"
  "ds_read_b64_tr_b16 v[252:253], v247 offset:35200\n"
  "v_mfma_f32_16x16x32_bf16 v[76:79], v[128:131], v[188:191], v[76:79]\n"
  "ds_read_b64_tr_b16 v[254:255], v247 offset:35712\n"
  "s_waitcnt lgkmcnt(6)\n"
  "v_mfma_f32_16x16x32_bf16 v[80:83], v[136:139], v[184:187], v[80:83]\n"
  "ds_read_b64_tr_b16 v[248:249], v247 offset:35208\n"
  "v_mfma_f32_16x16x32_bf16 v[84:87], v[136:139], v[188:191], v[84:87]\n"
  "ds_read_b64_tr_b16 v[250:251], v247 offset:35720\n"
  "v_mfma_f32_16x16x32_bf16 v[88:91], v[140:143], v[184:187], v[88:91]\n"
  "s_barrier\n"
  "ds_read_b64_tr_b16 v[240:241], v246 offset:0\n"
  "v_mfma_f32_16x16x32_bf16 v[92:95], v[140:143], v[188:191], v[92:95]\n"
  "ds_read_b64_tr_b16 v[242:243], v246 offset:512\n"
  "s_waitcnt lgkmcnt(6)\n"
  "v_mfma_f32_16x16x32_bf16 v[96:99], v[144:147], v[184:187], v[96:99]\n"
  "ds_read_b64_tr_b16 v[128:129], v246 offset:8\n"
  "v_mfma_f32_16x16x32_bf16 v[100:103], v[144:147], v[188:191], v[100:103]\n"
  "ds_read_b64_tr_b16 v[130:131], v246 offset:520\n"
  "v_mfma_f32_16x16x32_bf16 v[104:107], v[132:135], v[184:187], v[104:107]\n"
  "ds_read_b64_tr_b16 v[136:137], v246 offset:64\n"
  "v_mfma_f32_16x16x32_bf16 v[108:111], v[132:135], v[188:191], v[108:111]\n"
  "ds_read_b64_tr_b16 v[138:139], v246 offset:576\n"
  "s_waitcnt lgkmcnt(6)\n"
  "v_mfma_f32_16x16x32_bf16 v[112:115], v[252:255], v[184:187], v[112:115]\n"
  "ds_read_b64_tr_b16 v[140:141], v246 offset:72\n"
  "v_mfma_f32_16x16x32_bf16 v[116:119], v[252:255], v[188:191], v[116:119]\n"
  "ds_read_b64_tr_b16 v[142:143], v246 offset:584\n"
  "v_mfma_f32_16x16x32_bf16 v[120:123], v[248:251], v[184:187], v[120:123]\n"
  "ds_read_b64_tr_b16 v[144:145], v246 offset:128\n"
  "v_mfma_f32_16x16x32_bf16 v[124:127], v[248:251], v[188:191], v[124:127]\n"
  "ds_read_b64_tr_b16 v[146:147], v246 offset:640\n"
  "s_waitcnt lgkmcnt(6)\n"
  "v_mfma_f32_16x16x32_bf16 v[0:3], v[240:243], v[192:195], v[0:3]\n"
  "ds_read_b64_tr_b16 v[132:133], v246 offset:136\n"
  "v_mfma_f32_16x16x32_bf16 v[4:7], v[240:243], v[196:199], v[4:7]\n"
  "ds_read_b64_tr_b16 v[134:135], v246 offset:648\n"
  "v_mfma_f32_16x16x32_bf16 v[8:11], v[128:131], v[192:195], v[8:11]\n"
  "ds_read_b64_tr_b16 v[252:253], v246 offset:192\n"
  "v_mfma_f32_16x16x32_bf16 v[12:15], v[128:131], v[196:199], v[12:15]\n"
  "ds_read_b64_tr_b16 v[254:255], v246 offset:704\n"
  "s_waitcnt lgkmcnt(6)\n"
  "v_mfma_f32_16x16x32_bf16 v[16:19], v[136:139], v[192:195], v[16:19]\n"
  "ds_read_b64_tr_b16 v[248:249], v246 offset:200\n"
  "v_mfma_f32_16x16x32_bf16 v[20:23], v[136:139], v[196:199], v[20:23]\n"
  "ds_read_b64_tr_b16 v[250:251], v246 offset:712\n"
  "v_mfma_f32_16x16x32_bf16 v[24:27], v[140:143], v[192:195], v[24:27]\n"
  "ds_read_b64_tr_b16 v[240:241], v246 offset:256\n"
  "v_mfma_f32_16x16x32_bf16 v[28:31], v[140:143], v[196:199], v[28:31]\n"
  "ds_read_b64_tr_b16 v[242:243], v246 offset:768\n"
  "s_waitcnt lgkmcnt(6)\n"
  "v_mfma_f32_16x16x32_bf16 v[32:35], v[144:147], v[192:195], v[32:35]\n"
  "ds_read_b64_tr_b16 v[128:129], v246 offset:264\n"
  "v_mfma_f32_16x16x32_bf16 v[36:39], v[144:147], v[196:199], v[36:39]\n"
  "ds_read_b64_tr_b16 v[130:131], v246 offset:776\n"
  "v_mfma_f32_16x16x32_bf16 v[40:43], v[132:135], v[192:195], v[40:43]\n"
  "ds_read_b64_tr_b16 v[136:137], v246 offset:320\n"
  "v_mfma_f32_16x16x32_bf16 v[44:47], v[132:135], v[196:199], v[44:47]\n"
  "ds_read_b64_tr_b16 v[138:139], v246 offset:832\n"
  "s_waitcnt lgkmcnt(6)\n"
  "v_mfma_f32_16x16x32_bf16 v[48:51], v[252:255], v[192:195], v[48:51]\n"
  "ds_read_b64_tr_b16 v[140:141], v246 offset:328\n"
  "v_mfma_f32_16x16x32_bf16 v[52:55], v[252:255], v[196:199], v[52:55]\n"
  "ds_read_b64_tr_b16 v[142:143], v246 offset:840\n"
  "v_mfma_f32_16x16x32_bf16 v[56:59], v[248:251], v[192:195], v[56:59]\n"
  "ds_read_b64_tr_b16 v[144:145], v246 offset:384\n"
  "v_mfma_f32_16x16x32_bf16 v[60:63], v[248:251], v[196:199], v[60:63]\n"
  "ds_read_b64_tr_b16 v[146:147], v246 offset:896\n"
  "s_waitcnt lgkmcnt(6)\n"
  "v_mfma_f32_16x16x32_bf16 v[64:67], v[240:243], v[192:195], v[64:67]\n"
  "ds_read_b64_tr_b16 v[132:133], v246 offset:392\n"
  "v_mfma_f32_16x16x32_bf16 v[68:71], v[240:243], v[196:199], v[68:71]\n"
  "ds_read_b64_tr_b16 v[134:135], v246 offset:904\n"
  "v_mfma_f32_16x16x32_bf16 v[72:75], v[128:131], v[192:195], v[72:75]\n"
  "ds_read_b64_tr_b16 v[252:253], v246 offset:448\n"
  "v_mfma_f32_16x16x32_bf16 v[76:79], v[128:131], v[196:199], v[76:79]\n"
  "ds_read_b64_tr_b16 v[254:255], v246 offset:960\n"
  "s_waitcnt lgkmcnt(6)\n"
  "v_mfma_f32_16x16x32_bf16 v[80:83], v[136:139], v[192:195], v[80:83]\n"
  "ds_read_b64_tr_b16 v[248:249], v246 offset:456\n"
  "v_mfma_f32_16x16x32_bf16 v[84:87], v[136:139], v[196:199], v[84:87]\n"
  "ds_read_b64_tr_b16 v[250:251], v246 offset:968\n"
  "v_mfma_f32_16x16x32_bf16 v[88:91], v[140:143], v[192:195], v[88:91]\n"
  "s_barrier\n"
  "ds_read_b64_tr_b16 v[240:241], v246 offset:34752\n"
  "v_mfma_f32_16x16x32_bf16 v[92:95], v[140:143], v[196:199], v[92:95]\n"
  "ds_read_b64_tr_b16 v[242:243], v246 offset:35264\n"
  "s_waitcnt lgkmcnt(6)\n"
  "v_mfma_f32_16x16x32_bf16 v[96:99], v[144:147], v[192:195], v[96:99]\n"
  "ds_read_b64_tr_b16 v[128:129], v246 offset:34760\n"
  "v_mfma_f32_16x16x32_bf16 v[100:103], v[144:147], v[196:199], v[100:103]\n"
  "ds_read_b64_tr_b16 v[130:131], v246 offset:35272\n"
  "v_mfma_f32_16x16x32_bf16 v[104:107], v[132:135], v[192:195], v[104:107]\n"
  "ds_read_b64_tr_b16 v[136:137], v246 offset:34816\n"
  "v_mfma_f32_16x16x32_bf16 v[108:111], v[132:135], v[196:199], v[108:111]\n"
  "ds_read_b64_tr_b16 v[138:139], v246 offset:35328\n"
  "s_waitcnt lgkmcnt(6)\n"
  "v_mfma_f32_16x16x32_bf16 v[112:115], v[252:255], v[192:195], v[112:115]\n"
  "ds_read_b64_tr_b16 v[140:141], v246 offset:34824\n"
  "v_mfma_f32_16x16x32_bf16 v[116:119], v[252:255], v[196:199], v[116:119]\n"
  "ds_read_b64_tr_b16 v[142:143], v246 offset:35336\n"
  "v_mfma_f32_16x16x32_bf16 v[120:123], v[248:251], v[192:195], v[120:123]\n"
  "ds_read_b64_tr_b16 v[144:145], v246 offset:34880\n"
  "v_mfma_f32_16x16x32_bf16 v[124:127], v[248:251], v[196:199], v[124:127]\n"
  "ds_read_b64_tr_b16 v[146:147], v246 offset:35392\n"
  "s_waitcnt lgkmcnt(6)\n"
  "v_mfma_f32_16x16x32_bf16 v[0:3], v[240:243], v[200:203], v[0:3]\n"
  "ds_read_b64_tr_b16 v[132:133], v246 offset:34888\n"
  "v_mfma_f32_16x16x32_bf16 v[4:7], v[240:243], v[204:207], v[4:7]\n"
  "ds_read_b64_tr_b16 v[134:135], v246 offset:35400\n"
  "v_mfma_f32_16x16x32_bf16 v[8:11], v[128:131], v[200:203], v[8:11]\n"
  "ds_read_b64_tr_b16 v[252:253], v246 offset:34944\n"
  "v_mfma_f32_16x16x32_bf16 v[12:15], v[128:131], v[204:207], v[12:15]\n"
  "ds_read_b64_tr_b16 v[254:255], v246 offset:35456\n"
  "s_waitcnt lgkmcnt(6)\n"
  "v_mfma_f32_16x16x32_bf16 v[16:19], v[136:139], v[200:203], v[16:19]\n"
  "ds_read_b64_tr_b16 v[248:249], v246 offset:34952\n"
  "v_mfma_f32_16x16x32_bf16 v[20:23], v[136:139], v[204:207], v[20:23]\n"
  "ds_read_b64_tr_b16 v[250:251], v246 offset:35464\n"
  "v_mfma_f32_16x16x32_bf16 v[24:27], v[140:143], v[200:203], v[24:27]\n"
  "ds_read_b64_tr_b16 v[240:241], v246 offset:35008\n"
  "v_mfma_f32_16x16x32_bf16 v[28:31], v[140:143], v[204:207], v[28:31]\n"
  "ds_read_b64_tr_b16 v[242:243], v246 offset:35520\n"
  "s_waitcnt lgkmcnt(6)\n"
  "v_mfma_f32_16x16x32_bf16 v[32:35], v[144:147], v[200:203], v[32:35]\n"
  "ds_read_b64_tr_b16 v[128:129], v246 offset:35016\n"
  "v_mfma_f32_16x16x32_bf16 v[36:39], v[144:147], v[204:207], v[36:39]\n"
  "ds_read_b64_tr_b16 v[130:131], v246 offset:35528\n"
  "v_mfma_f32_16x16x32_bf16 v[40:43], v[132:135], v[200:203], v[40:43]\n"
  "ds_read_b64_tr_b16 v[136:137], v246 offset:35072\n"
  "v_mfma_f32_16x16x32_bf16 v[44:47], v[132:135], v[204:207], v[44:47]\n"
  "ds_read_b64_tr_b16 v[138:139], v246 offset:35584\n"
  "s_waitcnt lgkmcnt(6)\n"
  "v_mfma_f32_16x16x32_bf16 v[48:51], v[252:255], v[200:203], v[48:51]\n"
  "ds_read_b64_tr_b16 v[140:141], v246 offset:35080\n"
  "v_mfma_f32_16x16x32_bf16 v[52:55], v[252:255], v[204:207], v[52:55]\n"
  "ds_read_b64_tr_b16 v[142:143], v246 offset:35592\n"
  "v_mfma_f32_16x16x32_bf16 v[56:59], v[248:251], v[200:203], v[56:59]\n"
  "ds_read_b64_tr_b16 v[144:145], v246 offset:35136\n"
  "v_mfma_f32_16x16x32_bf16 v[60:63], v[248:251], v[204:207], v[60:63]\n"
  "ds_read_b64_tr_b16 v[146:147], v246 offset:35648\n"
  "s_waitcnt lgkmcnt(6)\n"
  "v_mfma_f32_16x16x32_bf16 v[64:67], v[240:243], v[200:203], v[64:67]\n"
  "ds_read_b64_tr_b16 v[132:133], v246 offset:35144\n"
  "v_mfma_f32_16x16x32_bf16 v[68:71], v[240:243], v[204:207], v[68:71]\n"
  "ds_read_b64_tr_b16 v[134:135], v246 offset:35656\n"
  "v_mfma_f32_16x16x32_bf16 v[72:75], v[128:131], v[200:203], v[72:75]\n"
  "ds_read_b64_tr_b16 v[252:253], v246 offset:35200\n"
  "v_mfma_f32_16x16x32_bf16 v[76:79], v[128:131], v[204:207], v[76:79]\n"
  "ds_read_b64_tr_b16 v[254:255], v246 offset:35712\n"
  "s_waitcnt lgkmcnt(6)\n"
  "v_mfma_f32_16x16x32_bf16 v[80:83], v[136:139], v[200:203], v[80:83]\n"
  "ds_read_b64_tr_b16 v[248:249], v246 offset:35208\n"
  "v_mfma_f32_16x16x32_bf16 v[84:87], v[136:139], v[204:207], v[84:87]\n"
  "ds_read_b64_tr_b16 v[250:251], v246 offset:35720\n"
  "v_mfma_f32_16x16x32_bf16 v[88:91], v[140:143], v[200:203], v[88:91]\n"
  "s_barrier\n"
  "ds_read_b64_tr_b16 v[240:241], v247 offset:0\n"
  "v_mfma_f32_16x16x32_bf16 v[92:95], v[140:143], v[204:207], v[92:95]\n"
  "ds_read_b64_tr_b16 v[242:243], v247 offset:512\n"
  "s_waitcnt lgkmcnt(6)\n"
  "v_mfma_f32_16x16x32_bf16 v[96:99], v[144:147], v[200:203], v[96:99]\n"
  "ds_read_b64_tr_b16 v[128:129], v247 offset:8\n"
  "v_mfma_f32_16x16x32_bf16 v[100:103], v[144:147], v[204:207], v[100:103]\n"
  "ds_read_b64_tr_b16 v[130:131], v247 offset:520\n"
  "v_mfma_f32_16x16x32_bf16 v[104:107], v[132:135], v[200:203], v[104:107]\n"
  "ds_read_b64_tr_b16 v[136:137], v247 offset:64\n"
  "v_mfma_f32_16x16x32_bf16 v[108:111], v[132:135], v[204:207], v[108:111]\n"
  "ds_read_b64_tr_b16 v[138:139], v247 offset:576\n"
  "s_waitcnt lgkmcnt(6)\n"
  "v_mfma_f32_16x16x32_bf16 v[112:115], v[252:255], v[200:203], v[112:115]\n"
  "ds_read_b64_tr_b16 v[140:141], v247 offset:72\n"
  "v_mfma_f32_16x16x32_bf16 v[116:119], v[252:255], v[204:207], v[116:119]\n"
  "ds_read_b64_tr_b16 v[142:143], v247 offset:584\n"
  "v_mfma_f32_16x16x32_bf16 v[120:123], v[248:251], v[200:203], v[120:123]\n"
  "ds_read_b64_tr_b16 v[144:145], v247 offset:128\n"
  "v_mfma_f32_16x16x32_bf16 v[124:127], v[248:251], v[204:207], v[124:127]\n"
  "ds_read_b64_tr_b16 v[146:147], v247 offset:640\n"
  "s_waitcnt lgkmcnt(6)\n"
  "v_mfma_f32_16x16x32_bf16 v[0:3], v[240:243], v[208:211], v[0:3]\n"
  "ds_read_b64_tr_b16 v[132:133], v247 offset:136\n"
  "v_mfma_f32_16x16x32_bf16 v[4:7], v[240:243], v[212:215], v[4:7]\n"
  "ds_read_b64_tr_b16 v[134:135], v247 offset:648\n"
  "v_mfma_f32_16x16x32_bf16 v[8:11], v[128:131], v[208:211], v[8:11]\n"
  "ds_read_b64_tr_b16 v[252:253], v247 offset:192\n"
  "v_mfma_f32_16x16x32_bf16 v[12:15], v[128:131], v[212:215], v[12:15]\n"
  "ds_read_b64_tr_b16 v[254:255], v247 offset:704\n"
  "s_waitcnt lgkmcnt(6)\n"
  "v_mfma_f32_16x16x32_bf16 v[16:19], v[136:139], v[208:211], v[16:19]\n"
  "ds_read_b64_tr_b16 v[248:249], v247 offset:200\n"
  "v_mfma_f32_16x16x32_bf16 v[20:23], v[136:139], v[212:215], v[20:23]\n"
  "ds_read_b64_tr_b16 v[250:251], v247 offset:712\n"
  "v_mfma_f32_16x16x32_bf16 v[24:27], v[140:143], v[208:211], v[24:27]\n"
  "ds_read_b64_tr_b16 v[240:241], v247 offset:256\n"
  "v_mfma_f32_16x16x32_bf16 v[28:31], v[140:143], v[212:215], v[28:31]\n"
  "ds_read_b64_tr_b16 v[242:243], v247 offset:768\n"
  "s_waitcnt lgkmcnt(6)\n"
  "v_mfma_f32_16x16x32_bf16 v[32:35], v[144:147], v[208:211], v[32:35]\n"
  "ds_read_b64_tr_b16 v[128:129], v247 offset:264\n"
  "v_mfma_f32_16x16x32_bf16 v[36:39], v[144:147], v[212:215], v[36:39]\n"
  "ds_read_b64_tr_b16 v[130:131], v247 offset:776\n"
  "v_mfma_f32_16x16x32_bf16 v[40:43], v[132:135], v[208:211], v[40:43]\n"
  "ds_read_b64_tr_b16 v[136:137], v247 offset:320\n"
  "v_mfma_f32_16x16x32_bf16 v[44:47], v[132:135], v[212:215], v[44:47]\n"
  "ds_read_b64_tr_b16 v[138:139], v247 offset:832\n"
  "s_waitcnt lgkmcnt(6)\n"
  "v_mfma_f32_16x16x32_bf16 v[48:51], v[252:255], v[208:211], v[48:51]\n"
  "ds_read_b64_tr_b16 v[140:141], v247 offset:328\n"
  "v_mfma_f32_16x16x32_bf16 v[52:55], v[252:255], v[212:215], v[52:55]\n"
  "ds_read_b64_tr_b16 v[142:143], v247 offset:840\n"
  "v_mfma_f32_16x16x32_bf16 v[56:59], v[248:251], v[208:211], v[56:59]\n"
  "ds_read_b64_tr_b16 v[144:145], v247 offset:384\n"
  "v_mfma_f32_16x16x32_bf16 v[60:63], v[248:251], v[212:215], v[60:63]\n"
  "ds_read_b64_tr_b16 v[146:147], v247 offset:896\n"
  "s_waitcnt lgkmcnt(6)\n"
  "v_mfma_f32_16x16x32_bf16 v[64:67], v[240:243], v[208:211], v[64:67]\n"
  "ds_read_b64_tr_b16 v[132:133], v247 offset:392\n"
  "v_mfma_f32_16x16x32_bf16 v[68:71], v[240:243], v[212:215], v[68:71]\n"
  "ds_read_b64_tr_b16 v[134:135], v247 offset:904\n"
  "v_mfma_f32_16x16x32_bf16 v[72:75], v[128:131], v[208:211], v[72:75]\n"
  "ds_read_b64_tr_b16 v[252:253], v247 offset:448\n"
  "v_mfma_f32_16x16x32_bf16 v[76:79], v[128:131], v[212:215], v[76:79]\n"
  "ds_read_b64_tr_b16 v[254:255], v247 offset:960\n"
  "s_waitcnt lgkmcnt(6)\n"
  "v_mfma_f32_16x16x32_bf16 v[80:83], v[136:139], v[208:211], v[80:83]\n"
  "ds_read_b64_tr_b16 v[248:249], v247 offset:456\n"
  "v_mfma_f32_16x16x32_bf16 v[84:87], v[136:139], v[212:215], v[84:87]\n"
  "ds_read_b64_tr_b16 v[250:251], v247 offset:968\n"
  "v_mfma_f32_16x16x32_bf16 v[88:91], v[140:143], v[208:211], v[88:91]\n"
  "s_barrier\n"
  "ds_read_b64_tr_b16 v[240:241], v247 offset:34752\n"
  "v_mfma_f32_16x16x32_bf16 v[92:95], v[140:143], v[212:215], v[92:95]\n"
  "ds_read_b64_tr_b16 v[242:243], v247 offset:35264\n"
  "s_waitcnt lgkmcnt(6)\n"
  "v_mfma_f32_16x16x32_bf16 v[96:99], v[144:147], v[208:211], v[96:99]\n"
  "ds_read_b64_tr_b16 v[128:129], v247 offset:34760\n"
  "v_mfma_f32_16x16x32_bf16 v[100:103], v[144:147], v[212:215], v[100:103]\n"
  "ds_read_b64_tr_b16 v[130:131], v247 offset:35272\n"
  "v_mfma_f32_16x16x32_bf16 v[104:107], v[132:135], v[208:211], v[104:107]\n"
  "ds_read_b64_tr_b16 v[136:137], v247 offset:34816\n"
  "v_mfma_f32_16x16x32_bf16 v[108:111], v[132:135], v[212:215], v[108:111]\n"
  "ds_read_b64_tr_b16 v[138:139], v247 offset:35328\n"
  "s_waitcnt lgkmcnt(6)\n"
  "v_mfma_f32_16x16x32_bf16 v[112:115], v[252:255], v[208:211], v[112:115]\n"
  "ds_read_b64_tr_b16 v[140:141], v247 offset:34824\n"
  "v_mfma_f32_16x16x32_bf16 v[116:119], v[252:255], v[212:215], v[116:119]\n"
  "ds_read_b64_tr_b16 v[142:143], v247 offset:35336\n"
  "v_mfma_f32_16x16x32_bf16 v[120:123], v[248:251], v[208:211], v[120:123]\n"
  "ds_read_b64_tr_b16 v[144:145], v247 offset:34880\n"
  "v_mfma_f32_16x16x32_bf16 v[124:127], v[248:251], v[212:215], v[124:127]\n"
  "ds_read_b64_tr_b16 v[146:147], v247 offset:35392\n"
  "s_waitcnt lgkmcnt(6)\n"
  "v_mfma_f32_16x16x32_bf16 v[0:3], v[240:243], v[216:219], v[0:3]\n"
  "ds_read_b64_tr_b16 v[132:133], v247 offset:34888\n"
  "v_mfma_f32_16x16x32_bf16 v[4:7], v[240:243], v[220:223], v[4:7]\n"
  "ds_read_b64_tr_b16 v[134:135], v247 offset:35400\n"
  "v_mfma_f32_16x16x32_bf16 v[8:11], v[128:131], v[216:219], v[8:11]\n"
  "ds_read_b64_tr_b16 v[252:253], v247 offset:34944\n"
  "v_mfma_f32_16x16x32_bf16 v[12:15], v[128:131], v[220:223], v[12:15]\n"
  "ds_read_b64_tr_b16 v[254:255], v247 offset:35456\n"
  "s_waitcnt lgkmcnt(6)\n"
  "v_mfma_f32_16x16x32_bf16 v[16:19], v[136:139], v[216:219], v[16:19]\n"
  "ds_read_b64_tr_b16 v[248:249], v247 offset:34952\n"
  "v_mfma_f32_16x16x32_bf16 v[20:23], v[136:139], v[220:223], v[20:23]\n"
  "ds_read_b64_tr_b16 v[250:251], v247 offset:35464\n"
  "v_mfma_f32_16x16x32_bf16 v[24:27], v[140:143], v[216:219], v[24:27]\n"
  "ds_read_b64_tr_b16 v[240:241], v247 offset:35008\n"
  "v_mfma_f32_16x16x32_bf16 v[28:31], v[140:143], v[220:223], v[28:31]\n"
  "ds_read_b64_tr_b16 v[242:243], v247 offset:35520\n"
  "s_waitcnt lgkmcnt(6)\n"
  "v_mfma_f32_16x16x32_bf16 v[32:35], v[144:147], v[216:219], v[32:35]\n"
  "ds_read_b64_tr_b16 v[128:129], v247 offset:35016\n"
  "v_mfma_f32_16x16x32_bf16 v[36:39], v[144:147], v[220:223], v[36:39]\n"
  "ds_read_b64_tr_b16 v[130:131], v247 offset:35528\n"
  "v_mfma_f32_16x16x32_bf16 v[40:43], v[132:135], v[216:219], v[40:43]\n"
  "ds_read_b64_tr_b16 v[136:137], v247 offset:35072\n"
  "v_mfma_f32_16x16x32_bf16 v[44:47], v[132:135], v[220:223], v[44:47]\n"
  "ds_read_b64_tr_b16 v[138:139], v247 offset:35584\n"
  "s_waitcnt lgkmcnt(6)\n"
  "v_mfma_f32_16x16x32_bf16 v[48:51], v[252:255], v[216:219], v[48:51]\n"
  "ds_read_b64_tr_b16 v[140:141], v247 offset:35080\n"
  "v_mfma_f32_16x16x32_bf16 v[52:55], v[252:255], v[220:223], v[52:55]\n"
  "ds_read_b64_tr_b16 v[142:143], v247 offset:35592\n"
  "v_mfma_f32_16x16x32_bf16 v[56:59], v[248:251], v[216:219], v[56:59]\n"
  "ds_read_b64_tr_b16 v[144:145], v247 offset:35136\n"
  "v_mfma_f32_16x16x32_bf16 v[60:63], v[248:251], v[220:223], v[60:63]\n"
  "ds_read_b64_tr_b16 v[146:147], v247 offset:35648\n"
  "s_waitcnt lgkmcnt(6)\n"
  "v_mfma_f32_16x16x32_bf16 v[64:67], v[240:243], v[216:219], v[64:67]\n"
  "ds_read_b64_tr_b16 v[132:133], v247 offset:35144\n"
  "v_mfma_f32_16x16x32_bf16 v[68:71], v[240:243], v[220:223], v[68:71]\n"
  "ds_read_b64_tr_b16 v[134:135], v247 offset:35656\n"
  "v_mfma_f32_16x16x32_bf16 v[72:75], v[128:131], v[216:219], v[72:75]\n"
  "ds_read_b64_tr_b16 v[252:253], v247 offset:35200\n"
  "v_mfma_f32_16x16x32_bf16 v[76:79], v[128:131], v[220:223], v[76:79]\n"
  "ds_read_b64_tr_b16 v[254:255], v247 offset:35712\n"
  "s_waitcnt lgkmcnt(6)\n"
  "v_mfma_f32_16x16x32_bf16 v[80:83], v[136:139], v[216:219], v[80:83]\n"
  "ds_read_b64_tr_b16 v[248:249], v247 offset:35208\n"
  "v_mfma_f32_16x16x32_bf16 v[84:87], v[136:139], v[220:223], v[84:87]\n"
  "ds_read_b64_tr_b16 v[250:251], v247 offset:35720\n"
  "v_mfma_f32_16x16x32_bf16 v[88:91], v[140:143], v[216:219], v[88:91]\n"
  "s_barrier\n"
  "ds_read_b64_tr_b16 v[192:193], v246 offset:0\n"
  "v_mfma_f32_16x16x32_bf16 v[92:95], v[140:143], v[220:223], v[92:95]\n"
  "ds_read_b64_tr_b16 v[194:195], v246 offset:512\n"
  "s_waitcnt lgkmcnt(6)\n"
  "v_mfma_f32_16x16x32_bf16 v[96:99], v[144:147], v[216:219], v[96:99]\n"
  "ds_read_b64_tr_b16 v[196:197], v246 offset:8\n"
  "v_mfma_f32_16x16x32_bf16 v[100:103], v[144:147], v[220:223], v[100:103]\n"
  "ds_read_b64_tr_b16 v[198:199], v246 offset:520\n"
  "v_mfma_f32_16x16x32_bf16 v[104:107], v[132:135], v[216:219], v[104:107]\n"
  "ds_read_b64_tr_b16 v[200:201], v246 offset:64\n"
  "v_mfma_f32_16x16x32_bf16 v[108:111], v[132:135], v[220:223], v[108:111]\n"
  "ds_read_b64_tr_b16 v[202:203], v246 offset:576\n"
  "s_waitcnt lgkmcnt(6)\n"
  "v_mfma_f32_16x16x32_bf16 v[112:115], v[252:255], v[216:219], v[112:115]\n"
  "ds_read_b64_tr_b16 v[204:205], v246 offset:72\n"
  "v_mfma_f32_16x16x32_bf16 v[116:119], v[252:255], v[220:223], v[116:119]\n"
  "ds_read_b64_tr_b16 v[206:207], v246 offset:584\n"
  "v_mfma_f32_16x16x32_bf16 v[120:123], v[248:251], v[216:219], v[120:123]\n"
  "ds_read_b64_tr_b16 v[208:209], v246 offset:128\n"
  "v_mfma_f32_16x16x32_bf16 v[124:127], v[248:251], v[220:223], v[124:127]\n"
  "ds_read_b64_tr_b16 v[210:211], v246 offset:640\n"
  "s_waitcnt lgkmcnt(6)\n"
  "v_mfma_f32_16x16x32_bf16 v[0:3], v[192:195], v[224:227], v[0:3]\n"
  "ds_read_b64_tr_b16 v[240:241], v246 offset:136\n"
  "v_mfma_f32_16x16x32_bf16 v[4:7], v[192:195], v[228:231], v[4:7]\n"
  "ds_read_b64_tr_b16 v[242:243], v246 offset:648\n"
  "v_mfma_f32_16x16x32_bf16 v[8:11], v[196:199], v[224:227], v[8:11]\n"
  "ds_read_b64_tr_b16 v[252:253], v246 offset:192\n"
  "v_mfma_f32_16x16x32_bf16 v[12:15], v[196:199], v[228:231], v[12:15]\n"
  "ds_read_b64_tr_b16 v[254:255], v246 offset:704\n"
  "s_waitcnt lgkmcnt(6)\n"
  "v_mfma_f32_16x16x32_bf16 v[16:19], v[200:203], v[224:227], v[16:19]\n"
  "ds_read_b64_tr_b16 v[248:249], v246 offset:200\n"
  "v_mfma_f32_16x16x32_bf16 v[20:23], v[200:203], v[228:231], v[20:23]\n"
  "ds_read_b64_tr_b16 v[250:251], v246 offset:712\n"
  "v_mfma_f32_16x16x32_bf16 v[24:27], v[204:207], v[224:227], v[24:27]\n"
  "ds_read_b64_tr_b16 v[192:193], v246 offset:256\n"
  "v_mfma_f32_16x16x32_bf16 v[28:31], v[204:207], v[228:231], v[28:31]\n"
  "ds_read_b64_tr_b16 v[194:195], v246 offset:768\n"
  "s_waitcnt lgkmcnt(6)\n"
  "v_mfma_f32_16x16x32_bf16 v[32:35], v[208:211], v[224:227], v[32:35]\n"
  "ds_read_b64_tr_b16 v[196:197], v246 offset:264\n"
  "v_mfma_f32_16x16x32_bf16 v[36:39], v[208:211], v[228:231], v[36:39]\n"
  "ds_read_b64_tr_b16 v[198:199], v246 offset:776\n"
  "v_mfma_f32_16x16x32_bf16 v[40:43], v[240:243], v[224:227], v[40:43]\n"
  "ds_read_b64_tr_b16 v[200:201], v246 offset:320\n"
  "v_mfma_f32_16x16x32_bf16 v[44:47], v[240:243], v[228:231], v[44:47]\n"
  "ds_read_b64_tr_b16 v[202:203], v246 offset:832\n"
  "s_waitcnt lgkmcnt(6)\n"
  "v_mfma_f32_16x16x32_bf16 v[48:51], v[252:255], v[224:227], v[48:51]\n"
  "ds_read_b64_tr_b16 v[204:205], v246 offset:328\n"
  "v_mfma_f32_16x16x32_bf16 v[52:55], v[252:255], v[228:231], v[52:55]\n"
  "ds_read_b64_tr_b16 v[206:207], v246 offset:840\n"
  "v_mfma_f32_16x16x32_bf16 v[56:59], v[248:251], v[224:227], v[56:59]\n"
  "ds_read_b64_tr_b16 v[208:209], v246 offset:384\n"
  "v_mfma_f32_16x16x32_bf16 v[60:63], v[248:251], v[228:231], v[60:63]\n"
  "ds_read_b64_tr_b16 v[210:211], v246 offset:896\n"
  "s_waitcnt lgkmcnt(6)\n"
  "v_mfma_f32_16x16x32_bf16 v[64:67], v[192:195], v[224:227], v[64:67]\n"
  "ds_read_b64_tr_b16 v[240:241], v246 offset:392\n"
  "v_mfma_f32_16x16x32_bf16 v[68:71], v[192:195], v[228:231], v[68:71]\n"
  "ds_read_b64_tr_b16 v[242:243], v246 offset:904\n"
  "v_mfma_f32_16x16x32_bf16 v[72:75], v[196:199], v[224:227], v[72:75]\n"
  "ds_read_b64_tr_b16 v[252:253], v246 offset:448\n"
  "v_mfma_f32_16x16x32_bf16 v[76:79], v[196:199], v[228:231], v[76:79]\n"
  "ds_read_b64_tr_b16 v[254:255], v246 offset:960\n"
  "s_waitcnt lgkmcnt(6)\n"
  "v_mfma_f32_16x16x32_bf16 v[80:83], v[200:203], v[224:227], v[80:83]\n"
  "ds_read_b64_tr_b16 v[248:249], v246 offset:456\n"
  "v_mfma_f32_16x16x32_bf16 v[84:87], v[200:203], v[228:231], v[84:87]\n"
  "ds_read_b64_tr_b16 v[250:251], v246 offset:968\n"
  "v_mfma_f32_16x16x32_bf16 v[88:91], v[204:207], v[224:227], v[88:91]\n"
  "s_barrier\n"
  "ds_read_b64_tr_b16 v[192:193], v246 offset:34752\n"
  "v_mfma_f32_16x16x32_bf16 v[92:95], v[204:207], v[228:231], v[92:95]\n"
  "ds_read_b64_tr_b16 v[194:195], v246 offset:35264\n"
  "s_waitcnt lgkmcnt(6)\n"
  "v_mfma_f32_16x16x32_bf16 v[96:99], v[208:211], v[224:227], v[96:99]\n"
  "ds_read_b64_tr_b16 v[196:197], v246 offset:34760\n"
  "v_mfma_f32_16x16x32_bf16 v[100:103], v[208:211], v[228:231], v[100:103]\n"
  "ds_read_b64_tr_b16 v[198:199], v246 offset:35272\n"
  "v_mfma_f32_16x16x32_bf16 v[104:107], v[240:243], v[224:227], v[104:107]\n"
  "ds_read_b64_tr_b16 v[200:201], v246 offset:34816\n"
  "v_mfma_f32_16x16x32_bf16 v[108:111], v[240:243], v[228:231], v[108:111]\n"
  "ds_read_b64_tr_b16 v[202:203], v246 offset:35328\n"
  "s_waitcnt lgkmcnt(6)\n"
  "v_mfma_f32_16x16x32_bf16 v[112:115], v[252:255], v[224:227], v[112:115]\n"
  "ds_read_b64_tr_b16 v[204:205], v246 offset:34824\n"
  "v_mfma_f32_16x16x32_bf16 v[116:119], v[252:255], v[228:231], v[116:119]\n"
  "ds_read_b64_tr_b16 v[206:207], v246 offset:35336\n"
  "v_mfma_f32_16x16x32_bf16 v[120:123], v[248:251], v[224:227], v[120:123]\n"
  "ds_read_b64_tr_b16 v[208:209], v246 offset:34880\n"
  "v_mfma_f32_16x16x32_bf16 v[124:127], v[248:251], v[228:231], v[124:127]\n"
  "ds_read_b64_tr_b16 v[210:211], v246 offset:35392\n"
  "s_waitcnt lgkmcnt(6)\n"
  "v_mfma_f32_16x16x32_bf16 v[0:3], v[192:195], v[232:235], v[0:3]\n"
  "ds_read_b64_tr_b16 v[240:241], v246 offset:34888\n"
  "v_mfma_f32_16x16x32_bf16 v[4:7], v[192:195], v[236:239], v[4:7]\n"
  "ds_read_b64_tr_b16 v[242:243], v246 offset:35400\n"
  "v_mfma_f32_16x16x32_bf16 v[8:11], v[196:199], v[232:235], v[8:11]\n"
  "ds_read_b64_tr_b16 v[252:253], v246 offset:34944\n"
  "v_mfma_f32_16x16x32_bf16 v[12:15], v[196:199], v[236:239], v[12:15]\n"
  "ds_read_b64_tr_b16 v[254:255], v246 offset:35456\n"
  "s_waitcnt lgkmcnt(6)\n"
  "v_mfma_f32_16x16x32_bf16 v[16:19], v[200:203], v[232:235], v[16:19]\n"
  "ds_read_b64_tr_b16 v[248:249], v246 offset:34952\n"
  "v_cvt_pk_bf16_f32 v128, v0, v1\n"
  "v_cvt_pk_bf16_f32 v129, v2, v3\n"
  "v_mfma_f32_16x16x32_bf16 v[20:23], v[200:203], v[236:239], v[20:23]\n"
  "ds_read_b64_tr_b16 v[250:251], v246 offset:35464\n"
  "v_cvt_pk_bf16_f32 v132, v4, v5\n"
  "v_cvt_pk_bf16_f32 v133, v6, v7\n"
  "v_mfma_f32_16x16x32_bf16 v[24:27], v[204:207], v[232:235], v[24:27]\n"
  "ds_read_b64_tr_b16 v[192:193], v246 offset:35008\n"
  "v_cvt_pk_bf16_f32 v130, v8, v9\n"
  "v_cvt_pk_bf16_f32 v131, v10, v11\n"
  "v_mfma_f32_16x16x32_bf16 v[28:31], v[204:207], v[236:239], v[28:31]\n"
  "ds_read_b64_tr_b16 v[194:195], v246 offset:35520\n"
  "v_cvt_pk_bf16_f32 v134, v12, v13\n"
  "v_cvt_pk_bf16_f32 v135, v14, v15\n"
  "s_waitcnt lgkmcnt(6)\n"
  "v_mfma_f32_16x16x32_bf16 v[32:35], v[208:211], v[232:235], v[32:35]\n"
  "ds_read_b64_tr_b16 v[196:197], v246 offset:35016\n"
  "v_cvt_pk_bf16_f32 v136, v16, v17\n"
  "v_cvt_pk_bf16_f32 v137, v18, v19\n"
  "v_mfma_f32_16x16x32_bf16 v[36:39], v[208:211], v[236:239], v[36:39]\n"
  "ds_read_b64_tr_b16 v[198:199], v246 offset:35528\n"
  "v_cvt_pk_bf16_f32 v140, v20, v21\n"
  "v_cvt_pk_bf16_f32 v141, v22, v23\n"
  "v_mfma_f32_16x16x32_bf16 v[40:43], v[240:243], v[232:235], v[40:43]\n"
  "ds_read_b64_tr_b16 v[200:201], v246 offset:35072\n"
  "v_cvt_pk_bf16_f32 v138, v24, v25\n"
  "v_cvt_pk_bf16_f32 v139, v26, v27\n"
  "v_mfma_f32_16x16x32_bf16 v[44:47], v[240:243], v[236:239], v[44:47]\n"
  "ds_read_b64_tr_b16 v[202:203], v246 offset:35584\n"
  "v_cvt_pk_bf16_f32 v142, v28, v29\n"
  "v_cvt_pk_bf16_f32 v143, v30, v31\n"
  "s_waitcnt lgkmcnt(6)\n"
  "v_mfma_f32_16x16x32_bf16 v[48:51], v[252:255], v[232:235], v[48:51]\n"
  "ds_read_b64_tr_b16 v[204:205], v246 offset:35080\n"
  "v_cvt_pk_bf16_f32 v144, v32, v33\n"
  "v_cvt_pk_bf16_f32 v145, v34, v35\n"
  "v_mfma_f32_16x16x32_bf16 v[52:55], v[252:255], v[236:239], v[52:55]\n"
  "ds_read_b64_tr_b16 v[206:207], v246 offset:35592\n"
  "v_cvt_pk_bf16_f32 v148, v36, v37\n"
  "v_cvt_pk_bf16_f32 v149, v38, v39\n"
  "v_mfma_f32_16x16x32_bf16 v[56:59], v[248:251], v[232:235], v[56:59]\n"
  "ds_read_b64_tr_b16 v[208:209], v246 offset:35136\n"
  "v_cvt_pk_bf16_f32 v146, v40, v41\n"
  "v_cvt_pk_bf16_f32 v147, v42, v43\n"
  "v_mfma_f32_16x16x32_bf16 v[60:63], v[248:251], v[236:239], v[60:63]\n"
  "ds_read_b64_tr_b16 v[210:211], v246 offset:35648\n"
  "v_cvt_pk_bf16_f32 v150, v44, v45\n"
  "v_cvt_pk_bf16_f32 v151, v46, v47\n"
  "s_waitcnt lgkmcnt(6)\n"
  "v_mfma_f32_16x16x32_bf16 v[64:67], v[192:195], v[232:235], v[64:67]\n"
  "ds_read_b64_tr_b16 v[240:241], v246 offset:35144\n"
  "v_cvt_pk_bf16_f32 v152, v48, v49\n"
  "v_cvt_pk_bf16_f32 v153, v50, v51\n"
  "v_mfma_f32_16x16x32_bf16 v[68:71], v[192:195], v[236:239], v[68:71]\n"
  "ds_read_b64_tr_b16 v[242:243], v246 offset:35656\n"
  "v_cvt_pk_bf16_f32 v156, v52, v53\n"
  "v_cvt_pk_bf16_f32 v157, v54, v55\n"
  "v_mfma_f32_16x16x32_bf16 v[72:75], v[196:199], v[232:235], v[72:75]\n"
  "ds_read_b64_tr_b16 v[252:253], v246 offset:35200\n"
  "v_cvt_pk_bf16_f32 v154, v56, v57\n"
  "v_cvt_pk_bf16_f32 v155, v58, v59\n"
  "v_mfma_f32_16x16x32_bf16 v[76:79], v[196:199], v[236:239], v[76:79]\n"
  "ds_read_b64_tr_b16 v[254:255], v246 offset:35712\n"
  "v_cvt_pk_bf16_f32 v158, v60, v61\n"
  "v_cvt_pk_bf16_f32 v159, v62, v63\n"
  "s_waitcnt lgkmcnt(6)\n"
  "v_mfma_f32_16x16x32_bf16 v[80:83], v[200:203], v[232:235], v[80:83]\n"
  "ds_read_b64_tr_b16 v[248:249], v246 offset:35208\n"
  "v_cvt_pk_bf16_f32 v160, v64, v65\n"
  "v_cvt_pk_bf16_f32 v161, v66, v67\n"
  "v_mfma_f32_16x16x32_bf16 v[84:87], v[200:203], v[236:239], v[84:87]\n"
  "ds_read_b64_tr_b16 v[250:251], v246 offset:35720\n"
  "v_cvt_pk_bf16_f32 v164, v68, v69\n"
  "v_cvt_pk_bf16_f32 v165, v70, v71\n"
  "v_mfma_f32_16x16x32_bf16 v[88:91], v[204:207], v[232:235], v[88:91]\n"
  "v_cvt_pk_bf16_f32 v162, v72, v73\n"
  "v_cvt_pk_bf16_f32 v163, v74, v75\n"
  "v_mfma_f32_16x16x32_bf16 v[92:95], v[204:207], v[236:239], v[92:95]\n"
  "s_barrier\n"
  "ds_read_b128 v[224:227], v244 offset:17376\n"
  "v_cvt_pk_bf16_f32 v166, v76, v77\n"
  "v_cvt_pk_bf16_f32 v167, v78, v79\n"
  "s_waitcnt lgkmcnt(5)\n"
  "v_mfma_f32_16x16x32_bf16 v[96:99], v[208:211], v[232:235], v[96:99]\n"
  "v_cvt_pk_bf16_f32 v168, v80, v81\n"
  "v_cvt_pk_bf16_f32 v169, v82, v83\n"
  "v_mfma_f32_16x16x32_bf16 v[100:103], v[208:211], v[236:239], v[100:103]\n"
  "ds_read_b128 v[228:231], v244 offset:17440\n"
  "v_cvt_pk_bf16_f32 v172, v84, v85\n"
  "v_cvt_pk_bf16_f32 v173, v86, v87\n"
  "v_mfma_f32_16x16x32_bf16 v[104:107], v[240:243], v[232:235], v[104:107]\n"
  "v_cvt_pk_bf16_f32 v170, v88, v89\n"
  "v_cvt_pk_bf16_f32 v171, v90, v91\n"
  "v_mfma_f32_16x16x32_bf16 v[108:111], v[240:243], v[236:239], v[108:111]\n"
  "ds_read_b128 v[216:219], v244 offset:17504\n"
  "v_cvt_pk_bf16_f32 v174, v92, v93\n"
  "v_cvt_pk_bf16_f32 v175, v94, v95\n"
  "s_waitcnt lgkmcnt(3)\n"
  "v_mfma_f32_16x16x32_bf16 v[112:115], v[252:255], v[232:235], v[112:115]\n"
  "v_cvt_pk_bf16_f32 v176, v96, v97\n"
  "v_cvt_pk_bf16_f32 v177, v98, v99\n"
  "v_mfma_f32_16x16x32_bf16 v[116:119], v[252:255], v[236:239], v[116:119]\n"
  "ds_read_b128 v[220:223], v244 offset:17568\n"
  "v_cvt_pk_bf16_f32 v180, v100, v101\n"
  "v_cvt_pk_bf16_f32 v181, v102, v103\n"
  "v_mfma_f32_16x16x32_bf16 v[120:123], v[248:251], v[232:235], v[120:123]\n"
  "v_cvt_pk_bf16_f32 v178, v104, v105\n"
  "v_cvt_pk_bf16_f32 v179, v106, v107\n"
  "v_mfma_f32_16x16x32_bf16 v[124:127], v[248:251], v[236:239], v[124:127]\n"
  "ds_read_b128 v[240:243], v244 offset:17632\n"
  "v_cvt_pk_bf16_f32 v182, v108, v109\n"
  "v_cvt_pk_bf16_f32 v183, v110, v111\n"
  "s_waitcnt lgkmcnt(3)\n"
  "v_mfma_f32_16x16x32_bf16 v[192:195], v[224:227], v[128:131], 0\n"
  "v_mfma_f32_16x16x32_bf16 v[192:195], v[228:231], v[136:139], v[192:195]\n"
  "ds_read_b128 v[252:255], v244 offset:17696\n"
  "v_mfma_f32_16x16x32_bf16 v[196:199], v[224:227], v[132:135], 0\n"
  "v_cvt_pk_bf16_f32 v184, v112, v113\n"
  "v_cvt_pk_bf16_f32 v185, v114, v115\n"
  "v_mfma_f32_16x16x32_bf16 v[196:199], v[228:231], v[140:143], v[196:199]\n"
  "ds_read_b128 v[232:235], v244 offset:17760\n"
  "v_cvt_pk_bf16_f32 v188, v116, v117\n"
  "v_cvt_pk_bf16_f32 v189, v118, v119\n"
  "s_waitcnt lgkmcnt(3)\n"
  "v_mfma_f32_16x16x32_bf16 v[196:199], v[216:219], v[148:151], v[196:199]\n"
  "v_cvt_pk_bf16_f32 v186, v120, v121\n"
  "v_cvt_pk_bf16_f32 v187, v122, v123\n"
  "v_mfma_f32_16x16x32_bf16 v[196:199], v[220:223], v[156:159], v[196:199]\n"
  "ds_read_b128 v[236:239], v244 offset:17824\n"
  "v_cvt_pk_bf16_f32 v190, v124, v125\n"
  "v_cvt_pk_bf16_f32 v191, v126, v127\n"
  "v_mfma_f32_16x16x32_bf16 v[192:195], v[216:219], v[144:147], v[192:195]\n"
  "v_mfma_f32_16x16x32_bf16 v[192:195], v[220:223], v[152:155], v[192:195]\n"
  "ds_read_b128 v[224:227], v244 offset:25824\n"
  "s_waitcnt lgkmcnt(3)\n"
  "v_mfma_f32_16x16x32_bf16 v[192:195], v[240:243], v[160:163], v[192:195]\n"
  "v_mfma_f32_16x16x32_bf16 v[192:195], v[252:255], v[168:171], v[192:195]\n"
  "ds_read_b128 v[228:231], v244 offset:25888\n"
  "v_mfma_f32_16x16x32_bf16 v[196:199], v[240:243], v[164:167], v[196:199]\n"
  "v_mfma_f32_16x16x32_bf16 v[196:199], v[252:255], v[172:175], v[196:199]\n"
  "s_waitcnt lgkmcnt(2)\n"
  "v_mfma_f32_16x16x32_bf16 v[196:199], v[232:235], v[180:183], v[196:199]\n"
  "v_mfma_f32_16x16x32_bf16 v[196:199], v[236:239], v[188:191], v[196:199]\n"
  "v_mfma_f32_16x16x32_bf16 v[192:195], v[232:235], v[176:179], v[192:195]\n"
  "v_mfma_f32_16x16x32_bf16 v[192:195], v[236:239], v[184:187], v[192:195]\n"
  "ds_read_b128 v[232:235], v244 offset:25952\n"
  "s_waitcnt lgkmcnt(1)\n"
  "v_mfma_f32_16x16x32_bf16 v[208:211], v[224:227], v[128:131], 0\n"
  "ds_read_b128 v[236:239], v244 offset:26016\n"
  "v_mfma_f32_16x16x32_bf16 v[208:211], v[228:231], v[136:139], v[208:211]\n"
  "ds_read_b128 v[240:243], v244 offset:26080\n"
  "v_mfma_f32_16x16x32_bf16 v[212:215], v[224:227], v[132:135], 0\n"
  "ds_read_b128 v[252:255], v244 offset:26144\n"
  "v_mfma_f32_16x16x32_bf16 v[212:215], v[228:231], v[140:143], v[212:215]\n"
  "ds_read_b128 v[224:227], v244 offset:26208\n"
  "s_waitcnt lgkmcnt(3)\n"
  "v_mfma_f32_16x16x32_bf16 v[212:215], v[232:235], v[148:151], v[212:215]\n"
  "v_mfma_f32_16x16x32_bf16 v[212:215], v[236:239], v[156:159], v[212:215]\n"
  "ds_read_b128 v[228:231], v244 offset:26272\n"
  "v_mfma_f32_16x16x32_bf16 v[208:211], v[232:235], v[144:147], v[208:211]\n"
  "v_mfma_f32_16x16x32_bf16 v[208:211], v[236:239], v[152:155], v[208:211]\n"
  "s_barrier\n"
  "ds_read_b128 v[232:235], v244 offset:34752\n"
  "s_waitcnt lgkmcnt(3)\n"
  "v_mfma_f32_16x16x32_bf16 v[208:211], v[240:243], v[160:163], v[208:211]\n"
  "ds_read_b128 v[236:239], v244 offset:34816\n"
  "v_mfma_f32_16x16x32_bf16 v[208:211], v[252:255], v[168:171], v[208:211]\n"
  "v_mfma_f32_16x16x32_bf16 v[212:215], v[240:243], v[164:167], v[212:215]\n"
  "v_mfma_f32_16x16x32_bf16 v[212:215], v[252:255], v[172:175], v[212:215]\n"
  "ds_read_b128 v[240:243], v244 offset:34880\n"
  "s_waitcnt lgkmcnt(3)\n"
  "v_mfma_f32_16x16x32_bf16 v[212:215], v[224:227], v[180:183], v[212:215]\n"
  "v_mfma_f32_16x16x32_bf16 v[212:215], v[228:231], v[188:191], v[212:215]\n"
  "v_mfma_f32_16x16x32_bf16 v[208:211], v[224:227], v[176:179], v[208:211]\n"
  "v_mfma_f32_16x16x32_bf16 v[208:211], v[228:231], v[184:187], v[208:211]\n"
  "ds_read_b128 v[224:227], v244 offset:34944\n"
  "v_max3_f32 v248, v192, v193, v194\n"
  "s_nop 5\n"
  "v_max3_f32 v249, v208, v209, v210\n"
  "v_max3_f32 v248, v248, v195, v211\n"
  "v_max_f32 v248, v248, v249\n"
  "v_max3_f32 v250, v196, v197, v198\n"
  "v_max3_f32 v251, v212, v213, v214\n"
  "v_max3_f32 v250, v250, v199, v215\n"
  "v_max_f32 v250, v250, v251\n"
  "v_mov_b32 v249, v248\n"
  "v_mov_b32 v251, v250\n"
  "s_nop 0\n"
  "v_permlane32_swap_b32 v248, v249\n"
  "v_permlane32_swap_b32 v250, v251\n"
  "v_max_f32 v248, v248, v249\n"
  "v_max_f32 v250, v250, v251\n"
  "v_mov_b32 v249, v248\n"
  "v_mov_b32 v251, v250\n"
  "s_nop 0\n"
  "v_permlane16_swap_b32 v248, v249\n"
  "v_permlane16_swap_b32 v250, v251\n"
  "v_max_f32 v248, v248, v249\n"
  "v_max_f32 v250, v250, v251\n"
  "v_sub_f32 v216, 0, v248\n"
  "v_sub_f32 v217, 0, v248\n"
  "v_sub_f32 v218, 0, v248\n"
  "v_sub_f32 v219, 0, v248\n"
  "v_mov_b32 v246, 0\n"
  "v_sub_f32 v220, 0, v250\n"
  "v_sub_f32 v221, 0, v250\n"
  "v_sub_f32 v222, 0, v250\n"
  "v_sub_f32 v223, 0, v250\n"
  "v_mov_b32 v247, 0\n"
  "s_waitcnt lgkmcnt(2)\n"
  "v_mfma_f32_16x16x32_bf16 v[200:203], v[232:235], v[128:131], v[216:219]\n"
  "ds_read_b128 v[228:231], v244 offset:35008\n"
  "v_add_f32 v192, v192, v216\n"
  "v_add_f32 v196, v196, v220\n"
  "v_add_f32 v193, v193, v216\n"
  "v_add_f32 v197, v197, v220\n"
  "v_mfma_f32_16x16x32_bf16 v[200:203], v[236:239], v[136:139], v[200:203]\n"
  "v_add_f32 v194, v194, v216\n"
  "v_add_f32 v198, v198, v220\n"
  "v_add_f32 v195, v195, v216\n"
  "v_add_f32 v199, v199, v220\n"
  "v_mfma_f32_16x16x32_bf16 v[204:207], v[232:235], v[132:135], v[220:223]\n"
  "v_exp_f32 v192, v192\n"
  "v_exp_f32 v196, v196\n"
  "v_exp_f32 v193, v193\n"
  "v_add_f32 v246, v246, v192\n"
  "v_mfma_f32_16x16x32_bf16 v[204:207], v[236:239], v[140:143], v[204:207]\n"
  "ds_read_b128 v[232:235], v244 offset:35072\n"
  "v_exp_f32 v197, v197\n"
  "v_add_f32 v247, v247, v196\n"
  "v_exp_f32 v194, v194\n"
  "v_add_f32 v246, v246, v193\n"
  "s_waitcnt lgkmcnt(2)\n"
  "v_mfma_f32_16x16x32_bf16 v[204:207], v[240:243], v[148:151], v[204:207]\n"
  "ds_read_b128 v[236:239], v244 offset:35136\n"
  "v_exp_f32 v198, v198\n"
  "v_add_f32 v247, v247, v197\n"
  "v_cvt_pk_bf16_f32 v192, v192, v193\n"
  "v_exp_f32 v195, v195\n"
  "v_mfma_f32_16x16x32_bf16 v[204:207], v[224:227], v[156:159], v[204:207]\n"
  "v_cvt_pk_bf16_f32 v196, v196, v197\n"
  "v_exp_f32 v199, v199\n"
  "v_add_f32 v246, v246, v194\n"
  "v_add_f32 v247, v247, v198\n"
  "v_mfma_f32_16x16x32_bf16 v[200:203], v[240:243], v[144:147], v[200:203]\n"
  "v_add_f32 v246, v246, v195\n"
  "v_add_f32 v247, v247, v199\n"
  "v_cvt_pk_bf16_f32 v193, v194, v195\n"
  "v_cvt_pk_bf16_f32 v197, v198, v199\n"
  "v_mfma_f32_16x16x32_bf16 v[200:203], v[224:227], v[152:155], v[200:203]\n"
  "ds_read_b128 v[240:243], v244 offset:35200\n"
  "v_add_f32 v208, v208, v216\n"
  "v_add_f32 v212, v212, v220\n"
  "v_add_f32 v209, v209, v216\n"
  "v_add_f32 v213, v213, v220\n"
  "s_waitcnt lgkmcnt(2)\n"
  "v_mfma_f32_16x16x32_bf16 v[200:203], v[228:231], v[160:163], v[200:203]\n"
  "ds_read_b128 v[252:255], v244 offset:43200\n"
  "v_add_f32 v210, v210, v216\n"
  "v_add_f32 v214, v214, v220\n"
  "v_add_f32 v211, v211, v216\n"
  "v_add_f32 v215, v215, v220\n"
  "v_mfma_f32_16x16x32_bf16 v[200:203], v[232:235], v[168:171], v[200:203]\n"
  "ds_read_b128 v[224:227], v244 offset:43264\n"
  "v_exp_f32 v208, v208\n"
  "v_exp_f32 v212, v212\n"
  "v_exp_f32 v209, v209\n"
  "v_add_f32 v246, v246, v208\n"
  "v_mfma_f32_16x16x32_bf16 v[204:207], v[228:231], v[164:167], v[204:207]\n"
  "v_exp_f32 v213, v213\n"
  "v_add_f32 v247, v247, v212\n"
  "v_exp_f32 v210, v210\n"
  "v_add_f32 v246, v246, v209\n"
  "v_mfma_f32_16x16x32_bf16 v[204:207], v[232:235], v[172:175], v[204:207]\n"
  "ds_read_b128 v[228:231], v244 offset:43328\n"
  "v_exp_f32 v214, v214\n"
  "v_add_f32 v247, v247, v213\n"
  "v_cvt_pk_bf16_f32 v194, v208, v209\n"
  "v_exp_f32 v211, v211\n"
  "s_waitcnt lgkmcnt(3)\n"
  "v_mfma_f32_16x16x32_bf16 v[204:207], v[236:239], v[180:183], v[204:207]\n"
  "ds_read_b128 v[232:235], v244 offset:43392\n"
  "v_cvt_pk_bf16_f32 v198, v212, v213\n"
  "v_exp_f32 v215, v215\n"
  "v_add_f32 v250, v210, v211\n"
  "v_cvt_pk_bf16_f32 v195, v210, v211\n"
  "v_mfma_f32_16x16x32_bf16 v[204:207], v[240:243], v[188:191], v[204:207]\n"
  "v_add_f32 v251, v214, v215\n"
  "v_cvt_pk_bf16_f32 v199, v214, v215\n"
  "v_add_f32 v246, v246, v250\n"
  "v_add_f32 v247, v247, v251\n"
  "v_mfma_f32_16x16x32_bf16 v[200:203], v[236:239], v[176:179], v[200:203]\n"
  "v_mfma_f32_16x16x32_bf16 v[200:203], v[240:243], v[184:187], v[200:203]\n"
  "ds_read_b128 v[236:239], v244 offset:43456\n"
  "s_waitcnt lgkmcnt(3)\n"
  "v_mfma_f32_16x16x32_bf16 v[208:211], v[252:255], v[128:131], v[216:219]\n"
  "ds_read_b128 v[240:243], v244 offset:43520\n"
  "v_mfma_f32_16x16x32_bf16 v[208:211], v[224:227], v[136:139], v[208:211]\n"
  "v_mfma_f32_16x16x32_bf16 v[212:215], v[252:255], v[132:135], v[220:223]\n"
  "s_nop 1\n"
  "v_max3_f32 v248, v200, v201, v202\n"
  "v_mfma_f32_16x16x32_bf16 v[212:215], v[224:227], v[140:143], v[212:215]\n"
  "ds_read_b128 v[252:255], v244 offset:43584\n"
  "s_waitcnt lgkmcnt(3)\n"
  "v_mfma_f32_16x16x32_bf16 v[212:215], v[228:231], v[148:151], v[212:215]\n"
  "ds_read_b128 v[224:227], v244 offset:43648\n"
  "v_mfma_f32_16x16x32_bf16 v[212:215], v[232:235], v[156:159], v[212:215]\n"
  "v_max3_f32 v249, v204, v205, v206\n"
  "v_max3_f32 v248, v248, v203, v207\n"
  "v_mfma_f32_16x16x32_bf16 v[208:211], v[228:231], v[144:147], v[208:211]\n"
  "v_max_f32 v248, v248, v249\n"
  "v_cmp_lt_f32 vcc, s42, v248\n"
  "v_mfma_f32_16x16x32_bf16 v[208:211], v[232:235], v[152:155], v[208:211]\n"
  "ds_read_b64_tr_b16 v[228:229], v245 offset:52128\n"
  "ds_read_b64_tr_b16 v[230:231], v245 offset:60576\n"
  "s_waitcnt lgkmcnt(4)\n"
  "v_mfma_f32_16x16x32_bf16 v[208:211], v[236:239], v[160:163], v[208:211]\n"
  "ds_read_b64_tr_b16 v[232:233], v245 offset:52160\n"
  "s_cbranch_vccnz .Lrare_pro_1_0\n"
  ".Lback_pro_1_0:\n"
  "v_mfma_f32_16x16x32_bf16 v[208:211], v[240:243], v[168:171], v[208:211]\n"
  "ds_read_b64_tr_b16 v[234:235], v245 offset:60608\n"
  "v_exp_f32 v200, v200\n"
  "v_mfma_f32_16x16x32_bf16 v[212:215], v[236:239], v[164:167], v[212:215]\n"
  "v_exp_f32 v204, v204\n"
  "v_mfma_f32_16x16x32_bf16 v[212:215], v[240:243], v[172:175], v[212:215]\n"
  "ds_read_b64_tr_b16 v[236:237], v245 offset:52192\n"
  "ds_read_b64_tr_b16 v[238:239], v245 offset:60640\n"
  "s_waitcnt lgkmcnt(6)\n"
  "v_mfma_f32_16x16x32_bf16 v[212:215], v[252:255], v[180:183], v[212:215]\n"
  "ds_read_b64_tr_b16 v[240:241], v245 offset:52224\n"
  "v_exp_f32 v201, v201\n"
  "v_mfma_f32_16x16x32_bf16 v[212:215], v[224:227], v[188:191], v[212:215]\n"
  "ds_read_b64_tr_b16 v[242:243], v245 offset:60672\n"
  "v_add_f32 v246, v246, v200\n"
  "v_mfma_f32_16x16x32_bf16 v[208:211], v[252:255], v[176:179], v[208:211]\n"
  "v_exp_f32 v205, v205\n"
  "v_mfma_f32_16x16x32_bf16 v[208:211], v[224:227], v[184:187], v[208:211]\n"
  "ds_read_b64_tr_b16 v[252:253], v245 offset:52256\n"
  "ds_read_b64_tr_b16 v[254:255], v245 offset:60704\n"
  "s_waitcnt lgkmcnt(6)\n"
  "v_mfma_f32_16x16x32_bf16 v[0:3], v[228:231], v[192:195], 0\n"
  "ds_read_b64_tr_b16 v[224:225], v245 offset:52288\n"
  "v_mfma_f32_16x16x32_bf16 v[4:7], v[228:231], v[196:199], 0\n"
  "ds_read_b64_tr_b16 v[226:227], v245 offset:60736\n"
  "v_add_f32 v247, v247, v204\n"
  "v_mfma_f32_16x16x32_bf16 v[8:11], v[232:235], v[192:195], 0\n"
  "ds_read_b64_tr_b16 v[228:229], v245 offset:52320\n"
  "v_exp_f32 v202, v202\n"
  "v_mfma_f32_16x16x32_bf16 v[12:15], v[232:235], v[196:199], 0\n"
  "ds_read_b64_tr_b16 v[230:231], v245 offset:60768\n"
  "s_waitcnt lgkmcnt(6)\n"
  "v_mfma_f32_16x16x32_bf16 v[16:19], v[236:239], v[192:195], 0\n"
  "ds_read_b64_tr_b16 v[232:233], v245 offset:52352\n"
  "v_add_f32 v246, v246, v201\n"
  "v_mfma_f32_16x16x32_bf16 v[20:23], v[236:239], v[196:199], 0\n"
  "ds_read_b64_tr_b16 v[234:235], v245 offset:60800\n"
  "v_exp_f32 v206, v206\n"
  "v_mfma_f32_16x16x32_bf16 v[24:27], v[240:243], v[192:195], 0\n"
  "ds_read_b64_tr_b16 v[236:237], v245 offset:52384\n"
  "v_add_f32 v247, v247, v205\n"
  "v_mfma_f32_16x16x32_bf16 v[28:31], v[240:243], v[196:199], 0\n"
  "ds_read_b64_tr_b16 v[238:239], v245 offset:60832\n"
  "s_waitcnt lgkmcnt(6)\n"
  "v_mfma_f32_16x16x32_bf16 v[32:35], v[252:255], v[192:195], 0\n"
  "ds_read_b64_tr_b16 v[240:241], v245 offset:52416\n"
  "v_cvt_pk_bf16_f32 v200, v200, v201\n"
  "v_mfma_f32_16x16x32_bf16 v[36:39], v[252:255], v[196:199], 0\n"
  "ds_read_b64_tr_b16 v[242:243], v245 offset:60864\n"
  "v_exp_f32 v203, v203\n"
  "v_mfma_f32_16x16x32_bf16 v[40:43], v[224:227], v[192:195], 0\n"
  "ds_read_b64_tr_b16 v[252:253], v245 offset:52448\n"
  "v_cvt_pk_bf16_f32 v204, v204, v205\n"
  "v_mfma_f32_16x16x32_bf16 v[44:47], v[224:227], v[196:199], 0\n"
  "ds_read_b64_tr_b16 v[254:255], v245 offset:60896\n"
  "s_waitcnt lgkmcnt(6)\n"
  "v_mfma_f32_16x16x32_bf16 v[48:51], v[228:231], v[192:195], 0\n"
  "ds_read_b64_tr_b16 v[224:225], v245 offset:52480\n"
  "v_exp_f32 v207, v207\n"
  "v_mfma_f32_16x16x32_bf16 v[52:55], v[228:231], v[196:199], 0\n"
  "ds_read_b64_tr_b16 v[226:227], v245 offset:60928\n"
  "v_add_f32 v246, v246, v202\n"
  "v_mfma_f32_16x16x32_bf16 v[56:59], v[232:235], v[192:195], 0\n"
  "ds_read_b64_tr_b16 v[228:229], v245 offset:52512\n"
  "v_add_f32 v247, v247, v206\n"
  "v_mfma_f32_16x16x32_bf16 v[60:63], v[232:235], v[196:199], 0\n"
  "ds_read_b64_tr_b16 v[230:231], v245 offset:60960\n"
  "s_waitcnt lgkmcnt(6)\n"
  "v_mfma_f32_16x16x32_bf16 v[64:67], v[236:239], v[192:195], 0\n"
  "ds_read_b64_tr_b16 v[232:233], v245 offset:52544\n"
  "v_add_f32 v246, v246, v203\n"
  "v_mfma_f32_16x16x32_bf16 v[68:71], v[236:239], v[196:199], 0\n"
  "ds_read_b64_tr_b16 v[234:235], v245 offset:60992\n"
  "v_add_f32 v247, v247, v207\n"
  "v_mfma_f32_16x16x32_bf16 v[72:75], v[240:243], v[192:195], 0\n"
  "ds_read_b64_tr_b16 v[236:237], v245 offset:52576\n"
  "v_cvt_pk_bf16_f32 v201, v202, v203\n"
  "v_mfma_f32_16x16x32_bf16 v[76:79], v[240:243], v[196:199], 0\n"
  "ds_read_b64_tr_b16 v[238:239], v245 offset:61024\n"
  "s_waitcnt lgkmcnt(6)\n"
  "v_mfma_f32_16x16x32_bf16 v[80:83], v[252:255], v[192:195], 0\n"
  "ds_read_b64_tr_b16 v[240:241], v245 offset:52608\n"
  "v_cvt_pk_bf16_f32 v205, v206, v207\n"
  "v_mfma_f32_16x16x32_bf16 v[84:87], v[252:255], v[196:199], 0\n"
  "ds_read_b64_tr_b16 v[242:243], v245 offset:61056\n"
  "v_max3_f32 v248, v208, v209, v210\n"
  "v_mfma_f32_16x16x32_bf16 v[88:91], v[224:227], v[192:195], 0\n"
  "v_max3_f32 v249, v212, v213, v214\n"
  "v_max3_f32 v248, v248, v211, v215\n"
  "v_mfma_f32_16x16x32_bf16 v[92:95], v[224:227], v[196:199], 0\n"
  "s_barrier\n"
  "v_add_u32 v244, s41, v244\n"
  "ds_read_b128 v[224:227], v244 offset:0\n"
  "s_waitcnt lgkmcnt(5)\n"
  "v_mfma_f32_16x16x32_bf16 v[96:99], v[228:231], v[192:195], 0\n"
  "v_max_f32 v248, v248, v249\n"
  "v_cmp_lt_f32 vcc, s42, v248\n"
  "v_mfma_f32_16x16x32_bf16 v[100:103], v[228:231], v[196:199], 0\n"
  "ds_read_b128 v[228:231], v244 offset:64\n"
  "s_cbranch_vccnz .Lrare_pro_1_1\n"
  ".Lback_pro_1_1:\n"
  "v_mfma_f32_16x16x32_bf16 v[104:107], v[232:235], v[192:195], 0\n"
  "v_exp_f32 v208, v208\n"
  "v_mfma_f32_16x16x32_bf16 v[108:111], v[232:235], v[196:199], 0\n"
  "ds_read_b128 v[232:235], v244 offset:128\n"
  "s_waitcnt lgkmcnt(3)\n"
  "v_mfma_f32_16x16x32_bf16 v[112:115], v[236:239], v[192:195], 0\n"
  "v_exp_f32 v212, v212\n"
  "v_mfma_f32_16x16x32_bf16 v[116:119], v[236:239], v[196:199], 0\n"
  "ds_read_b128 v[236:239], v244 offset:192\n"
  "v_mfma_f32_16x16x32_bf16 v[120:123], v[240:243], v[192:195], 0\n"
  "v_exp_f32 v209, v209\n"
  "v_mfma_f32_16x16x32_bf16 v[124:127], v[240:243], v[196:199], 0\n"
  "ds_read_b128 v[240:243], v244 offset:256\n"
  "s_branch .Lfl_mid\n"
  ".p2align 6\n"
  ".Lfl_top:\n"
  "s_barrier\n"
  "v_add_u32 v244, s41, v244\n"
  "ds_read_b128 v[224:227], v244 offset:0\n"
  "s_waitcnt lgkmcnt(5)\n"
  "v_mfma_f32_16x16x32_bf16 v[96:99], v[228:231], v[192:195], v[96:99]\n"
  "v_max_f32 v248, v248, v249\n"
  "v_cmp_lt_f32 vcc, s42, v248\n"
  "v_mfma_f32_16x16x32_bf16 v[100:103], v[228:231], v[196:199], v[100:103]\n"
  "ds_read_b128 v[228:231], v244 offset:64\n"
  "s_cbranch_vccnz .Lrare_lp_1_1\n"
  ".Lback_lp_1_1:\n"
  "v_mfma_f32_16x16x32_bf16 v[104:107], v[232:235], v[192:195], v[104:107]\n"
  "v_exp_f32 v208, v208\n"
  "v_mfma_f32_16x16x32_bf16 v[108:111], v[232:235], v[196:199], v[108:111]\n"
  "ds_read_b128 v[232:235], v244 offset:128\n"
  "s_waitcnt lgkmcnt(3)\n"
  "v_mfma_f32_16x16x32_bf16 v[112:115], v[236:239], v[192:195], v[112:115]\n"
  "v_exp_f32 v212, v212\n"
  "v_mfma_f32_16x16x32_bf16 v[116:119], v[236:239], v[196:199], v[116:119]\n"
  "ds_read_b128 v[236:239], v244 offset:192\n"
  "v_mfma_f32_16x16x32_bf16 v[120:123], v[240:243], v[192:195], v[120:123]\n"
  "v_exp_f32 v209, v209\n"
  "v_mfma_f32_16x16x32_bf16 v[124:127], v[240:243], v[196:199], v[124:127]\n"
  "ds_read_b128 v[240:243], v244 offset:256\n"
  ".Lfl_mid:\n"
  "s_waitcnt lgkmcnt(3)\n"
  "v_mfma_f32_16x16x32_bf16 v[192:195], v[224:227], v[128:131], v[216:219]\n"
  "ds_read_b128 v[252:255], v244 offset:320\n"
  "v_add_f32 v246, v246, v208\n"
  "v_mfma_f32_16x16x32_bf16 v[192:195], v[228:231], v[136:139], v[192:195]\n"
  "v_exp_f32 v213, v213\n"
  "v_mfma_f32_16x16x32_bf16 v[196:199], v[224:227], v[132:135], v[220:223]\n"
  "v_add_f32 v247, v247, v212\n"
  "v_exp_f32 v210, v210\n"
  "v_mfma_f32_16x16x32_bf16 v[196:199], v[228:231], v[140:143], v[196:199]\n"
  "ds_read_b128 v[224:227], v244 offset:384\n"
  "s_waitcnt lgkmcnt(3)\n"
  "v_mfma_f32_16x16x32_bf16 v[196:199], v[232:235], v[148:151], v[196:199]\n"
  "ds_read_b128 v[228:231], v244 offset:448\n"
  "v_add_f32 v246, v246, v209\n"
  "v_mfma_f32_16x16x32_bf16 v[196:199], v[236:239], v[156:159], v[196:199]\n"
  "v_exp_f32 v214, v214\n"
  "v_mfma_f32_16x16x32_bf16 v[192:195], v[232:235], v[144:147], v[192:195]\n"
  "v_add_f32 v247, v247, v213\n"
  "v_cvt_pk_bf16_f32 v202, v208, v209\n"
  "v_mfma_f32_16x16x32_bf16 v[192:195], v[236:239], v[152:155], v[192:195]\n"
  "ds_read_b128 v[232:235], v244 offset:8448\n"
  "s_waitcnt lgkmcnt(3)\n"
  "v_mfma_f32_16x16x32_bf16 v[192:195], v[240:243], v[160:163], v[192:195]\n"
  "ds_read_b128 v[236:239], v244 offset:8512\n"
  "v_exp_f32 v211, v211\n"
  "v_mfma_f32_16x16x32_bf16 v[192:195], v[252:255], v[168:171], v[192:195]\n"
  "v_cvt_pk_bf16_f32 v206, v212, v213\n"
  "v_mfma_f32_16x16x32_bf16 v[196:199], v[240:243], v[164:167], v[196:199]\n"
  "v_exp_f32 v215, v215\n"
  "v_mfma_f32_16x16x32_bf16 v[196:199], v[252:255], v[172:175], v[196:199]\n"
  "ds_read_b128 v[240:243], v244 offset:8576\n"
  "s_waitcnt lgkmcnt(3)\n"
  "v_mfma_f32_16x16x32_bf16 v[196:199], v[224:227], v[180:183], v[196:199]\n"
  "ds_read_b128 v[252:255], v244 offset:8640\n"
  "v_mfma_f32_16x16x32_bf16 v[196:199], v[228:231], v[188:191], v[196:199]\n"
  "v_add_f32 v250, v210, v211\n"
  "v_cvt_pk_bf16_f32 v203, v210, v211\n"
  "v_mfma_f32_16x16x32_bf16 v[192:195], v[224:227], v[176:179], v[192:195]\n"
  "v_add_f32 v251, v214, v215\n"
  "v_cvt_pk_bf16_f32 v207, v214, v215\n"
  "v_mfma_f32_16x16x32_bf16 v[192:195], v[228:231], v[184:187], v[192:195]\n"
  "ds_read_b128 v[224:227], v244 offset:8704\n"
  "s_waitcnt lgkmcnt(3)\n"
  "v_mfma_f32_16x16x32_bf16 v[208:211], v[232:235], v[128:131], v[216:219]\n"
  "ds_read_b128 v[228:231], v244 offset:8768\n"
  "v_mfma_f32_16x16x32_bf16 v[208:211], v[236:239], v[136:139], v[208:211]\n"
  "v_add_f32 v246, v246, v250\n"
  "v_add_f32 v247, v247, v251\n"
  "v_mfma_f32_16x16x32_bf16 v[212:215], v[232:235], v[132:135], v[220:223]\n"
  "v_max3_f32 v248, v192, v193, v194\n"
  "v_mfma_f32_16x16x32_bf16 v[212:215], v[236:239], v[140:143], v[212:215]\n"
  "ds_read_b128 v[232:235], v244 offset:8832\n"
  "s_waitcnt lgkmcnt(3)\n"
  "v_mfma_f32_16x16x32_bf16 v[212:215], v[240:243], v[148:151], v[212:215]\n"
  "ds_read_b128 v[236:239], v244 offset:8896\n"
  "v_mfma_f32_16x16x32_bf16 v[212:215], v[252:255], v[156:159], v[212:215]\n"
  "v_max3_f32 v249, v196, v197, v198\n"
  "v_max3_f32 v248, v248, v195, v199\n"
  "v_mfma_f32_16x16x32_bf16 v[208:211], v[240:243], v[144:147], v[208:211]\n"
  "v_max_f32 v248, v248, v249\n"
  "v_cmp_lt_f32 vcc, s42, v248\n"
  "v_mfma_f32_16x16x32_bf16 v[208:211], v[252:255], v[152:155], v[208:211]\n"
  "v_add_u32 v245, s41, v245\n"
  "ds_read_b64_tr_b16 v[240:241], v245 offset:17376\n"
  "ds_read_b64_tr_b16 v[242:243], v245 offset:25824\n"
  "s_waitcnt lgkmcnt(4)\n"
  "v_mfma_f32_16x16x32_bf16 v[208:211], v[224:227], v[160:163], v[208:211]\n"
  "ds_read_b64_tr_b16 v[252:253], v245 offset:17408\n"
  "s_cbranch_vccnz .Lrare_lp_0_0\n"
  ".Lback_lp_0_0:\n"
  "v_mfma_f32_16x16x32_bf16 v[208:211], v[228:231], v[168:171], v[208:211]\n"
  "ds_read_b64_tr_b16 v[254:255], v245 offset:25856\n"
  "v_exp_f32 v192, v192\n"
  "v_mfma_f32_16x16x32_bf16 v[212:215], v[224:227], v[164:167], v[212:215]\n"
  "v_exp_f32 v196, v196\n"
  "v_mfma_f32_16x16x32_bf16 v[212:215], v[228:231], v[172:175], v[212:215]\n"
  "ds_read_b64_tr_b16 v[224:225], v245 offset:17440\n"
  "ds_read_b64_tr_b16 v[226:227], v245 offset:25888\n"
  "s_waitcnt lgkmcnt(6)\n"
  "v_mfma_f32_16x16x32_bf16 v[212:215], v[232:235], v[180:183], v[212:215]\n"
  "ds_read_b64_tr_b16 v[228:229], v245 offset:17472\n"
  "v_exp_f32 v193, v193\n"
  "v_mfma_f32_16x16x32_bf16 v[212:215], v[236:239], v[188:191], v[212:215]\n"
  "ds_read_b64_tr_b16 v[230:231], v245 offset:25920\n"
  "v_add_f32 v246, v246, v192\n"
  "v_mfma_f32_16x16x32_bf16 v[208:211], v[232:235], v[176:179], v[208:211]\n"
  "v_exp_f32 v197, v197\n"
  "v_mfma_f32_16x16x32_bf16 v[208:211], v[236:239], v[184:187], v[208:211]\n"
  "ds_read_b64_tr_b16 v[232:233], v245 offset:17504\n"
  "ds_read_b64_tr_b16 v[234:235], v245 offset:25952\n"
  "s_waitcnt lgkmcnt(6)\n"
  "v_mfma_f32_16x16x32_bf16 v[0:3], v[240:243], v[200:203], v[0:3]\n"
  "ds_read_b64_tr_b16 v[236:237], v245 offset:17536\n"
  "v_mfma_f32_16x16x32_bf16 v[4:7], v[240:243], v[204:207], v[4:7]\n"
  "ds_read_b64_tr_b16 v[238:239], v245 offset:25984\n"
  "v_mfma_f32_16x16x32_bf16 v[8:11], v[252:255], v[200:203], v[8:11]\n"
  "ds_read_b64_tr_b16 v[240:241], v245 offset:17568\n"
  "v_add_f32 v247, v247, v196\n"
  "v_mfma_f32_16x16x32_bf16 v[12:15], v[252:255], v[204:207], v[12:15]\n"
  "ds_read_b64_tr_b16 v[242:243], v245 offset:26016\n"
  "s_waitcnt lgkmcnt(6)\n"
  "v_mfma_f32_16x16x32_bf16 v[16:19], v[224:227], v[200:203], v[16:19]\n"
  "ds_read_b64_tr_b16 v[252:253], v245 offset:17600\n"
  "v_exp_f32 v194, v194\n"
  "v_mfma_f32_16x16x32_bf16 v[20:23], v[224:227], v[204:207], v[20:23]\n"
  "ds_read_b64_tr_b16 v[254:255], v245 offset:26048\n"
  "v_add_f32 v246, v246, v193\n"
  "v_mfma_f32_16x16x32_bf16 v[24:27], v[228:231], v[200:203], v[24:27]\n"
  "ds_read_b64_tr_b16 v[224:225], v245 offset:17632\n"
  "v_exp_f32 v198, v198\n"
  "v_mfma_f32_16x16x32_bf16 v[28:31], v[228:231], v[204:207], v[28:31]\n"
  "ds_read_b64_tr_b16 v[226:227], v245 offset:26080\n"
  "s_waitcnt lgkmcnt(6)\n"
  "v_mfma_f32_16x16x32_bf16 v[32:35], v[232:235], v[200:203], v[32:35]\n"
  "ds_read_b64_tr_b16 v[228:229], v245 offset:17664\n"
  "v_add_f32 v247, v247, v197\n"
  "v_mfma_f32_16x16x32_bf16 v[36:39], v[232:235], v[204:207], v[36:39]\n"
  "ds_read_b64_tr_b16 v[230:231], v245 offset:26112\n"
  "v_cvt_pk_bf16_f32 v192, v192, v193\n"
  "v_mfma_f32_16x16x32_bf16 v[40:43], v[236:239], v[200:203], v[40:43]\n"
  "ds_read_b64_tr_b16 v[232:233], v245 offset:17696\n"
  "v_exp_f32 v195, v195\n"
  "v_mfma_f32_16x16x32_bf16 v[44:47], v[236:239], v[204:207], v[44:47]\n"
  "ds_read_b64_tr_b16 v[234:235], v245 offset:26144\n"
  "s_waitcnt lgkmcnt(6)\n"
  "v_mfma_f32_16x16x32_bf16 v[48:51], v[240:243], v[200:203], v[48:51]\n"
  "ds_read_b64_tr_b16 v[236:237], v245 offset:17728\n"
  "v_cvt_pk_bf16_f32 v196, v196, v197\n"
  "v_mfma_f32_16x16x32_bf16 v[52:55], v[240:243], v[204:207], v[52:55]\n"
  "ds_read_b64_tr_b16 v[238:239], v245 offset:26176\n"
  "v_exp_f32 v199, v199\n"
  "v_mfma_f32_16x16x32_bf16 v[56:59], v[252:255], v[200:203], v[56:59]\n"
  "ds_read_b64_tr_b16 v[240:241], v245 offset:17760\n"
  "v_add_f32 v246, v246, v194\n"
  "v_mfma_f32_16x16x32_bf16 v[60:63], v[252:255], v[204:207], v[60:63]\n"
  "ds_read_b64_tr_b16 v[242:243], v245 offset:26208\n"
  "s_waitcnt lgkmcnt(6)\n"
  "v_mfma_f32_16x16x32_bf16 v[64:67], v[224:227], v[200:203], v[64:67]\n"
  "ds_read_b64_tr_b16 v[252:253], v245 offset:17792\n"
  "v_add_f32 v247, v247, v198\n"
  "v_mfma_f32_16x16x32_bf16 v[68:71], v[224:227], v[204:207], v[68:71]\n"
  "ds_read_b64_tr_b16 v[254:255], v245 offset:26240\n"
  "v_add_f32 v246, v246, v195\n"
  "v_mfma_f32_16x16x32_bf16 v[72:75], v[228:231], v[200:203], v[72:75]\n"
  "ds_read_b64_tr_b16 v[224:225], v245 offset:17824\n"
  "v_add_f32 v247, v247, v199\n"
  "v_mfma_f32_16x16x32_bf16 v[76:79], v[228:231], v[204:207], v[76:79]\n"
  "ds_read_b64_tr_b16 v[226:227], v245 offset:26272\n"
  "s_waitcnt lgkmcnt(6)\n"
  "v_mfma_f32_16x16x32_bf16 v[80:83], v[232:235], v[200:203], v[80:83]\n"
  "ds_read_b64_tr_b16 v[228:229], v245 offset:17856\n"
  "v_cvt_pk_bf16_f32 v193, v194, v195\n"
  "v_mfma_f32_16x16x32_bf16 v[84:87], v[232:235], v[204:207], v[84:87]\n"
  "ds_read_b64_tr_b16 v[230:231], v245 offset:26304\n"
  "v_cvt_pk_bf16_f32 v197, v198, v199\n"
  "v_mfma_f32_16x16x32_bf16 v[88:91], v[236:239], v[200:203], v[88:91]\n"
  "s_barrier\n"
  "ds_read_b128 v[232:235], v244 offset:34752\n"
  "v_mfma_f32_16x16x32_bf16 v[92:95], v[236:239], v[204:207], v[92:95]\n"
  "ds_read_b128 v[236:239], v244 offset:34816\n"
  "s_waitcnt lgkmcnt(6)\n"
  "v_mfma_f32_16x16x32_bf16 v[96:99], v[240:243], v[200:203], v[96:99]\n"
  "v_max3_f32 v248, v208, v209, v210\n"
  "v_max3_f32 v249, v212, v213, v214\n"
  "v_mfma_f32_16x16x32_bf16 v[100:103], v[240:243], v[204:207], v[100:103]\n"
  "ds_read_b128 v[240:243], v244 offset:34880\n"
  "v_max3_f32 v248, v248, v211, v215\n"
  "v_mfma_f32_16x16x32_bf16 v[104:107], v[252:255], v[200:203], v[104:107]\n"
  "v_max_f32 v248, v248, v249\n"
  "v_cmp_lt_f32 vcc, s42, v248\n"
  "v_mfma_f32_16x16x32_bf16 v[108:111], v[252:255], v[204:207], v[108:111]\n"
  "s_cbranch_vccnz .Lrare_lp_0_1\n"
  ".Lback_lp_0_1:\n"
  "s_waitcnt lgkmcnt(3)\n"
  "v_mfma_f32_16x16x32_bf16 v[112:115], v[224:227], v[200:203], v[112:115]\n"
  "v_exp_f32 v208, v208\n"
  "v_mfma_f32_16x16x32_bf16 v[116:119], v[224:227], v[204:207], v[116:119]\n"
  "ds_read_b128 v[224:227], v244 offset:34944\n"
  "v_mfma_f32_16x16x32_bf16 v[120:123], v[228:231], v[200:203], v[120:123]\n"
  "v_exp_f32 v212, v212\n"
  "v_mfma_f32_16x16x32_bf16 v[124:127], v[228:231], v[204:207], v[124:127]\n"
  "ds_read_b128 v[228:231], v244 offset:35008\n"
  "s_waitcnt lgkmcnt(3)\n"
  "v_mfma_f32_16x16x32_bf16 v[200:203], v[232:235], v[128:131], v[216:219]\n"
  "v_exp_f32 v209, v209\n"
  "v_mfma_f32_16x16x32_bf16 v[200:203], v[236:239], v[136:139], v[200:203]\n"
  "v_add_f32 v246, v246, v208\n"
  "v_exp_f32 v213, v213\n"
  "v_mfma_f32_16x16x32_bf16 v[204:207], v[232:235], v[132:135], v[220:223]\n"
  "v_add_f32 v247, v247, v212\n"
  "v_exp_f32 v210, v210\n"
  "v_mfma_f32_16x16x32_bf16 v[204:207], v[236:239], v[140:143], v[204:207]\n"
  "ds_read_b128 v[232:235], v244 offset:35072\n"
  "s_waitcnt lgkmcnt(2)\n"
  "v_mfma_f32_16x16x32_bf16 v[204:207], v[240:243], v[148:151], v[204:207]\n"
  "ds_read_b128 v[236:239], v244 offset:35136\n"
  "v_add_f32 v246, v246, v209\n"
  "v_mfma_f32_16x16x32_bf16 v[204:207], v[224:227], v[156:159], v[204:207]\n"
  "v_exp_f32 v214, v214\n"
  "v_mfma_f32_16x16x32_bf16 v[200:203], v[240:243], v[144:147], v[200:203]\n"
  "v_add_f32 v247, v247, v213\n"
  "v_cvt_pk_bf16_f32 v194, v208, v209\n"
  "v_mfma_f32_16x16x32_bf16 v[200:203], v[224:227], v[152:155], v[200:203]\n"
  "ds_read_b128 v[240:243], v244 offset:35200\n"
  "s_waitcnt lgkmcnt(2)\n"
  "v_mfma_f32_16x16x32_bf16 v[200:203], v[228:231], v[160:163], v[200:203]\n"
  "ds_read_b128 v[252:255], v244 offset:43200\n"
  "v_exp_f32 v211, v211\n"
  "v_mfma_f32_16x16x32_bf16 v[200:203], v[232:235], v[168:171], v[200:203]\n"
  "ds_read_b128 v[224:227], v244 offset:43264\n"
  "v_cvt_pk_bf16_f32 v198, v212, v213\n"
  "v_mfma_f32_16x16x32_bf16 v[204:207], v[228:231], v[164:167], v[204:207]\n"
  "v_exp_f32 v215, v215\n"
  "v_mfma_f32_16x16x32_bf16 v[204:207], v[232:235], v[172:175], v[204:207]\n"
  "ds_read_b128 v[228:231], v244 offset:43328\n"
  "s_waitcnt lgkmcnt(3)\n"
  "v_mfma_f32_16x16x32_bf16 v[204:207], v[236:239], v[180:183], v[204:207]\n"
  "ds_read_b128 v[232:235], v244 offset:43392\n"
  "v_add_f32 v250, v210, v211\n"
  "v_mfma_f32_16x16x32_bf16 v[204:207], v[240:243], v[188:191], v[204:207]\n"
  "v_cvt_pk_bf16_f32 v195, v210, v211\n"
  "v_add_f32 v251, v214, v215\n"
  "v_mfma_f32_16x16x32_bf16 v[200:203], v[236:239], v[176:179], v[200:203]\n"
  "v_cvt_pk_bf16_f32 v199, v214, v215\n"
  "v_add_f32 v246, v246, v250\n"
  "v_mfma_f32_16x16x32_bf16 v[200:203], v[240:243], v[184:187], v[200:203]\n"
  "ds_read_b128 v[236:239], v244 offset:43456\n"
  "s_waitcnt lgkmcnt(3)\n"
  "v_mfma_f32_16x16x32_bf16 v[208:211], v[252:255], v[128:131], v[216:219]\n"
  "ds_read_b128 v[240:243], v244 offset:43520\n"
  "s_mul_i32 s41, s41, -1\n"
  "v_mfma_f32_16x16x32_bf16 v[208:211], v[224:227], v[136:139], v[208:211]\n"
  "v_add_f32 v247, v247, v251\n"
  "s_sub_u32 s40, s40, 1\n"
  "v_mfma_f32_16x16x32_bf16 v[212:215], v[252:255], v[132:135], v[220:223]\n"
  "v_max3_f32 v248, v200, v201, v202\n"
  "v_mfma_f32_16x16x32_bf16 v[212:215], v[224:227], v[140:143], v[212:215]\n"
  "ds_read_b128 v[252:255], v244 offset:43584\n"
  "s_waitcnt lgkmcnt(3)\n"
  "v_mfma_f32_16x16x32_bf16 v[212:215], v[228:231], v[148:151], v[212:215]\n"
  "ds_read_b128 v[224:227], v244 offset:43648\n"
  "v_mfma_f32_16x16x32_bf16 v[212:215], v[232:235], v[156:159], v[212:215]\n"
  "v_max3_f32 v249, v204, v205, v206\n"
  "v_max3_f32 v248, v248, v203, v207\n"
  "v_mfma_f32_16x16x32_bf16 v[208:211], v[228:231], v[144:147], v[208:211]\n"
  "v_max_f32 v248, v248, v249\n"
  "v_cmp_lt_f32 vcc, s42, v248\n"
  "v_mfma_f32_16x16x32_bf16 v[208:211], v[232:235], v[152:155], v[208:211]\n"
  "ds_read_b64_tr_b16 v[228:229], v245 offset:52128\n"
  "ds_read_b64_tr_b16 v[230:231], v245 offset:60576\n"
  "s_waitcnt lgkmcnt(4)\n"
  "v_mfma_f32_16x16x32_bf16 v[208:211], v[236:239], v[160:163], v[208:211]\n"
  "ds_read_b64_tr_b16 v[232:233], v245 offset:52160\n"
  "s_cbranch_vccnz .Lrare_lp_1_0\n"
  ".Lback_lp_1_0:\n"
  "v_mfma_f32_16x16x32_bf16 v[208:211], v[240:243], v[168:171], v[208:211]\n"
  "ds_read_b64_tr_b16 v[234:235], v245 offset:60608\n"
  "v_exp_f32 v200, v200\n"
  "v_mfma_f32_16x16x32_bf16 v[212:215], v[236:239], v[164:167], v[212:215]\n"
  "v_exp_f32 v204, v204\n"
  "v_mfma_f32_16x16x32_bf16 v[212:215], v[240:243], v[172:175], v[212:215]\n"
  "ds_read_b64_tr_b16 v[236:237], v245 offset:52192\n"
  "ds_read_b64_tr_b16 v[238:239], v245 offset:60640\n"
  "s_waitcnt lgkmcnt(6)\n"
  "v_mfma_f32_16x16x32_bf16 v[212:215], v[252:255], v[180:183], v[212:215]\n"
  "ds_read_b64_tr_b16 v[240:241], v245 offset:52224\n"
  "v_exp_f32 v201, v201\n"
  "v_mfma_f32_16x16x32_bf16 v[212:215], v[224:227], v[188:191], v[212:215]\n"
  "ds_read_b64_tr_b16 v[242:243], v245 offset:60672\n"
  "v_add_f32 v246, v246, v200\n"
  "v_mfma_f32_16x16x32_bf16 v[208:211], v[252:255], v[176:179], v[208:211]\n"
  "v_exp_f32 v205, v205\n"
  "v_mfma_f32_16x16x32_bf16 v[208:211], v[224:227], v[184:187], v[208:211]\n"
  "ds_read_b64_tr_b16 v[252:253], v245 offset:52256\n"
  "ds_read_b64_tr_b16 v[254:255], v245 offset:60704\n"
  "s_waitcnt lgkmcnt(6)\n"
  "v_mfma_f32_16x16x32_bf16 v[0:3], v[228:231], v[192:195], v[0:3]\n"
  "ds_read_b64_tr_b16 v[224:225], v245 offset:52288\n"
  "v_mfma_f32_16x16x32_bf16 v[4:7], v[228:231], v[196:199], v[4:7]\n"
  "ds_read_b64_tr_b16 v[226:227], v245 offset:60736\n"
  "v_add_f32 v247, v247, v204\n"
  "v_mfma_f32_16x16x32_bf16 v[8:11], v[232:235], v[192:195], v[8:11]\n"
  "ds_read_b64_tr_b16 v[228:229], v245 offset:52320\n"
  "v_exp_f32 v202, v202\n"
  "v_mfma_f32_16x16x32_bf16 v[12:15], v[232:235], v[196:199], v[12:15]\n"
  "ds_read_b64_tr_b16 v[230:231], v245 offset:60768\n"
  "s_waitcnt lgkmcnt(6)\n"
  "v_mfma_f32_16x16x32_bf16 v[16:19], v[236:239], v[192:195], v[16:19]\n"
  "ds_read_b64_tr_b16 v[232:233], v245 offset:52352\n"
  "v_add_f32 v246, v246, v201\n"
  "v_mfma_f32_16x16x32_bf16 v[20:23], v[236:239], v[196:199], v[20:23]\n"
  "ds_read_b64_tr_b16 v[234:235], v245 offset:60800\n"
  "v_exp_f32 v206, v206\n"
  "v_mfma_f32_16x16x32_bf16 v[24:27], v[240:243], v[192:195], v[24:27]\n"
  "ds_read_b64_tr_b16 v[236:237], v245 offset:52384\n"
  "v_add_f32 v247, v247, v205\n"
  "v_mfma_f32_16x16x32_bf16 v[28:31], v[240:243], v[196:199], v[28:31]\n"
  "ds_read_b64_tr_b16 v[238:239], v245 offset:60832\n"
  "s_waitcnt lgkmcnt(6)\n"
  "v_mfma_f32_16x16x32_bf16 v[32:35], v[252:255], v[192:195], v[32:35]\n"
  "ds_read_b64_tr_b16 v[240:241], v245 offset:52416\n"
  "v_cvt_pk_bf16_f32 v200, v200, v201\n"
  "v_mfma_f32_16x16x32_bf16 v[36:39], v[252:255], v[196:199], v[36:39]\n"
  "ds_read_b64_tr_b16 v[242:243], v245 offset:60864\n"
  "v_exp_f32 v203, v203\n"
  "v_mfma_f32_16x16x32_bf16 v[40:43], v[224:227], v[192:195], v[40:43]\n"
  "ds_read_b64_tr_b16 v[252:253], v245 offset:52448\n"
  "v_cvt_pk_bf16_f32 v204, v204, v205\n"
  "v_mfma_f32_16x16x32_bf16 v[44:47], v[224:227], v[196:199], v[44:47]\n"
  "ds_read_b64_tr_b16 v[254:255], v245 offset:60896\n"
  "s_waitcnt lgkmcnt(6)\n"
  "v_mfma_f32_16x16x32_bf16 v[48:51], v[228:231], v[192:195], v[48:51]\n"
  "ds_read_b64_tr_b16 v[224:225], v245 offset:52480\n"
  "v_exp_f32 v207, v207\n"
  "v_mfma_f32_16x16x32_bf16 v[52:55], v[228:231], v[196:199], v[52:55]\n"
  "ds_read_b64_tr_b16 v[226:227], v245 offset:60928\n"
  "v_add_f32 v246, v246, v202\n"
  "v_mfma_f32_16x16x32_bf16 v[56:59], v[232:235], v[192:195], v[56:59]\n"
  "ds_read_b64_tr_b16 v[228:229], v245 offset:52512\n"
  "v_add_f32 v247, v247, v206\n"
  "v_mfma_f32_16x16x32_bf16 v[60:63], v[232:235], v[196:199], v[60:63]\n"
  "ds_read_b64_tr_b16 v[230:231], v245 offset:60960\n"
  "s_waitcnt lgkmcnt(6)\n"
  "v_mfma_f32_16x16x32_bf16 v[64:67], v[236:239], v[192:195], v[64:67]\n"
  "ds_read_b64_tr_b16 v[232:233], v245 offset:52544\n"
  "v_add_f32 v246, v246, v203\n"
  "v_mfma_f32_16x16x32_bf16 v[68:71], v[236:239], v[196:199], v[68:71]\n"
  "ds_read_b64_tr_b16 v[234:235], v245 offset:60992\n"
  "v_add_f32 v247, v247, v207\n"
  "v_mfma_f32_16x16x32_bf16 v[72:75], v[240:243], v[192:195], v[72:75]\n"
  "ds_read_b64_tr_b16 v[236:237], v245 offset:52576\n"
  "v_cvt_pk_bf16_f32 v201, v202, v203\n"
  "v_mfma_f32_16x16x32_bf16 v[76:79], v[240:243], v[196:199], v[76:79]\n"
  "ds_read_b64_tr_b16 v[238:239], v245 offset:61024\n"
  "s_waitcnt lgkmcnt(6)\n"
  "v_mfma_f32_16x16x32_bf16 v[80:83], v[252:255], v[192:195], v[80:83]\n"
  "ds_read_b64_tr_b16 v[240:241], v245 offset:52608\n"
  "v_cvt_pk_bf16_f32 v205, v206, v207\n"
  "v_mfma_f32_16x16x32_bf16 v[84:87], v[252:255], v[196:199], v[84:87]\n"
  "ds_read_b64_tr_b16 v[242:243], v245 offset:61056\n"
  "v_max3_f32 v248, v208, v209, v210\n"
  "v_mfma_f32_16x16x32_bf16 v[88:91], v[224:227], v[192:195], v[88:91]\n"
  "v_max3_f32 v249, v212, v213, v214\n"
  "v_max3_f32 v248, v248, v211, v215\n"
  "v_mfma_f32_16x16x32_bf16 v[92:95], v[224:227], v[196:199], v[92:95]\n"
  "s_cbranch_scc0 .Lfl_top\n"
  "s_barrier\n"
  "v_add_u32 v245, s41, v245\n"
  "ds_read_b64_tr_b16 v[224:225], v245 offset:0\n"
  "s_waitcnt lgkmcnt(5)\n"
  "v_mfma_f32_16x16x32_bf16 v[96:99], v[228:231], v[192:195], v[96:99]\n"
  "ds_read_b64_tr_b16 v[226:227], v245 offset:8448\n"
  "v_max_f32 v248, v248, v249\n"
  "v_cmp_lt_f32 vcc, s42, v248\n"
  "v_mfma_f32_16x16x32_bf16 v[100:103], v[228:231], v[196:199], v[100:103]\n"
  "ds_read_b64_tr_b16 v[228:229], v245 offset:32\n"
  "s_cbranch_vccnz .Lrare_ep_1_1\n"
  ".Lback_ep_1_1:\n"
  "v_mfma_f32_16x16x32_bf16 v[104:107], v[232:235], v[192:195], v[104:107]\n"
  "ds_read_b64_tr_b16 v[230:231], v245 offset:8480\n"
  "v_exp_f32 v208, v208\n"
  "v_mfma_f32_16x16x32_bf16 v[108:111], v[232:235], v[196:199], v[108:111]\n"
  "ds_read_b64_tr_b16 v[232:233], v245 offset:64\n"
  "s_waitcnt lgkmcnt(5)\n"
  "v_mfma_f32_16x16x32_bf16 v[112:115], v[236:239], v[192:195], v[112:115]\n"
  "ds_read_b64_tr_b16 v[234:235], v245 offset:8512\n"
  "v_exp_f32 v212, v212\n"
  "v_mfma_f32_16x16x32_bf16 v[116:119], v[236:239], v[196:199], v[116:119]\n"
  "ds_read_b64_tr_b16 v[236:237], v245 offset:96\n"
  "v_mfma_f32_16x16x32_bf16 v[120:123], v[240:243], v[192:195], v[120:123]\n"
  "ds_read_b64_tr_b16 v[238:239], v245 offset:8544\n"
  "v_exp_f32 v209, v209\n"
  "v_mfma_f32_16x16x32_bf16 v[124:127], v[240:243], v[196:199], v[124:127]\n"
  "ds_read_b64_tr_b16 v[240:241], v245 offset:128\n"
  "ds_read_b64_tr_b16 v[242:243], v245 offset:8576\n"
  "v_add_f32 v246, v246, v208\n"
  "v_exp_f32 v213, v213\n"
  "v_add_f32 v247, v247, v212\n"
  "v_exp_f32 v210, v210\n"
  "v_add_f32 v246, v246, v209\n"
  "v_exp_f32 v214, v214\n"
  "v_add_f32 v247, v247, v213\n"
  "v_cvt_pk_bf16_f32 v202, v208, v209\n"
  "v_exp_f32 v211, v211\n"
  "v_cvt_pk_bf16_f32 v206, v212, v213\n"
  "v_exp_f32 v215, v215\n"
  "v_add_f32 v250, v210, v211\n"
  "v_cvt_pk_bf16_f32 v203, v210, v211\n"
  "v_add_f32 v251, v214, v215\n"
  "v_cvt_pk_bf16_f32 v207, v214, v215\n"
  "v_add_f32 v246, v246, v250\n"
  "v_add_f32 v247, v247, v251\n"
  "s_waitcnt lgkmcnt(6)\n"
  "v_mfma_f32_16x16x32_bf16 v[0:3], v[224:227], v[200:203], v[0:3]\n"
  "ds_read_b64_tr_b16 v[252:253], v245 offset:160\n"
  "v_mbcnt_lo_u32_b32 v249, -1, 0\n"
  "v_mbcnt_hi_u32_b32 v249, -1, v249\n"
  "s_cmp_ge_u32 %6, 2\n"
  "v_mfma_f32_16x16x32_bf16 v[4:7], v[224:227], v[204:207], v[4:7]\n"
  "ds_read_b64_tr_b16 v[254:255], v245 offset:8608\n"
  "s_mov_b32 s88, 139072\n"
  "s_cselect_b32 s88, 86880, s88\n"
  "s_and_b32 s45, %6, 1\n"
  "v_mfma_f32_16x16x32_bf16 v[8:11], v[228:231], v[200:203], v[8:11]\n"
  "ds_read_b64_tr_b16 v[224:225], v245 offset:192\n"
  "s_lshl_b32 s89, s45, 13\n"
  "v_lshl_add_u32 v251, v249, 4, s88\n"
  "v_add_u32 v248, s89, v251\n"
  "v_mfma_f32_16x16x32_bf16 v[12:15], v[228:231], v[204:207], v[12:15]\n"
  "ds_read_b64_tr_b16 v[226:227], v245 offset:8640\n"
  "v_bfe_u32 v244, v249, 2, 2\n"
  "v_mul_u32_u24 v244, 1088, v244\n"
  "v_bfe_u32 v250, v249, 4, 1\n"
  "s_waitcnt lgkmcnt(6)\n"
  "v_mfma_f32_16x16x32_bf16 v[16:19], v[232:235], v[200:203], v[16:19]\n"
  "ds_read_b64_tr_b16 v[228:229], v245 offset:224\n"
  "s_lshl_b32 s45, s45, 6\n"
  "v_cvt_pk_bf16_f32 v128, v0, v1\n"
  "v_cvt_pk_bf16_f32 v129, v2, v3\n"
  "v_mfma_f32_16x16x32_bf16 v[20:23], v[232:235], v[204:207], v[20:23]\n"
  "ds_read_b64_tr_b16 v[230:231], v245 offset:8672\n"
  "v_mul_u32_u24 v250, 4360, v250\n"
  "v_cvt_pk_bf16_f32 v132, v4, v5\n"
  "v_cvt_pk_bf16_f32 v133, v6, v7\n"
  "v_mfma_f32_16x16x32_bf16 v[24:27], v[236:239], v[200:203], v[24:27]\n"
  "ds_read_b64_tr_b16 v[232:233], v245 offset:256\n"
  "s_add_u32 s45, s45, 69504\n"
  "v_cvt_pk_bf16_f32 v130, v8, v9\n"
  "v_cvt_pk_bf16_f32 v131, v10, v11\n"
  "v_mfma_f32_16x16x32_bf16 v[28:31], v[236:239], v[204:207], v[28:31]\n"
  "ds_read_b64_tr_b16 v[234:235], v245 offset:8704\n"
  "v_add3_u32 v244, v244, v250, s45\n"
  "v_cvt_pk_bf16_f32 v134, v12, v13\n"
  "v_cvt_pk_bf16_f32 v135, v14, v15\n"
  "ds_write_b128 v248, v[128:131] offset:0\n"
  "s_waitcnt lgkmcnt(7)\n"
  "v_mfma_f32_16x16x32_bf16 v[32:35], v[240:243], v[200:203], v[32:35]\n"
  "ds_read_b64_tr_b16 v[236:237], v245 offset:288\n"
  "v_lshrrev_b32 v250, 5, v249\n"
  "ds_write_b128 v248, v[132:135] offset:1024\n"
  "v_cvt_pk_bf16_f32 v136, v16, v17\n"
  "v_cvt_pk_bf16_f32 v137, v18, v19\n"
  "v_mfma_f32_16x16x32_bf16 v[36:39], v[240:243], v[204:207], v[36:39]\n"
  "ds_read_b64_tr_b16 v[238:239], v245 offset:8736\n"
  "v_lshl_add_u32 v244, v250, 7, v244\n"
  "v_cvt_pk_bf16_f32 v140, v20, v21\n"
  "v_cvt_pk_bf16_f32 v141, v22, v23\n"
  "v_mfma_f32_16x16x32_bf16 v[40:43], v[252:255], v[200:203], v[40:43]\n"
  "ds_read_b64_tr_b16 v[240:241], v245 offset:320\n"
  "v_and_b32 v250, 3, v249\n"
  "v_cvt_pk_bf16_f32 v138, v24, v25\n"
  "v_cvt_pk_bf16_f32 v139, v26, v27\n"
  "v_mfma_f32_16x16x32_bf16 v[44:47], v[252:255], v[204:207], v[44:47]\n"
  "ds_read_b64_tr_b16 v[242:243], v245 offset:8768\n"
  "v_lshl_add_u32 v244, v250, 4, v244\n"
  "v_cvt_pk_bf16_f32 v142, v28, v29\n"
  "v_cvt_pk_bf16_f32 v143, v30, v31\n"
  "ds_write_b128 v248, v[136:139] offset:2048\n"
  "s_waitcnt lgkmcnt(9)\n"
  "v_mfma_f32_16x16x32_bf16 v[48:51], v[224:227], v[200:203], v[48:51]\n"
  "ds_read_b64_tr_b16 v[252:253], v245 offset:352\n"
  "ds_write_b128 v248, v[140:143] offset:3072\n"
  "v_cvt_pk_bf16_f32 v144, v32, v33\n"
  "v_cvt_pk_bf16_f32 v145, v34, v35\n"
  "v_mfma_f32_16x16x32_bf16 v[52:55], v[224:227], v[204:207], v[52:55]\n"
  "ds_read_b64_tr_b16 v[254:255], v245 offset:8800\n"
  "v_cvt_pk_bf16_f32 v148, v36, v37\n"
  "v_cvt_pk_bf16_f32 v149, v38, v39\n"
  "v_mfma_f32_16x16x32_bf16 v[56:59], v[228:231], v[200:203], v[56:59]\n"
  "ds_read_b64_tr_b16 v[224:225], v245 offset:384\n"
  "v_cvt_pk_bf16_f32 v146, v40, v41\n"
  "v_cvt_pk_bf16_f32 v147, v42, v43\n"
  "v_mfma_f32_16x16x32_bf16 v[60:63], v[228:231], v[204:207], v[60:63]\n"
  "ds_read_b64_tr_b16 v[226:227], v245 offset:8832\n"
  "v_cvt_pk_bf16_f32 v150, v44, v45\n"
  "v_cvt_pk_bf16_f32 v151, v46, v47\n"
  "ds_write_b128 v248, v[144:147] offset:4096\n"
  "s_waitcnt lgkmcnt(9)\n"
  "v_mfma_f32_16x16x32_bf16 v[64:67], v[232:235], v[200:203], v[64:67]\n"
  "ds_read_b64_tr_b16 v[228:229], v245 offset:416\n"
  "ds_write_b128 v248, v[148:151] offset:5120\n"
  "v_cvt_pk_bf16_f32 v152, v48, v49\n"
  "v_cvt_pk_bf16_f32 v153, v50, v51\n"
  "v_mfma_f32_16x16x32_bf16 v[68:71], v[232:235], v[204:207], v[68:71]\n"
  "ds_read_b64_tr_b16 v[230:231], v245 offset:8864\n"
  "v_cvt_pk_bf16_f32 v156, v52, v53\n"
  "v_cvt_pk_bf16_f32 v157, v54, v55\n"
  "v_mfma_f32_16x16x32_bf16 v[72:75], v[236:239], v[200:203], v[72:75]\n"
  "ds_read_b64_tr_b16 v[232:233], v245 offset:448\n"
  "v_cvt_pk_bf16_f32 v154, v56, v57\n"
  "v_cvt_pk_bf16_f32 v155, v58, v59\n"
  "v_mfma_f32_16x16x32_bf16 v[76:79], v[236:239], v[204:207], v[76:79]\n"
  "ds_read_b64_tr_b16 v[234:235], v245 offset:8896\n"
  "v_cvt_pk_bf16_f32 v158, v60, v61\n"
  "v_cvt_pk_bf16_f32 v159, v62, v63\n"
  "ds_write_b128 v248, v[152:155] offset:6144\n"
  "s_waitcnt lgkmcnt(9)\n"
  "v_mfma_f32_16x16x32_bf16 v[80:83], v[240:243], v[200:203], v[80:83]\n"
  "ds_read_b64_tr_b16 v[236:237], v245 offset:480\n"
  "ds_write_b128 v248, v[156:159] offset:7168\n"
  "v_cvt_pk_bf16_f32 v160, v64, v65\n"
  "v_cvt_pk_bf16_f32 v161, v66, v67\n"
  "v_mfma_f32_16x16x32_bf16 v[84:87], v[240:243], v[204:207], v[84:87]\n"
  "ds_read_b64_tr_b16 v[238:239], v245 offset:8928\n"
  "v_cvt_pk_bf16_f32 v164, v68, v69\n"
  "v_cvt_pk_bf16_f32 v165, v70, v71\n"
  "v_mfma_f32_16x16x32_bf16 v[88:91], v[252:255], v[200:203], v[88:91]\n"
  "v_cvt_pk_bf16_f32 v162, v72, v73\n"
  "v_cvt_pk_bf16_f32 v163, v74, v75\n"
  "v_mfma_f32_16x16x32_bf16 v[92:95], v[252:255], v[204:207], v[92:95]\n"
  "v_cvt_pk_bf16_f32 v166, v76, v77\n"
  "v_cvt_pk_bf16_f32 v167, v78, v79\n"
  "s_waitcnt lgkmcnt(6)\n"
  "v_mfma_f32_16x16x32_bf16 v[96:99], v[224:227], v[200:203], v[96:99]\n"
  "v_cvt_pk_bf16_f32 v168, v80, v81\n"
  "v_cvt_pk_bf16_f32 v169, v82, v83\n"
  "v_mfma_f32_16x16x32_bf16 v[100:103], v[224:227], v[204:207], v[100:103]\n"
  "s_waitcnt lgkmcnt(1)\n"
  "s_barrier\n"
  "ds_read_b128 v[128:131], v251 offset:0\n"
  "ds_read_b128 v[132:135], v251 offset:1024\n"
  "ds_read_b128 v[136:139], v251 offset:2048\n"
  "ds_read_b128 v[140:143], v251 offset:3072\n"
  "ds_read_b64_tr_b16 v[192:193], v244 offset:34752\n"
  "v_cvt_pk_bf16_f32 v172, v84, v85\n"
  "v_cvt_pk_bf16_f32 v173, v86, v87\n"
  "v_mfma_f32_16x16x32_bf16 v[104:107], v[228:231], v[200:203], v[104:107]\n"
  "ds_read_b64_tr_b16 v[194:195], v244 offset:35008\n"
  "v_cvt_pk_bf16_f32 v170, v88, v89\n"
  "v_cvt_pk_bf16_f32 v171, v90, v91\n"
  "ds_read_b128 v[144:147], v251 offset:4096\n"
  "ds_read_b128 v[148:151], v251 offset:5120\n"
  "v_mfma_f32_16x16x32_bf16 v[108:111], v[228:231], v[204:207], v[108:111]\n"
  "ds_read_b64_tr_b16 v[196:197], v244 offset:35264\n"
  "v_cvt_pk_bf16_f32 v174, v92, v93\n"
  "v_cvt_pk_bf16_f32 v175, v94, v95\n"
  "ds_read_b128 v[152:155], v251 offset:6144\n"
  "ds_read_b128 v[156:159], v251 offset:7168\n"
  "s_waitcnt lgkmcnt(11)\n"
  "v_mfma_f32_16x16x32_bf16 v[112:115], v[232:235], v[200:203], v[112:115]\n"
  "ds_read_b64_tr_b16 v[198:199], v244 offset:35520\n"
  "v_cvt_pk_bf16_f32 v176, v96, v97\n"
  "v_cvt_pk_bf16_f32 v177, v98, v99\n"
  "ds_read_b128 v[0:3], v251 offset:8192\n"
  "ds_read_b128 v[4:7], v251 offset:9216\n"
  "v_mfma_f32_16x16x32_bf16 v[116:119], v[232:235], v[204:207], v[116:119]\n"
  "ds_read_b64_tr_b16 v[208:209], v244 offset:43472\n"
  "v_cvt_pk_bf16_f32 v180, v100, v101\n"
  "v_cvt_pk_bf16_f32 v181, v102, v103\n"
  "s_waitcnt lgkmcnt(11)\n"
  "ds_read_b128 v[8:11], v251 offset:10240\n"
  "ds_read_b128 v[12:15], v251 offset:11264\n"
  "v_mfma_f32_16x16x32_bf16 v[120:123], v[236:239], v[200:203], v[120:123]\n"
  "ds_read_b64_tr_b16 v[210:211], v244 offset:43728\n"
  "v_cvt_pk_bf16_f32 v178, v104, v105\n"
  "v_cvt_pk_bf16_f32 v179, v106, v107\n"
  "ds_read_b128 v[16:19], v251 offset:12288\n"
  "s_waitcnt lgkmcnt(11)\n"
  "ds_read_b128 v[20:23], v251 offset:13312\n"
  "v_mfma_f32_16x16x32_bf16 v[124:127], v[236:239], v[204:207], v[124:127]\n"
  "ds_read_b64_tr_b16 v[212:213], v244 offset:43984\n"
  "ds_read_b64_tr_b16 v[214:215], v244 offset:44240\n"
  "v_cvt_pk_bf16_f32 v182, v108, v109\n"
  "v_cvt_pk_bf16_f32 v183, v110, v111\n"
  "ds_read_b128 v[24:27], v251 offset:14336\n"
  "s_waitcnt lgkmcnt(11)\n"
  "ds_read_b128 v[28:31], v251 offset:15360\n"
  "v_cvt_pk_bf16_f32 v184, v112, v113\n"
  "v_cvt_pk_bf16_f32 v185, v114, v115\n"
  "v_cvt_pk_bf16_f32 v188, v116, v117\n"
  "v_cvt_pk_bf16_f32 v189, v118, v119\n"
  "v_cvt_pk_bf16_f32 v186, v120, v121\n"
  "v_cvt_pk_bf16_f32 v187, v122, v123\n"
  "v_cvt_pk_bf16_f32 v190, v124, v125\n"
  "v_cvt_pk_bf16_f32 v191, v126, v127\n"
  "s_waitcnt lgkmcnt(0)\n"
  "s_barrier\n"
  "v_mfma_f32_16x16x32_bf16 v[64:67], v[192:195], v[128:131], 0\n"
  "ds_read_b64_tr_b16 v[216:217], v244 offset:34760\n"
  "ds_read_b64_tr_b16 v[218:219], v244 offset:35016\n"
  "ds_write_b128 v248, v[160:163] offset:0\n"
  "v_subrev_u32 v245, 69504, v244\n"
  "s_lshl_b32 s88, %6, 9\n"
  "v_mfma_f32_16x16x32_bf16 v[64:67], v[196:199], v[136:139], v[64:67]\n"
  "ds_read_b64_tr_b16 v[220:221], v244 offset:35272\n"
  "ds_read_b64_tr_b16 v[222:223], v244 offset:35528\n"
  "ds_write_b128 v248, v[164:167] offset:1024\n"
  "s_add_u32 s89, s88, 155456\n"
  "v_mfma_f32_16x16x32_bf16 v[68:71], v[192:195], v[132:135], 0\n"
  "ds_read_b64_tr_b16 v[200:201], v244 offset:43480\n"
  "s_and_b32 s88, s88, 1024\n"
  "s_add_u32 s88, s88, 155456\n"
  "v_mfma_f32_16x16x32_bf16 v[68:71], v[196:199], v[140:143], v[68:71]\n"
  "ds_read_b64_tr_b16 v[202:203], v244 offset:43736\n"
  "ds_write_b128 v248, v[168:171] offset:2048\n"
  "v_lshl_add_u32 v124, v249, 3, s89\n"
  "v_mfma_f32_16x16x32_bf16 v[68:71], v[208:211], v[148:151], v[68:71]\n"
  "ds_read_b64_tr_b16 v[204:205], v244 offset:43992\n"
  "ds_write_b128 v248, v[172:175] offset:3072\n"
  "v_lshl_add_u32 v125, v249, 3, s88\n"
  "v_mov_b32 v120, v246\n"
  "v_mfma_f32_16x16x32_bf16 v[68:71], v[212:215], v[156:159], v[68:71]\n"
  "ds_read_b64_tr_b16 v[206:207], v244 offset:44248\n"
  "v_mov_b32 v121, v247\n"
  "v_mfma_f32_16x16x32_bf16 v[64:67], v[208:211], v[144:147], v[64:67]\n"
  "ds_read_b64_tr_b16 v[192:193], v244 offset:52128\n"
  "ds_write_b128 v248, v[176:179] offset:4096\n"
  "v_permlane32_swap_b32 v246, v120\n"
  "v_permlane32_swap_b32 v247, v121\n"
  "v_mfma_f32_16x16x32_bf16 v[64:67], v[212:215], v[152:155], v[64:67]\n"
  "ds_read_b64_tr_b16 v[194:195], v244 offset:52384\n"
  "s_waitcnt lgkmcnt(11)\n"
  "ds_write_b128 v248, v[180:183] offset:5120\n"
  "v_add_f32 v246, v246, v120\n"
  "s_waitcnt lgkmcnt(11)\n"
  "v_mfma_f32_16x16x32_bf16 v[72:75], v[216:219], v[128:131], 0\n"
  "ds_read_b64_tr_b16 v[196:197], v244 offset:52640\n"
  "v_add_f32 v247, v247, v121\n"
  "v_mov_b32 v120, v246\n"
  "v_mfma_f32_16x16x32_bf16 v[72:75], v[220:223], v[136:139], v[72:75]\n"
  "ds_read_b64_tr_b16 v[198:199], v244 offset:52896\n"
  "ds_write_b128 v248, v[184:187] offset:6144\n"
  "v_mov_b32 v121, v247\n"
  "v_mfma_f32_16x16x32_bf16 v[76:79], v[216:219], v[132:135], 0\n"
  "ds_read_b64_tr_b16 v[208:209], v244 offset:60848\n"
  "s_waitcnt lgkmcnt(11)\n"
  "ds_write_b128 v248, v[188:191] offset:7168\n"
  "v_permlane16_swap_b32 v246, v120\n"
  "v_permlane16_swap_b32 v247, v121\n"
  "v_mfma_f32_16x16x32_bf16 v[76:79], v[220:223], v[140:143], v[76:79]\n"
  "ds_read_b64_tr_b16 v[210:211], v244 offset:61104\n"
  "v_add_f32 v246, v246, v120\n"
  "s_waitcnt lgkmcnt(10)\n"
  "v_mfma_f32_16x16x32_bf16 v[76:79], v[200:203], v[148:151], v[76:79]\n"
  "ds_read_b64_tr_b16 v[212:213], v244 offset:61360\n"
  "v_add_f32 v247, v247, v121\n"
  "v_rcp_f32 v246, v246\n"
  "v_mfma_f32_16x16x32_bf16 v[76:79], v[204:207], v[156:159], v[76:79]\n"
  "ds_read_b64_tr_b16 v[214:215], v244 offset:61616\n"
  "v_rcp_f32 v247, v247\n"
  "v_mfma_f32_16x16x32_bf16 v[72:75], v[200:203], v[144:147], v[72:75]\n"
  "ds_read_b64_tr_b16 v[216:217], v244 offset:52136\n"
  "ds_write_b64 v124, v[246:247] offset:0\n"
  "v_lshlrev_b32 v249, 4, v249\n"
  "v_mfma_f32_16x16x32_bf16 v[72:75], v[204:207], v[152:155], v[72:75]\n"
  "ds_read_b64_tr_b16 v[218:219], v244 offset:52392\n"
  "s_lshl_b32 s43, %6, 11\n"
  "s_waitcnt lgkmcnt(9)\n"
  "v_mfma_f32_16x16x32_bf16 v[80:83], v[192:195], v[0:3], 0\n"
  "ds_read_b64_tr_b16 v[220:221], v244 offset:52648\n"
  "v_mfma_f32_16x16x32_bf16 v[80:83], v[196:199], v[8:11], v[80:83]\n"
  "ds_read_b64_tr_b16 v[222:223], v244 offset:52904\n"
  "v_mfma_f32_16x16x32_bf16 v[84:87], v[192:195], v[4:7], 0\n"
  "ds_read_b64_tr_b16 v[200:201], v244 offset:60856\n"
  "v_mfma_f32_16x16x32_bf16 v[84:87], v[196:199], v[12:15], v[84:87]\n"
  "ds_read_b64_tr_b16 v[202:203], v244 offset:61112\n"
  "s_waitcnt lgkmcnt(7)\n"
  "v_mfma_f32_16x16x32_bf16 v[84:87], v[208:211], v[20:23], v[84:87]\n"
  "ds_read_b64_tr_b16 v[204:205], v244 offset:61368\n"
  "v_mfma_f32_16x16x32_bf16 v[84:87], v[212:215], v[28:31], v[84:87]\n"
  "ds_read_b64_tr_b16 v[206:207], v244 offset:61624\n"
  "v_mfma_f32_16x16x32_bf16 v[80:83], v[208:211], v[16:19], v[80:83]\n"
  "s_waitcnt lgkmcnt(7)\n"
  "s_barrier\n"
  "ds_read_b128 v[160:163], v251 offset:0\n"
  "ds_read_b128 v[164:167], v251 offset:1024\n"
  "ds_read_b128 v[168:171], v251 offset:2048\n"
  "ds_read_b128 v[172:175], v251 offset:3072\n"
  "ds_read_b64_tr_b16 v[192:193], v245 offset:0\n"
  "v_mfma_f32_16x16x32_bf16 v[80:83], v[212:215], v[24:27], v[80:83]\n"
  "ds_read_b64_tr_b16 v[194:195], v245 offset:256\n"
  "ds_read_b128 v[176:179], v251 offset:4096\n"
  "s_waitcnt lgkmcnt(11)\n"
  "v_mfma_f32_16x16x32_bf16 v[88:91], v[216:219], v[0:3], 0\n"
  "ds_read_b64_tr_b16 v[196:197], v245 offset:512\n"
  "ds_read_b128 v[180:183], v251 offset:5120\n"
  "v_mfma_f32_16x16x32_bf16 v[88:91], v[220:223], v[8:11], v[88:91]\n"
  "ds_read_b64_tr_b16 v[198:199], v245 offset:768\n"
  "ds_read_b128 v[184:187], v251 offset:6144\n"
  "v_mfma_f32_16x16x32_bf16 v[92:95], v[216:219], v[4:7], 0\n"
  "s_waitcnt lgkmcnt(11)\n"
  "ds_read_b64_tr_b16 v[208:209], v245 offset:8720\n"
  "ds_read_b128 v[188:191], v251 offset:7168\n"
  "v_mfma_f32_16x16x32_bf16 v[92:95], v[220:223], v[12:15], v[92:95]\n"
  "ds_read_b64_tr_b16 v[210:211], v245 offset:8976\n"
  "ds_read_b128 v[32:35], v251 offset:8192\n"
  "v_mfma_f32_16x16x32_bf16 v[92:95], v[200:203], v[20:23], v[92:95]\n"
  "s_waitcnt lgkmcnt(11)\n"
  "ds_read_b64_tr_b16 v[212:213], v245 offset:9232\n"
  "ds_read_b128 v[36:39], v251 offset:9216\n"
  "v_mfma_f32_16x16x32_bf16 v[92:95], v[204:207], v[28:31], v[92:95]\n"
  "ds_read_b64_tr_b16 v[214:215], v245 offset:9488\n"
  "ds_read_b128 v[40:43], v251 offset:10240\n"
  "v_mfma_f32_16x16x32_bf16 v[88:91], v[200:203], v[16:19], v[88:91]\n"
  "s_waitcnt lgkmcnt(11)\n"
  "ds_read_b64_tr_b16 v[216:217], v245 offset:8\n"
  "ds_read_b128 v[44:47], v251 offset:11264\n"
  "v_mfma_f32_16x16x32_bf16 v[88:91], v[204:207], v[24:27], v[88:91]\n"
  "ds_read_b64_tr_b16 v[218:219], v245 offset:264\n"
  "ds_read_b128 v[48:51], v251 offset:12288\n"
  "s_waitcnt lgkmcnt(13)\n"
  "v_mfma_f32_16x16x32_bf16 v[64:67], v[192:195], v[160:163], v[64:67]\n"
  "ds_read_b64_tr_b16 v[220:221], v245 offset:520\n"
  "ds_read_b128 v[52:55], v251 offset:13312\n"
  "v_mfma_f32_16x16x32_bf16 v[64:67], v[196:199], v[168:171], v[64:67]\n"
  "s_waitcnt lgkmcnt(11)\n"
  "ds_read_b64_tr_b16 v[222:223], v245 offset:776\n"
  "ds_read_b128 v[56:59], v251 offset:14336\n"
  "v_mfma_f32_16x16x32_bf16 v[68:71], v[192:195], v[164:167], v[68:71]\n"
  "ds_read_b64_tr_b16 v[200:201], v245 offset:8728\n"
  "ds_read_b128 v[60:63], v251 offset:15360\n"
  "v_mfma_f32_16x16x32_bf16 v[68:71], v[196:199], v[172:175], v[68:71]\n"
  "s_waitcnt lgkmcnt(11)\n"
  "ds_read_b64_tr_b16 v[202:203], v245 offset:8984\n"
  "ds_read_b64 v[246:247], v125 offset:0\n"
  "v_mfma_f32_16x16x32_bf16 v[68:71], v[208:211], v[180:183], v[68:71]\n"
  "ds_read_b64_tr_b16 v[204:205], v245 offset:9240\n"
  "ds_read_b64 v[122:123], v125 offset:512\n"
  "v_mfma_f32_16x16x32_bf16 v[68:71], v[212:215], v[188:191], v[68:71]\n"
  "s_waitcnt lgkmcnt(11)\n"
  "ds_read_b64_tr_b16 v[206:207], v245 offset:9496\n"
  "v_mfma_f32_16x16x32_bf16 v[64:67], v[208:211], v[176:179], v[64:67]\n"
  "ds_read_b64_tr_b16 v[192:193], v245 offset:17376\n"
  "v_mfma_f32_16x16x32_bf16 v[64:67], v[212:215], v[184:187], v[64:67]\n"
  "ds_read_b64_tr_b16 v[194:195], v245 offset:17632\n"
  "s_waitcnt lgkmcnt(3)\n"
  "v_mfma_f32_16x16x32_bf16 v[72:75], v[216:219], v[160:163], v[72:75]\n"
  "ds_read_b64_tr_b16 v[196:197], v245 offset:17888\n"
  "v_mfma_f32_16x16x32_bf16 v[72:75], v[220:223], v[168:171], v[72:75]\n"
  "ds_read_b64_tr_b16 v[198:199], v245 offset:18144\n"
  "v_mfma_f32_16x16x32_bf16 v[76:79], v[216:219], v[164:167], v[76:79]\n"
  "ds_read_b64_tr_b16 v[208:209], v245 offset:26096\n"
  "v_mul_f32 v64, v246, v64\n"
  "v_mfma_f32_16x16x32_bf16 v[76:79], v[220:223], v[172:175], v[76:79]\n"
  "ds_read_b64_tr_b16 v[210:211], v245 offset:26352\n"
  "s_waitcnt lgkmcnt(6)\n"
  "v_mfma_f32_16x16x32_bf16 v[76:79], v[200:203], v[180:183], v[76:79]\n"
  "ds_read_b64_tr_b16 v[212:213], v245 offset:26608\n"
  "v_mul_f32 v65, v246, v65\n"
  "v_mul_f32 v66, v246, v66\n"
  "v_mfma_f32_16x16x32_bf16 v[76:79], v[204:207], v[188:191], v[76:79]\n"
  "ds_read_b64_tr_b16 v[214:215], v245 offset:26864\n"
  "v_mul_f32 v67, v246, v67\n"
  "v_mfma_f32_16x16x32_bf16 v[72:75], v[200:203], v[176:179], v[72:75]\n"
  "ds_read_b64_tr_b16 v[216:217], v245 offset:17384\n"
  "v_mul_f32 v68, v247, v68\n"
  "v_mfma_f32_16x16x32_bf16 v[72:75], v[204:207], v[184:187], v[72:75]\n"
  "ds_read_b64_tr_b16 v[218:219], v245 offset:17640\n"
  "s_waitcnt lgkmcnt(6)\n"
  "v_mfma_f32_16x16x32_bf16 v[80:83], v[192:195], v[32:35], v[80:83]\n"
  "ds_read_b64_tr_b16 v[220:221], v245 offset:17896\n"
  "v_mul_f32 v69, v247, v69\n"
  "v_mfma_f32_16x16x32_bf16 v[80:83], v[196:199], v[40:43], v[80:83]\n"
  "ds_read_b64_tr_b16 v[222:223], v245 offset:18152\n"
  "v_mul_f32 v70, v247, v70\n"
  "v_mfma_f32_16x16x32_bf16 v[84:87], v[192:195], v[36:39], v[84:87]\n"
  "ds_read_b64_tr_b16 v[200:201], v245 offset:26104\n"
  "v_mul_f32 v71, v247, v71\n"
  "v_mfma_f32_16x16x32_bf16 v[84:87], v[196:199], v[44:47], v[84:87]\n"
  "ds_read_b64_tr_b16 v[202:203], v245 offset:26360\n"
  "s_waitcnt lgkmcnt(6)\n"
  "v_mfma_f32_16x16x32_bf16 v[84:87], v[208:211], v[52:55], v[84:87]\n"
  "ds_read_b64_tr_b16 v[204:205], v245 offset:26616\n"
  "v_mul_f32 v72, v246, v72\n"
  "v_mfma_f32_16x16x32_bf16 v[84:87], v[212:215], v[60:63], v[84:87]\n"
  "ds_read_b64_tr_b16 v[206:207], v245 offset:26872\n"
  "v_mul_f32 v73, v246, v73\n"
  "v_mfma_f32_16x16x32_bf16 v[80:83], v[208:211], v[48:51], v[80:83]\n"
  "s_barrier\n"
  "ds_read_b64_tr_b16 v[192:193], v245 offset:34752\n"
  "v_mfma_f32_16x16x32_bf16 v[80:83], v[212:215], v[56:59], v[80:83]\n"
  "ds_read_b64_tr_b16 v[194:195], v245 offset:35008\n"
  "s_waitcnt lgkmcnt(6)\n"
  "v_mfma_f32_16x16x32_bf16 v[88:91], v[216:219], v[32:35], v[88:91]\n"
  "ds_read_b64_tr_b16 v[196:197], v245 offset:35264\n"
  "v_mul_f32 v74, v246, v74\n"
  "v_mul_f32 v75, v246, v75\n"
  "v_mfma_f32_16x16x32_bf16 v[88:91], v[220:223], v[40:43], v[88:91]\n"
  "ds_read_b64_tr_b16 v[198:199], v245 offset:35520\n"
  "v_mul_f32 v76, v247, v76\n"
  "v_mul_f32 v77, v247, v77\n"
  "v_mfma_f32_16x16x32_bf16 v[92:95], v[216:219], v[36:39], v[92:95]\n"
  "ds_read_b64_tr_b16 v[208:209], v245 offset:43472\n"
  "v_mul_f32 v78, v247, v78\n"
  "v_mul_f32 v79, v247, v79\n"
  "v_mfma_f32_16x16x32_bf16 v[92:95], v[220:223], v[44:47], v[92:95]\n"
  "ds_read_b64_tr_b16 v[210:211], v245 offset:43728\n"
  "v_fma_f32 v80, v80, v122, v64\n"
  "s_waitcnt lgkmcnt(6)\n"
  "v_mfma_f32_16x16x32_bf16 v[92:95], v[200:203], v[52:55], v[92:95]\n"
  "ds_read_b64_tr_b16 v[212:213], v245 offset:43984\n"
  "v_fma_f32 v81, v81, v122, v65\n"
  "v_fma_f32 v82, v82, v122, v66\n"
  "v_mfma_f32_16x16x32_bf16 v[92:95], v[204:207], v[60:63], v[92:95]\n"
  "ds_read_b64_tr_b16 v[214:215], v245 offset:44240\n"
  "v_fma_f32 v83, v83, v122, v67\n"
  "v_fma_f32 v84, v84, v123, v68\n"
  "v_mfma_f32_16x16x32_bf16 v[88:91], v[200:203], v[48:51], v[88:91]\n"
  "ds_read_b64_tr_b16 v[216:217], v245 offset:34760\n"
  "v_fma_f32 v85, v85, v123, v69\n"
  "v_fma_f32 v86, v86, v123, v70\n"
  "v_mfma_f32_16x16x32_bf16 v[88:91], v[204:207], v[56:59], v[88:91]\n"
  "ds_read_b64_tr_b16 v[218:219], v245 offset:35016\n"
  "v_fma_f32 v87, v87, v123, v71\n"
  "s_waitcnt lgkmcnt(6)\n"
  "v_mfma_f32_16x16x32_bf16 v[64:67], v[192:195], v[128:131], 0\n"
  "ds_read_b64_tr_b16 v[220:221], v245 offset:35272\n"
  "v_mfma_f32_16x16x32_bf16 v[64:67], v[196:199], v[136:139], v[64:67]\n"
  "ds_read_b64_tr_b16 v[222:223], v245 offset:35528\n"
  "v_mfma_f32_16x16x32_bf16 v[68:71], v[192:195], v[132:135], 0\n"
  "ds_read_b64_tr_b16 v[200:201], v245 offset:43480\n"
  "v_fma_f32 v88, v88, v122, v72\n"
  "v_mfma_f32_16x16x32_bf16 v[68:71], v[196:199], v[140:143], v[68:71]\n"
  "ds_read_b64_tr_b16 v[202:203], v245 offset:43736\n"
  "s_waitcnt lgkmcnt(6)\n"
  "v_mfma_f32_16x16x32_bf16 v[68:71], v[208:211], v[148:151], v[68:71]\n"
  "ds_read_b64_tr_b16 v[204:205], v245 offset:43992\n"
  "v_fma_f32 v89, v89, v122, v73\n"
  "v_fma_f32 v90, v90, v122, v74\n"
  "v_mfma_f32_16x16x32_bf16 v[68:71], v[212:215], v[156:159], v[68:71]\n"
  "ds_read_b64_tr_b16 v[206:207], v245 offset:44248\n"
  "v_fma_f32 v91, v91, v122, v75\n"
  "v_fma_f32 v92, v92, v123, v76\n"
  "v_mfma_f32_16x16x32_bf16 v[64:67], v[208:211], v[144:147], v[64:67]\n"
  "ds_read_b64_tr_b16 v[192:193], v245 offset:52128\n"
  "v_fma_f32 v93, v93, v123, v77\n"
  "v_fma_f32 v94, v94, v123, v78\n"
  "v_mfma_f32_16x16x32_bf16 v[64:67], v[212:215], v[152:155], v[64:67]\n"
  "ds_read_b64_tr_b16 v[194:195], v245 offset:52384\n"
  "v_fma_f32 v95, v95, v123, v79\n"
  "s_waitcnt lgkmcnt(6)\n"
  "v_mfma_f32_16x16x32_bf16 v[72:75], v[216:219], v[128:131], 0\n"
  "ds_read_b64_tr_b16 v[196:197], v245 offset:52640\n"
  "v_mfma_f32_16x16x32_bf16 v[72:75], v[220:223], v[136:139], v[72:75]\n"
  "ds_read_b64_tr_b16 v[198:199], v245 offset:52896\n"
  "v_cvt_pk_bf16_f32 v96, v80, v81\n"
  "v_mfma_f32_16x16x32_bf16 v[76:79], v[216:219], v[132:135], 0\n"
  "ds_read_b64_tr_b16 v[208:209], v245 offset:60848\n"
  "v_cvt_pk_bf16_f32 v97, v82, v83\n"
  "v_mfma_f32_16x16x32_bf16 v[76:79], v[220:223], v[140:143], v[76:79]\n"
  "ds_read_b64_tr_b16 v[210:211], v245 offset:61104\n"
  "s_waitcnt lgkmcnt(6)\n"
  "v_mfma_f32_16x16x32_bf16 v[76:79], v[200:203], v[148:151], v[76:79]\n"
  "ds_read_b64_tr_b16 v[212:213], v245 offset:61360\n"
  "v_cvt_pk_bf16_f32 v100, v84, v85\n"
  "v_mfma_f32_16x16x32_bf16 v[76:79], v[204:207], v[156:159], v[76:79]\n"
  "ds_read_b64_tr_b16 v[214:215], v245 offset:61616\n"
  "v_cvt_pk_bf16_f32 v101, v86, v87\n"
  "v_mfma_f32_16x16x32_bf16 v[72:75], v[200:203], v[144:147], v[72:75]\n"
  "ds_read_b64_tr_b16 v[216:217], v245 offset:52136\n"
  "v_cvt_pk_bf16_f32 v98, v88, v89\n"
  "v_mfma_f32_16x16x32_bf16 v[72:75], v[204:207], v[152:155], v[72:75]\n"
  "ds_read_b64_tr_b16 v[218:219], v245 offset:52392\n"
  "s_waitcnt lgkmcnt(6)\n"
  "v_mfma_f32_16x16x32_bf16 v[80:83], v[192:195], v[0:3], 0\n"
  "ds_read_b64_tr_b16 v[220:221], v245 offset:52648\n"
  "v_cvt_pk_bf16_f32 v99, v90, v91\n"
  "v_mfma_f32_16x16x32_bf16 v[80:83], v[196:199], v[8:11], v[80:83]\n"
  "ds_read_b64_tr_b16 v[222:223], v245 offset:52904\n"
  "v_cvt_pk_bf16_f32 v102, v92, v93\n"
  "v_mfma_f32_16x16x32_bf16 v[84:87], v[192:195], v[4:7], 0\n"
  "ds_read_b64_tr_b16 v[200:201], v245 offset:60856\n"
  "v_cvt_pk_bf16_f32 v103, v94, v95\n"
  "v_mfma_f32_16x16x32_bf16 v[84:87], v[196:199], v[12:15], v[84:87]\n"
  "ds_read_b64_tr_b16 v[202:203], v245 offset:61112\n"
  "s_waitcnt lgkmcnt(6)\n"
  "v_mfma_f32_16x16x32_bf16 v[84:87], v[208:211], v[20:23], v[84:87]\n"
  "ds_read_b64_tr_b16 v[204:205], v245 offset:61368\n"
  "buffer_store_dwordx4 v[96:99], v249, %5, s43 offen offset:0 sc1\n"
  "v_mfma_f32_16x16x32_bf16 v[84:87], v[212:215], v[28:31], v[84:87]\n"
  "ds_read_b64_tr_b16 v[206:207], v245 offset:61624\n"
  "buffer_store_dwordx4 v[100:103], v249, %5, s43 offen offset:1024 sc1\n"
  "v_mfma_f32_16x16x32_bf16 v[80:83], v[208:211], v[16:19], v[80:83]\n"
  "s_barrier\n"
  "ds_read_b64_tr_b16 v[192:193], v244 offset:0\n"
  "s_add_u32 s43, s43, 8192\n"
  "v_mfma_f32_16x16x32_bf16 v[80:83], v[212:215], v[24:27], v[80:83]\n"
  "ds_read_b64_tr_b16 v[194:195], v244 offset:256\n"
  "s_waitcnt lgkmcnt(6)\n"
  "v_mfma_f32_16x16x32_bf16 v[88:91], v[216:219], v[0:3], 0\n"
  "ds_read_b64_tr_b16 v[196:197], v244 offset:512\n"
  "v_mfma_f32_16x16x32_bf16 v[88:91], v[220:223], v[8:11], v[88:91]\n"
  "ds_read_b64_tr_b16 v[198:199], v244 offset:768\n"
  "v_mfma_f32_16x16x32_bf16 v[92:95], v[216:219], v[4:7], 0\n"
  "ds_read_b64_tr_b16 v[208:209], v244 offset:8720\n"
  "v_mfma_f32_16x16x32_bf16 v[92:95], v[220:223], v[12:15], v[92:95]\n"
  "ds_read_b64_tr_b16 v[210:211], v244 offset:8976\n"
  "s_waitcnt lgkmcnt(6)\n"
  "v_mfma_f32_16x16x32_bf16 v[92:95], v[200:203], v[20:23], v[92:95]\n"
  "ds_read_b64_tr_b16 v[212:213], v244 offset:9232\n"
  "v_mfma_f32_16x16x32_bf16 v[92:95], v[204:207], v[28:31], v[92:95]\n"
  "ds_read_b64_tr_b16 v[214:215], v244 offset:9488\n"
  "v_mfma_f32_16x16x32_bf16 v[88:91], v[200:203], v[16:19], v[88:91]\n"
  "ds_read_b64_tr_b16 v[216:217], v244 offset:8\n"
  "v_mfma_f32_16x16x32_bf16 v[88:91], v[204:207], v[24:27], v[88:91]\n"
  "ds_read_b64_tr_b16 v[218:219], v244 offset:264\n"
  "s_waitcnt lgkmcnt(6)\n"
  "v_mfma_f32_16x16x32_bf16 v[64:67], v[192:195], v[160:163], v[64:67]\n"
  "ds_read_b64_tr_b16 v[220:221], v244 offset:520\n"
  "v_mfma_f32_16x16x32_bf16 v[64:67], v[196:199], v[168:171], v[64:67]\n"
  "ds_read_b64_tr_b16 v[222:223], v244 offset:776\n"
  "v_mfma_f32_16x16x32_bf16 v[68:71], v[192:195], v[164:167], v[68:71]\n"
  "ds_read_b64_tr_b16 v[200:201], v244 offset:8728\n"
  "v_mfma_f32_16x16x32_bf16 v[68:71], v[196:199], v[172:175], v[68:71]\n"
  "ds_read_b64_tr_b16 v[202:203], v244 offset:8984\n"
  "s_waitcnt lgkmcnt(6)\n"
  "v_mfma_f32_16x16x32_bf16 v[68:71], v[208:211], v[180:183], v[68:71]\n"
  "ds_read_b64_tr_b16 v[204:205], v244 offset:9240\n"
  "v_mfma_f32_16x16x32_bf16 v[68:71], v[212:215], v[188:191], v[68:71]\n"
  "ds_read_b64_tr_b16 v[206:207], v244 offset:9496\n"
  "v_mfma_f32_16x16x32_bf16 v[64:67], v[208:211], v[176:179], v[64:67]\n"
  "ds_read_b64_tr_b16 v[192:193], v244 offset:34752\n"
  "v_mfma_f32_16x16x32_bf16 v[64:67], v[212:215], v[184:187], v[64:67]\n"
  "ds_read_b64_tr_b16 v[194:195], v244 offset:35008\n"
  "s_waitcnt lgkmcnt(6)\n"
  "v_mfma_f32_16x16x32_bf16 v[72:75], v[216:219], v[160:163], v[72:75]\n"
  "ds_read_b64_tr_b16 v[196:197], v244 offset:35264\n"
  "v_mfma_f32_16x16x32_bf16 v[72:75], v[220:223], v[168:171], v[72:75]\n"
  "ds_read_b64_tr_b16 v[198:199], v244 offset:35520\n"
  "v_mfma_f32_16x16x32_bf16 v[76:79], v[216:219], v[164:167], v[76:79]\n"
  "ds_read_b64_tr_b16 v[208:209], v244 offset:43472\n"
  "v_mul_f32 v64, v246, v64\n"
  "v_mfma_f32_16x16x32_bf16 v[76:79], v[220:223], v[172:175], v[76:79]\n"
  "ds_read_b64_tr_b16 v[210:211], v244 offset:43728\n"
  "s_waitcnt lgkmcnt(6)\n"
  "v_mfma_f32_16x16x32_bf16 v[76:79], v[200:203], v[180:183], v[76:79]\n"
  "ds_read_b64_tr_b16 v[212:213], v244 offset:43984\n"
  "v_mul_f32 v65, v246, v65\n"
  "v_mul_f32 v66, v246, v66\n"
  "v_mfma_f32_16x16x32_bf16 v[76:79], v[204:207], v[188:191], v[76:79]\n"
  "ds_read_b64_tr_b16 v[214:215], v244 offset:44240\n"
  "v_mul_f32 v67, v246, v67\n"
  "v_mfma_f32_16x16x32_bf16 v[72:75], v[200:203], v[176:179], v[72:75]\n"
  "ds_read_b64_tr_b16 v[216:217], v244 offset:34760\n"
  "v_mul_f32 v68, v247, v68\n"
  "v_mfma_f32_16x16x32_bf16 v[72:75], v[204:207], v[184:187], v[72:75]\n"
  "ds_read_b64_tr_b16 v[218:219], v244 offset:35016\n"
  "s_waitcnt lgkmcnt(6)\n"
  "v_mfma_f32_16x16x32_bf16 v[80:83], v[192:195], v[32:35], v[80:83]\n"
  "ds_read_b64_tr_b16 v[220:221], v244 offset:35272\n"
  "v_mul_f32 v69, v247, v69\n"
  "v_mfma_f32_16x16x32_bf16 v[80:83], v[196:199], v[40:43], v[80:83]\n"
  "ds_read_b64_tr_b16 v[222:223], v244 offset:35528\n"
  "v_mul_f32 v70, v247, v70\n"
  "v_mfma_f32_16x16x32_bf16 v[84:87], v[192:195], v[36:39], v[84:87]\n"
  "ds_read_b64_tr_b16 v[200:201], v244 offset:43480\n"
  "v_mul_f32 v71, v247, v71\n"
  "v_mfma_f32_16x16x32_bf16 v[84:87], v[196:199], v[44:47], v[84:87]\n"
  "ds_read_b64_tr_b16 v[202:203], v244 offset:43736\n"
  "s_waitcnt lgkmcnt(6)\n"
  "v_mfma_f32_16x16x32_bf16 v[84:87], v[208:211], v[52:55], v[84:87]\n"
  "ds_read_b64_tr_b16 v[204:205], v244 offset:43992\n"
  "v_mul_f32 v72, v246, v72\n"
  "v_mfma_f32_16x16x32_bf16 v[84:87], v[212:215], v[60:63], v[84:87]\n"
  "ds_read_b64_tr_b16 v[206:207], v244 offset:44248\n"
  "v_mul_f32 v73, v246, v73\n"
  "v_mfma_f32_16x16x32_bf16 v[80:83], v[208:211], v[48:51], v[80:83]\n"
  "s_barrier\n"
  "ds_read_b64_tr_b16 v[192:193], v244 offset:52128\n"
  "v_mfma_f32_16x16x32_bf16 v[80:83], v[212:215], v[56:59], v[80:83]\n"
  "ds_read_b64_tr_b16 v[194:195], v244 offset:52384\n"
  "s_waitcnt lgkmcnt(6)\n"
  "v_mfma_f32_16x16x32_bf16 v[88:91], v[216:219], v[32:35], v[88:91]\n"
  "ds_read_b64_tr_b16 v[196:197], v244 offset:52640\n"
  "v_mul_f32 v74, v246, v74\n"
  "v_mul_f32 v75, v246, v75\n"
  "v_mfma_f32_16x16x32_bf16 v[88:91], v[220:223], v[40:43], v[88:91]\n"
  "ds_read_b64_tr_b16 v[198:199], v244 offset:52896\n"
  "v_mul_f32 v76, v247, v76\n"
  "v_mul_f32 v77, v247, v77\n"
  "v_mfma_f32_16x16x32_bf16 v[92:95], v[216:219], v[36:39], v[92:95]\n"
  "ds_read_b64_tr_b16 v[208:209], v244 offset:60848\n"
  "v_mul_f32 v78, v247, v78\n"
  "v_mul_f32 v79, v247, v79\n"
  "v_mfma_f32_16x16x32_bf16 v[92:95], v[220:223], v[44:47], v[92:95]\n"
  "ds_read_b64_tr_b16 v[210:211], v244 offset:61104\n"
  "v_fma_f32 v80, v80, v122, v64\n"
  "s_waitcnt lgkmcnt(6)\n"
  "v_mfma_f32_16x16x32_bf16 v[92:95], v[200:203], v[52:55], v[92:95]\n"
  "ds_read_b64_tr_b16 v[212:213], v244 offset:61360\n"
  "v_fma_f32 v81, v81, v122, v65\n"
  "v_fma_f32 v82, v82, v122, v66\n"
  "v_mfma_f32_16x16x32_bf16 v[92:95], v[204:207], v[60:63], v[92:95]\n"
  "ds_read_b64_tr_b16 v[214:215], v244 offset:61616\n"
  "v_fma_f32 v83, v83, v122, v67\n"
  "v_fma_f32 v84, v84, v123, v68\n"
  "v_mfma_f32_16x16x32_bf16 v[88:91], v[200:203], v[48:51], v[88:91]\n"
  "ds_read_b64_tr_b16 v[216:217], v244 offset:52136\n"
  "v_fma_f32 v85, v85, v123, v69\n"
  "v_fma_f32 v86, v86, v123, v70\n"
  "v_mfma_f32_16x16x32_bf16 v[88:91], v[204:207], v[56:59], v[88:91]\n"
  "ds_read_b64_tr_b16 v[218:219], v244 offset:52392\n"
  "v_fma_f32 v87, v87, v123, v71\n"
  "s_waitcnt lgkmcnt(6)\n"
  "v_mfma_f32_16x16x32_bf16 v[64:67], v[192:195], v[128:131], 0\n"
  "ds_read_b64_tr_b16 v[220:221], v244 offset:52648\n"
  "v_mfma_f32_16x16x32_bf16 v[64:67], v[196:199], v[136:139], v[64:67]\n"
  "ds_read_b64_tr_b16 v[222:223], v244 offset:52904\n"
  "v_mfma_f32_16x16x32_bf16 v[68:71], v[192:195], v[132:135], 0\n"
  "ds_read_b64_tr_b16 v[200:201], v244 offset:60856\n"
  "v_fma_f32 v88, v88, v122, v72\n"
  "v_mfma_f32_16x16x32_bf16 v[68:71], v[196:199], v[140:143], v[68:71]\n"
  "ds_read_b64_tr_b16 v[202:203], v244 offset:61112\n"
  "s_waitcnt lgkmcnt(6)\n"
  "v_mfma_f32_16x16x32_bf16 v[68:71], v[208:211], v[148:151], v[68:71]\n"
  "ds_read_b64_tr_b16 v[204:205], v244 offset:61368\n"
  "v_fma_f32 v89, v89, v122, v73\n"
  "v_fma_f32 v90, v90, v122, v74\n"
  "v_mfma_f32_16x16x32_bf16 v[68:71], v[212:215], v[156:159], v[68:71]\n"
  "ds_read_b64_tr_b16 v[206:207], v244 offset:61624\n"
  "v_fma_f32 v91, v91, v122, v75\n"
  "v_fma_f32 v92, v92, v123, v76\n"
  "v_mfma_f32_16x16x32_bf16 v[64:67], v[208:211], v[144:147], v[64:67]\n"
  "ds_read_b64_tr_b16 v[192:193], v245 offset:0\n"
  "v_fma_f32 v93, v93, v123, v77\n"
  "v_fma_f32 v94, v94, v123, v78\n"
  "v_mfma_f32_16x16x32_bf16 v[64:67], v[212:215], v[152:155], v[64:67]\n"
  "ds_read_b64_tr_b16 v[194:195], v245 offset:256\n"
  "v_fma_f32 v95, v95, v123, v79\n"
  "s_waitcnt lgkmcnt(6)\n"
  "v_mfma_f32_16x16x32_bf16 v[72:75], v[216:219], v[128:131], 0\n"
  "ds_read_b64_tr_b16 v[196:197], v245 offset:512\n"
  "v_mfma_f32_16x16x32_bf16 v[72:75], v[220:223], v[136:139], v[72:75]\n"
  "ds_read_b64_tr_b16 v[198:199], v245 offset:768\n"
  "v_cvt_pk_bf16_f32 v104, v80, v81\n"
  "v_mfma_f32_16x16x32_bf16 v[76:79], v[216:219], v[132:135], 0\n"
  "ds_read_b64_tr_b16 v[208:209], v245 offset:8720\n"
  "v_cvt_pk_bf16_f32 v105, v82, v83\n"
  "v_mfma_f32_16x16x32_bf16 v[76:79], v[220:223], v[140:143], v[76:79]\n"
  "ds_read_b64_tr_b16 v[210:211], v245 offset:8976\n"
  "s_waitcnt lgkmcnt(6)\n"
  "v_mfma_f32_16x16x32_bf16 v[76:79], v[200:203], v[148:151], v[76:79]\n"
  "ds_read_b64_tr_b16 v[212:213], v245 offset:9232\n"
  "v_cvt_pk_bf16_f32 v108, v84, v85\n"
  "v_mfma_f32_16x16x32_bf16 v[76:79], v[204:207], v[156:159], v[76:79]\n"
  "ds_read_b64_tr_b16 v[214:215], v245 offset:9488\n"
  "v_cvt_pk_bf16_f32 v109, v86, v87\n"
  "v_mfma_f32_16x16x32_bf16 v[72:75], v[200:203], v[144:147], v[72:75]\n"
  "ds_read_b64_tr_b16 v[216:217], v245 offset:8\n"
  "v_cvt_pk_bf16_f32 v106, v88, v89\n"
  "v_mfma_f32_16x16x32_bf16 v[72:75], v[204:207], v[152:155], v[72:75]\n"
  "ds_read_b64_tr_b16 v[218:219], v245 offset:264\n"
  "s_waitcnt lgkmcnt(6)\n"
  "v_mfma_f32_16x16x32_bf16 v[80:83], v[192:195], v[0:3], 0\n"
  "ds_read_b64_tr_b16 v[220:221], v245 offset:520\n"
  "v_cvt_pk_bf16_f32 v107, v90, v91\n"
  "v_mfma_f32_16x16x32_bf16 v[80:83], v[196:199], v[8:11], v[80:83]\n"
  "ds_read_b64_tr_b16 v[222:223], v245 offset:776\n"
  "v_cvt_pk_bf16_f32 v110, v92, v93\n"
  "v_mfma_f32_16x16x32_bf16 v[84:87], v[192:195], v[4:7], 0\n"
  "ds_read_b64_tr_b16 v[200:201], v245 offset:8728\n"
  "v_cvt_pk_bf16_f32 v111, v94, v95\n"
  "v_mfma_f32_16x16x32_bf16 v[84:87], v[196:199], v[12:15], v[84:87]\n"
  "ds_read_b64_tr_b16 v[202:203], v245 offset:8984\n"
  "s_waitcnt lgkmcnt(6)\n"
  "v_mfma_f32_16x16x32_bf16 v[84:87], v[208:211], v[20:23], v[84:87]\n"
  "ds_read_b64_tr_b16 v[204:205], v245 offset:9240\n"
  "buffer_store_dwordx4 v[104:107], v249, %5, s43 offen offset:0 sc1\n"
  "v_mfma_f32_16x16x32_bf16 v[84:87], v[212:215], v[28:31], v[84:87]\n"
  "ds_read_b64_tr_b16 v[206:207], v245 offset:9496\n"
  "buffer_store_dwordx4 v[108:111], v249, %5, s43 offen offset:1024 sc1\n"
  "v_mfma_f32_16x16x32_bf16 v[80:83], v[208:211], v[16:19], v[80:83]\n"
  "s_barrier\n"
  "ds_read_b64_tr_b16 v[192:193], v244 offset:17376\n"
  "s_add_u32 s43, s43, 8192\n"
  "v_mfma_f32_16x16x32_bf16 v[80:83], v[212:215], v[24:27], v[80:83]\n"
  "ds_read_b64_tr_b16 v[194:195], v244 offset:17632\n"
  "s_waitcnt lgkmcnt(6)\n"
  "v_mfma_f32_16x16x32_bf16 v[88:91], v[216:219], v[0:3], 0\n"
  "ds_read_b64_tr_b16 v[196:197], v244 offset:17888\n"
  "v_mfma_f32_16x16x32_bf16 v[88:91], v[220:223], v[8:11], v[88:91]\n"
  "ds_read_b64_tr_b16 v[198:199], v244 offset:18144\n"
  "v_mfma_f32_16x16x32_bf16 v[92:95], v[216:219], v[4:7], 0\n"
  "ds_read_b64_tr_b16 v[208:209], v244 offset:26096\n"
  "v_mfma_f32_16x16x32_bf16 v[92:95], v[220:223], v[12:15], v[92:95]\n"
  "ds_read_b64_tr_b16 v[210:211], v244 offset:26352\n"
  "s_waitcnt lgkmcnt(6)\n"
  "v_mfma_f32_16x16x32_bf16 v[92:95], v[200:203], v[20:23], v[92:95]\n"
  "ds_read_b64_tr_b16 v[212:213], v244 offset:26608\n"
  "v_mfma_f32_16x16x32_bf16 v[92:95], v[204:207], v[28:31], v[92:95]\n"
  "ds_read_b64_tr_b16 v[214:215], v244 offset:26864\n"
  "v_mfma_f32_16x16x32_bf16 v[88:91], v[200:203], v[16:19], v[88:91]\n"
  "ds_read_b64_tr_b16 v[216:217], v244 offset:17384\n"
  "v_mfma_f32_16x16x32_bf16 v[88:91], v[204:207], v[24:27], v[88:91]\n"
  "ds_read_b64_tr_b16 v[218:219], v244 offset:17640\n"
  "s_waitcnt lgkmcnt(6)\n"
  "v_mfma_f32_16x16x32_bf16 v[64:67], v[192:195], v[160:163], v[64:67]\n"
  "ds_read_b64_tr_b16 v[220:221], v244 offset:17896\n"
  "v_mfma_f32_16x16x32_bf16 v[64:67], v[196:199], v[168:171], v[64:67]\n"
  "ds_read_b64_tr_b16 v[222:223], v244 offset:18152\n"
  "v_mfma_f32_16x16x32_bf16 v[68:71], v[192:195], v[164:167], v[68:71]\n"
  "ds_read_b64_tr_b16 v[200:201], v244 offset:26104\n"
  "v_mfma_f32_16x16x32_bf16 v[68:71], v[196:199], v[172:175], v[68:71]\n"
  "ds_read_b64_tr_b16 v[202:203], v244 offset:26360\n"
  "s_waitcnt lgkmcnt(6)\n"
  "v_mfma_f32_16x16x32_bf16 v[68:71], v[208:211], v[180:183], v[68:71]\n"
  "ds_read_b64_tr_b16 v[204:205], v244 offset:26616\n"
  "v_mfma_f32_16x16x32_bf16 v[68:71], v[212:215], v[188:191], v[68:71]\n"
  "ds_read_b64_tr_b16 v[206:207], v244 offset:26872\n"
  "v_mfma_f32_16x16x32_bf16 v[64:67], v[208:211], v[176:179], v[64:67]\n"
  "ds_read_b64_tr_b16 v[192:193], v245 offset:17376\n"
  "v_mfma_f32_16x16x32_bf16 v[64:67], v[212:215], v[184:187], v[64:67]\n"
  "ds_read_b64_tr_b16 v[194:195], v245 offset:17632\n"
  "s_waitcnt lgkmcnt(6)\n"
  "v_mfma_f32_16x16x32_bf16 v[72:75], v[216:219], v[160:163], v[72:75]\n"
  "ds_read_b64_tr_b16 v[196:197], v245 offset:17888\n"
  "v_mfma_f32_16x16x32_bf16 v[72:75], v[220:223], v[168:171], v[72:75]\n"
  "ds_read_b64_tr_b16 v[198:199], v245 offset:18144\n"
  "v_mfma_f32_16x16x32_bf16 v[76:79], v[216:219], v[164:167], v[76:79]\n"
  "ds_read_b64_tr_b16 v[208:209], v245 offset:26096\n"
  "v_mul_f32 v64, v246, v64\n"
  "v_mfma_f32_16x16x32_bf16 v[76:79], v[220:223], v[172:175], v[76:79]\n"
  "ds_read_b64_tr_b16 v[210:211], v245 offset:26352\n"
  "s_waitcnt lgkmcnt(6)\n"
  "v_mfma_f32_16x16x32_bf16 v[76:79], v[200:203], v[180:183], v[76:79]\n"
  "ds_read_b64_tr_b16 v[212:213], v245 offset:26608\n"
  "v_mul_f32 v65, v246, v65\n"
  "v_mul_f32 v66, v246, v66\n"
  "v_mfma_f32_16x16x32_bf16 v[76:79], v[204:207], v[188:191], v[76:79]\n"
  "ds_read_b64_tr_b16 v[214:215], v245 offset:26864\n"
  "v_mul_f32 v67, v246, v67\n"
  "v_mfma_f32_16x16x32_bf16 v[72:75], v[200:203], v[176:179], v[72:75]\n"
  "ds_read_b64_tr_b16 v[216:217], v245 offset:17384\n"
  "v_mul_f32 v68, v247, v68\n"
  "v_mfma_f32_16x16x32_bf16 v[72:75], v[204:207], v[184:187], v[72:75]\n"
  "ds_read_b64_tr_b16 v[218:219], v245 offset:17640\n"
  "s_waitcnt lgkmcnt(6)\n"
  "v_mfma_f32_16x16x32_bf16 v[80:83], v[192:195], v[32:35], v[80:83]\n"
  "ds_read_b64_tr_b16 v[220:221], v245 offset:17896\n"
  "v_mul_f32 v69, v247, v69\n"
  "v_mfma_f32_16x16x32_bf16 v[80:83], v[196:199], v[40:43], v[80:83]\n"
  "ds_read_b64_tr_b16 v[222:223], v245 offset:18152\n"
  "v_mul_f32 v70, v247, v70\n"
  "v_mfma_f32_16x16x32_bf16 v[84:87], v[192:195], v[36:39], v[84:87]\n"
  "ds_read_b64_tr_b16 v[200:201], v245 offset:26104\n"
  "v_mul_f32 v71, v247, v71\n"
  "v_mfma_f32_16x16x32_bf16 v[84:87], v[196:199], v[44:47], v[84:87]\n"
  "ds_read_b64_tr_b16 v[202:203], v245 offset:26360\n"
  "s_waitcnt lgkmcnt(6)\n"
  "v_mfma_f32_16x16x32_bf16 v[84:87], v[208:211], v[52:55], v[84:87]\n"
  "ds_read_b64_tr_b16 v[204:205], v245 offset:26616\n"
  "v_mul_f32 v72, v246, v72\n"
  "v_mfma_f32_16x16x32_bf16 v[84:87], v[212:215], v[60:63], v[84:87]\n"
  "ds_read_b64_tr_b16 v[206:207], v245 offset:26872\n"
  "v_mul_f32 v73, v246, v73\n"
  "v_mfma_f32_16x16x32_bf16 v[80:83], v[208:211], v[48:51], v[80:83]\n"
  "s_barrier\n"
  "ds_read_b64_tr_b16 v[192:193], v245 offset:34752\n"
  "v_mfma_f32_16x16x32_bf16 v[80:83], v[212:215], v[56:59], v[80:83]\n"
  "ds_read_b64_tr_b16 v[194:195], v245 offset:35008\n"
  "s_waitcnt lgkmcnt(6)\n"
  "v_mfma_f32_16x16x32_bf16 v[88:91], v[216:219], v[32:35], v[88:91]\n"
  "ds_read_b64_tr_b16 v[196:197], v245 offset:35264\n"
  "v_mul_f32 v74, v246, v74\n"
  "v_mul_f32 v75, v246, v75\n"
  "v_mfma_f32_16x16x32_bf16 v[88:91], v[220:223], v[40:43], v[88:91]\n"
  "ds_read_b64_tr_b16 v[198:199], v245 offset:35520\n"
  "v_mul_f32 v76, v247, v76\n"
  "v_mul_f32 v77, v247, v77\n"
  "v_mfma_f32_16x16x32_bf16 v[92:95], v[216:219], v[36:39], v[92:95]\n"
  "ds_read_b64_tr_b16 v[208:209], v245 offset:43472\n"
  "v_mul_f32 v78, v247, v78\n"
  "v_mul_f32 v79, v247, v79\n"
  "v_mfma_f32_16x16x32_bf16 v[92:95], v[220:223], v[44:47], v[92:95]\n"
  "ds_read_b64_tr_b16 v[210:211], v245 offset:43728\n"
  "v_fma_f32 v80, v80, v122, v64\n"
  "s_waitcnt lgkmcnt(6)\n"
  "v_mfma_f32_16x16x32_bf16 v[92:95], v[200:203], v[52:55], v[92:95]\n"
  "ds_read_b64_tr_b16 v[212:213], v245 offset:43984\n"
  "v_fma_f32 v81, v81, v122, v65\n"
  "v_fma_f32 v82, v82, v122, v66\n"
  "v_mfma_f32_16x16x32_bf16 v[92:95], v[204:207], v[60:63], v[92:95]\n"
  "ds_read_b64_tr_b16 v[214:215], v245 offset:44240\n"
  "v_fma_f32 v83, v83, v122, v67\n"
  "v_fma_f32 v84, v84, v123, v68\n"
  "v_mfma_f32_16x16x32_bf16 v[88:91], v[200:203], v[48:51], v[88:91]\n"
  "ds_read_b64_tr_b16 v[216:217], v245 offset:34760\n"
  "v_fma_f32 v85, v85, v123, v69\n"
  "v_fma_f32 v86, v86, v123, v70\n"
  "v_mfma_f32_16x16x32_bf16 v[88:91], v[204:207], v[56:59], v[88:91]\n"
  "ds_read_b64_tr_b16 v[218:219], v245 offset:35016\n"
  "v_fma_f32 v87, v87, v123, v71\n"
  "s_waitcnt lgkmcnt(6)\n"
  "v_mfma_f32_16x16x32_bf16 v[64:67], v[192:195], v[128:131], 0\n"
  "ds_read_b64_tr_b16 v[220:221], v245 offset:35272\n"
  "v_mfma_f32_16x16x32_bf16 v[64:67], v[196:199], v[136:139], v[64:67]\n"
  "ds_read_b64_tr_b16 v[222:223], v245 offset:35528\n"
  "v_mfma_f32_16x16x32_bf16 v[68:71], v[192:195], v[132:135], 0\n"
  "ds_read_b64_tr_b16 v[200:201], v245 offset:43480\n"
  "v_fma_f32 v88, v88, v122, v72\n"
  "v_mfma_f32_16x16x32_bf16 v[68:71], v[196:199], v[140:143], v[68:71]\n"
  "ds_read_b64_tr_b16 v[202:203], v245 offset:43736\n"
  "s_waitcnt lgkmcnt(6)\n"
  "v_mfma_f32_16x16x32_bf16 v[68:71], v[208:211], v[148:151], v[68:71]\n"
  "ds_read_b64_tr_b16 v[204:205], v245 offset:43992\n"
  "v_fma_f32 v89, v89, v122, v73\n"
  "v_fma_f32 v90, v90, v122, v74\n"
  "v_mfma_f32_16x16x32_bf16 v[68:71], v[212:215], v[156:159], v[68:71]\n"
  "ds_read_b64_tr_b16 v[206:207], v245 offset:44248\n"
  "v_fma_f32 v91, v91, v122, v75\n"
  "v_fma_f32 v92, v92, v123, v76\n"
  "v_mfma_f32_16x16x32_bf16 v[64:67], v[208:211], v[144:147], v[64:67]\n"
  "ds_read_b64_tr_b16 v[192:193], v245 offset:52128\n"
  "v_fma_f32 v93, v93, v123, v77\n"
  "v_fma_f32 v94, v94, v123, v78\n"
  "v_mfma_f32_16x16x32_bf16 v[64:67], v[212:215], v[152:155], v[64:67]\n"
  "ds_read_b64_tr_b16 v[194:195], v245 offset:52384\n"
  "v_fma_f32 v95, v95, v123, v79\n"
  "s_waitcnt lgkmcnt(6)\n"
  "v_mfma_f32_16x16x32_bf16 v[72:75], v[216:219], v[128:131], 0\n"
  "ds_read_b64_tr_b16 v[196:197], v245 offset:52640\n"
  "v_mfma_f32_16x16x32_bf16 v[72:75], v[220:223], v[136:139], v[72:75]\n"
  "ds_read_b64_tr_b16 v[198:199], v245 offset:52896\n"
  "v_cvt_pk_bf16_f32 v112, v80, v81\n"
  "v_mfma_f32_16x16x32_bf16 v[76:79], v[216:219], v[132:135], 0\n"
  "ds_read_b64_tr_b16 v[208:209], v245 offset:60848\n"
  "v_cvt_pk_bf16_f32 v113, v82, v83\n"
  "v_mfma_f32_16x16x32_bf16 v[76:79], v[220:223], v[140:143], v[76:79]\n"
  "ds_read_b64_tr_b16 v[210:211], v245 offset:61104\n"
  "s_waitcnt lgkmcnt(6)\n"
  "v_mfma_f32_16x16x32_bf16 v[76:79], v[200:203], v[148:151], v[76:79]\n"
  "ds_read_b64_tr_b16 v[212:213], v245 offset:61360\n"
  "v_cvt_pk_bf16_f32 v116, v84, v85\n"
  "v_mfma_f32_16x16x32_bf16 v[76:79], v[204:207], v[156:159], v[76:79]\n"
  "ds_read_b64_tr_b16 v[214:215], v245 offset:61616\n"
  "v_cvt_pk_bf16_f32 v117, v86, v87\n"
  "v_mfma_f32_16x16x32_bf16 v[72:75], v[200:203], v[144:147], v[72:75]\n"
  "ds_read_b64_tr_b16 v[216:217], v245 offset:52136\n"
  "v_cvt_pk_bf16_f32 v114, v88, v89\n"
  "v_mfma_f32_16x16x32_bf16 v[72:75], v[204:207], v[152:155], v[72:75]\n"
  "ds_read_b64_tr_b16 v[218:219], v245 offset:52392\n"
  "s_waitcnt lgkmcnt(6)\n"
  "v_mfma_f32_16x16x32_bf16 v[80:83], v[192:195], v[0:3], 0\n"
  "ds_read_b64_tr_b16 v[220:221], v245 offset:52648\n"
  "v_cvt_pk_bf16_f32 v115, v90, v91\n"
  "v_mfma_f32_16x16x32_bf16 v[80:83], v[196:199], v[8:11], v[80:83]\n"
  "ds_read_b64_tr_b16 v[222:223], v245 offset:52904\n"
  "v_cvt_pk_bf16_f32 v118, v92, v93\n"
  "v_mfma_f32_16x16x32_bf16 v[84:87], v[192:195], v[4:7], 0\n"
  "ds_read_b64_tr_b16 v[200:201], v245 offset:60856\n"
  "v_cvt_pk_bf16_f32 v119, v94, v95\n"
  "v_mfma_f32_16x16x32_bf16 v[84:87], v[196:199], v[12:15], v[84:87]\n"
  "ds_read_b64_tr_b16 v[202:203], v245 offset:61112\n"
  "s_waitcnt lgkmcnt(6)\n"
  "v_mfma_f32_16x16x32_bf16 v[84:87], v[208:211], v[20:23], v[84:87]\n"
  "ds_read_b64_tr_b16 v[204:205], v245 offset:61368\n"
  "buffer_store_dwordx4 v[112:115], v249, %5, s43 offen offset:0 sc1\n"
  "v_mfma_f32_16x16x32_bf16 v[84:87], v[212:215], v[28:31], v[84:87]\n"
  "ds_read_b64_tr_b16 v[206:207], v245 offset:61624\n"
  "buffer_store_dwordx4 v[116:119], v249, %5, s43 offen offset:1024 sc1\n"
  "v_mfma_f32_16x16x32_bf16 v[80:83], v[208:211], v[16:19], v[80:83]\n"
  "s_barrier\n"
  "ds_read_b64_tr_b16 v[192:193], v244 offset:0\n"
  "s_add_u32 s43, s43, 8192\n"
  "v_mfma_f32_16x16x32_bf16 v[80:83], v[212:215], v[24:27], v[80:83]\n"
  "ds_read_b64_tr_b16 v[194:195], v244 offset:256\n"
  "s_waitcnt lgkmcnt(6)\n"
  "v_mfma_f32_16x16x32_bf16 v[88:91], v[216:219], v[0:3], 0\n"
  "ds_read_b64_tr_b16 v[196:197], v244 offset:512\n"
  "v_mfma_f32_16x16x32_bf16 v[88:91], v[220:223], v[8:11], v[88:91]\n"
  "ds_read_b64_tr_b16 v[198:199], v244 offset:768\n"
  "v_mfma_f32_16x16x32_bf16 v[92:95], v[216:219], v[4:7], 0\n"
  "ds_read_b64_tr_b16 v[208:209], v244 offset:8720\n"
  "v_mfma_f32_16x16x32_bf16 v[92:95], v[220:223], v[12:15], v[92:95]\n"
  "ds_read_b64_tr_b16 v[210:211], v244 offset:8976\n"
  "s_waitcnt lgkmcnt(6)\n"
  "v_mfma_f32_16x16x32_bf16 v[92:95], v[200:203], v[20:23], v[92:95]\n"
  "ds_read_b64_tr_b16 v[212:213], v244 offset:9232\n"
  "v_mfma_f32_16x16x32_bf16 v[92:95], v[204:207], v[28:31], v[92:95]\n"
  "ds_read_b64_tr_b16 v[214:215], v244 offset:9488\n"
  "v_mfma_f32_16x16x32_bf16 v[88:91], v[200:203], v[16:19], v[88:91]\n"
  "ds_read_b64_tr_b16 v[216:217], v244 offset:8\n"
  "v_mfma_f32_16x16x32_bf16 v[88:91], v[204:207], v[24:27], v[88:91]\n"
  "ds_read_b64_tr_b16 v[218:219], v244 offset:264\n"
  "s_waitcnt lgkmcnt(6)\n"
  "v_mfma_f32_16x16x32_bf16 v[64:67], v[192:195], v[160:163], v[64:67]\n"
  "ds_read_b64_tr_b16 v[220:221], v244 offset:520\n"
  "v_mfma_f32_16x16x32_bf16 v[64:67], v[196:199], v[168:171], v[64:67]\n"
  "ds_read_b64_tr_b16 v[222:223], v244 offset:776\n"
  "v_mfma_f32_16x16x32_bf16 v[68:71], v[192:195], v[164:167], v[68:71]\n"
  "ds_read_b64_tr_b16 v[200:201], v244 offset:8728\n"
  "v_mfma_f32_16x16x32_bf16 v[68:71], v[196:199], v[172:175], v[68:71]\n"
  "ds_read_b64_tr_b16 v[202:203], v244 offset:8984\n"
  "s_waitcnt lgkmcnt(6)\n"
  "v_mfma_f32_16x16x32_bf16 v[68:71], v[208:211], v[180:183], v[68:71]\n"
  "ds_read_b64_tr_b16 v[204:205], v244 offset:9240\n"
  "v_mfma_f32_16x16x32_bf16 v[68:71], v[212:215], v[188:191], v[68:71]\n"
  "ds_read_b64_tr_b16 v[206:207], v244 offset:9496\n"
  "v_mfma_f32_16x16x32_bf16 v[64:67], v[208:211], v[176:179], v[64:67]\n"
  "ds_read_b64_tr_b16 v[192:193], v244 offset:34752\n"
  "v_mfma_f32_16x16x32_bf16 v[64:67], v[212:215], v[184:187], v[64:67]\n"
  "ds_read_b64_tr_b16 v[194:195], v244 offset:35008\n"
  "s_waitcnt lgkmcnt(6)\n"
  "v_mfma_f32_16x16x32_bf16 v[72:75], v[216:219], v[160:163], v[72:75]\n"
  "ds_read_b64_tr_b16 v[196:197], v244 offset:35264\n"
  "v_mfma_f32_16x16x32_bf16 v[72:75], v[220:223], v[168:171], v[72:75]\n"
  "ds_read_b64_tr_b16 v[198:199], v244 offset:35520\n"
  "v_mfma_f32_16x16x32_bf16 v[76:79], v[216:219], v[164:167], v[76:79]\n"
  "ds_read_b64_tr_b16 v[208:209], v244 offset:43472\n"
  "v_mul_f32 v64, v246, v64\n"
  "v_mfma_f32_16x16x32_bf16 v[76:79], v[220:223], v[172:175], v[76:79]\n"
  "ds_read_b64_tr_b16 v[210:211], v244 offset:43728\n"
  "s_waitcnt lgkmcnt(6)\n"
  "v_mfma_f32_16x16x32_bf16 v[76:79], v[200:203], v[180:183], v[76:79]\n"
  "ds_read_b64_tr_b16 v[212:213], v244 offset:43984\n"
  "v_mul_f32 v65, v246, v65\n"
  "v_mul_f32 v66, v246, v66\n"
  "v_mfma_f32_16x16x32_bf16 v[76:79], v[204:207], v[188:191], v[76:79]\n"
  "ds_read_b64_tr_b16 v[214:215], v244 offset:44240\n"
  "v_mul_f32 v67, v246, v67\n"
  "v_mfma_f32_16x16x32_bf16 v[72:75], v[200:203], v[176:179], v[72:75]\n"
  "ds_read_b64_tr_b16 v[216:217], v244 offset:34760\n"
  "v_mul_f32 v68, v247, v68\n"
  "v_mfma_f32_16x16x32_bf16 v[72:75], v[204:207], v[184:187], v[72:75]\n"
  "ds_read_b64_tr_b16 v[218:219], v244 offset:35016\n"
  "s_waitcnt lgkmcnt(6)\n"
  "v_mfma_f32_16x16x32_bf16 v[80:83], v[192:195], v[32:35], v[80:83]\n"
  "ds_read_b64_tr_b16 v[220:221], v244 offset:35272\n"
  "v_mul_f32 v69, v247, v69\n"
  "v_mfma_f32_16x16x32_bf16 v[80:83], v[196:199], v[40:43], v[80:83]\n"
  "ds_read_b64_tr_b16 v[222:223], v244 offset:35528\n"
  "v_mul_f32 v70, v247, v70\n"
  "v_mfma_f32_16x16x32_bf16 v[84:87], v[192:195], v[36:39], v[84:87]\n"
  "ds_read_b64_tr_b16 v[200:201], v244 offset:43480\n"
  "v_mul_f32 v71, v247, v71\n"
  "v_mfma_f32_16x16x32_bf16 v[84:87], v[196:199], v[44:47], v[84:87]\n"
  "ds_read_b64_tr_b16 v[202:203], v244 offset:43736\n"
  "s_waitcnt lgkmcnt(6)\n"
  "v_mfma_f32_16x16x32_bf16 v[84:87], v[208:211], v[52:55], v[84:87]\n"
  "ds_read_b64_tr_b16 v[204:205], v244 offset:43992\n"
  "v_mul_f32 v72, v246, v72\n"
  "v_mfma_f32_16x16x32_bf16 v[84:87], v[212:215], v[60:63], v[84:87]\n"
  "ds_read_b64_tr_b16 v[206:207], v244 offset:44248\n"
  "v_mul_f32 v73, v246, v73\n"
  "v_mfma_f32_16x16x32_bf16 v[80:83], v[208:211], v[48:51], v[80:83]\n"
  "s_barrier\n"
  "ds_read_b64_tr_b16 v[192:193], v244 offset:52128\n"
  "v_mfma_f32_16x16x32_bf16 v[80:83], v[212:215], v[56:59], v[80:83]\n"
  "ds_read_b64_tr_b16 v[194:195], v244 offset:52384\n"
  "s_waitcnt lgkmcnt(6)\n"
  "v_mfma_f32_16x16x32_bf16 v[88:91], v[216:219], v[32:35], v[88:91]\n"
  "ds_read_b64_tr_b16 v[196:197], v244 offset:52640\n"
  "v_mul_f32 v74, v246, v74\n"
  "v_mul_f32 v75, v246, v75\n"
  "v_mfma_f32_16x16x32_bf16 v[88:91], v[220:223], v[40:43], v[88:91]\n"
  "ds_read_b64_tr_b16 v[198:199], v244 offset:52896\n"
  "v_mul_f32 v76, v247, v76\n"
  "v_mul_f32 v77, v247, v77\n"
  "v_mfma_f32_16x16x32_bf16 v[92:95], v[216:219], v[36:39], v[92:95]\n"
  "ds_read_b64_tr_b16 v[208:209], v244 offset:60848\n"
  "v_mul_f32 v78, v247, v78\n"
  "v_mul_f32 v79, v247, v79\n"
  "v_mfma_f32_16x16x32_bf16 v[92:95], v[220:223], v[44:47], v[92:95]\n"
  "ds_read_b64_tr_b16 v[210:211], v244 offset:61104\n"
  "v_fma_f32 v80, v80, v122, v64\n"
  "s_waitcnt lgkmcnt(6)\n"
  "v_mfma_f32_16x16x32_bf16 v[92:95], v[200:203], v[52:55], v[92:95]\n"
  "ds_read_b64_tr_b16 v[212:213], v244 offset:61360\n"
  "v_fma_f32 v81, v81, v122, v65\n"
  "v_fma_f32 v82, v82, v122, v66\n"
  "v_mfma_f32_16x16x32_bf16 v[92:95], v[204:207], v[60:63], v[92:95]\n"
  "ds_read_b64_tr_b16 v[214:215], v244 offset:61616\n"
  "v_fma_f32 v83, v83, v122, v67\n"
  "v_fma_f32 v84, v84, v123, v68\n"
  "v_mfma_f32_16x16x32_bf16 v[88:91], v[200:203], v[48:51], v[88:91]\n"
  "ds_read_b64_tr_b16 v[216:217], v244 offset:52136\n"
  "v_fma_f32 v85, v85, v123, v69\n"
  "v_fma_f32 v86, v86, v123, v70\n"
  "v_mfma_f32_16x16x32_bf16 v[88:91], v[204:207], v[56:59], v[88:91]\n"
  "ds_read_b64_tr_b16 v[218:219], v244 offset:52392\n"
  "v_fma_f32 v87, v87, v123, v71\n"
  "s_waitcnt lgkmcnt(6)\n"
  "v_mfma_f32_16x16x32_bf16 v[64:67], v[192:195], v[128:131], 0\n"
  "ds_read_b64_tr_b16 v[220:221], v244 offset:52648\n"
  "v_mfma_f32_16x16x32_bf16 v[64:67], v[196:199], v[136:139], v[64:67]\n"
  "ds_read_b64_tr_b16 v[222:223], v244 offset:52904\n"
  "v_mfma_f32_16x16x32_bf16 v[68:71], v[192:195], v[132:135], 0\n"
  "ds_read_b64_tr_b16 v[200:201], v244 offset:60856\n"
  "v_fma_f32 v88, v88, v122, v72\n"
  "v_mfma_f32_16x16x32_bf16 v[68:71], v[196:199], v[140:143], v[68:71]\n"
  "ds_read_b64_tr_b16 v[202:203], v244 offset:61112\n"
  "s_waitcnt lgkmcnt(6)\n"
  "v_mfma_f32_16x16x32_bf16 v[68:71], v[208:211], v[148:151], v[68:71]\n"
  "ds_read_b64_tr_b16 v[204:205], v244 offset:61368\n"
  "v_fma_f32 v89, v89, v122, v73\n"
  "v_fma_f32 v90, v90, v122, v74\n"
  "v_mfma_f32_16x16x32_bf16 v[68:71], v[212:215], v[156:159], v[68:71]\n"
  "ds_read_b64_tr_b16 v[206:207], v244 offset:61624\n"
  "v_fma_f32 v91, v91, v122, v75\n"
  "v_fma_f32 v92, v92, v123, v76\n"
  "v_mfma_f32_16x16x32_bf16 v[64:67], v[208:211], v[144:147], v[64:67]\n"
  "ds_read_b64_tr_b16 v[192:193], v245 offset:0\n"
  "v_fma_f32 v93, v93, v123, v77\n"
  "v_fma_f32 v94, v94, v123, v78\n"
  "v_mfma_f32_16x16x32_bf16 v[64:67], v[212:215], v[152:155], v[64:67]\n"
  "ds_read_b64_tr_b16 v[194:195], v245 offset:256\n"
  "v_fma_f32 v95, v95, v123, v79\n"
  "s_waitcnt lgkmcnt(6)\n"
  "v_mfma_f32_16x16x32_bf16 v[72:75], v[216:219], v[128:131], 0\n"
  "ds_read_b64_tr_b16 v[196:197], v245 offset:512\n"
  "v_mfma_f32_16x16x32_bf16 v[72:75], v[220:223], v[136:139], v[72:75]\n"
  "ds_read_b64_tr_b16 v[198:199], v245 offset:768\n"
  "v_cvt_pk_bf16_f32 v96, v80, v81\n"
  "v_mfma_f32_16x16x32_bf16 v[76:79], v[216:219], v[132:135], 0\n"
  "ds_read_b64_tr_b16 v[208:209], v245 offset:8720\n"
  "v_cvt_pk_bf16_f32 v97, v82, v83\n"
  "v_mfma_f32_16x16x32_bf16 v[76:79], v[220:223], v[140:143], v[76:79]\n"
  "ds_read_b64_tr_b16 v[210:211], v245 offset:8976\n"
  "s_waitcnt lgkmcnt(6)\n"
  "v_mfma_f32_16x16x32_bf16 v[76:79], v[200:203], v[148:151], v[76:79]\n"
  "ds_read_b64_tr_b16 v[212:213], v245 offset:9232\n"
  "v_cvt_pk_bf16_f32 v100, v84, v85\n"
  "v_mfma_f32_16x16x32_bf16 v[76:79], v[204:207], v[156:159], v[76:79]\n"
  "ds_read_b64_tr_b16 v[214:215], v245 offset:9488\n"
  "v_cvt_pk_bf16_f32 v101, v86, v87\n"
  "v_mfma_f32_16x16x32_bf16 v[72:75], v[200:203], v[144:147], v[72:75]\n"
  "ds_read_b64_tr_b16 v[216:217], v245 offset:8\n"
  "v_cvt_pk_bf16_f32 v98, v88, v89\n"
  "v_mfma_f32_16x16x32_bf16 v[72:75], v[204:207], v[152:155], v[72:75]\n"
  "ds_read_b64_tr_b16 v[218:219], v245 offset:264\n"
  "s_waitcnt lgkmcnt(6)\n"
  "v_mfma_f32_16x16x32_bf16 v[80:83], v[192:195], v[0:3], 0\n"
  "ds_read_b64_tr_b16 v[220:221], v245 offset:520\n"
  "v_cvt_pk_bf16_f32 v99, v90, v91\n"
  "v_mfma_f32_16x16x32_bf16 v[80:83], v[196:199], v[8:11], v[80:83]\n"
  "ds_read_b64_tr_b16 v[222:223], v245 offset:776\n"
  "v_cvt_pk_bf16_f32 v102, v92, v93\n"
  "v_mfma_f32_16x16x32_bf16 v[84:87], v[192:195], v[4:7], 0\n"
  "ds_read_b64_tr_b16 v[200:201], v245 offset:8728\n"
  "v_cvt_pk_bf16_f32 v103, v94, v95\n"
  "v_mfma_f32_16x16x32_bf16 v[84:87], v[196:199], v[12:15], v[84:87]\n"
  "ds_read_b64_tr_b16 v[202:203], v245 offset:8984\n"
  "s_waitcnt lgkmcnt(6)\n"
  "v_mfma_f32_16x16x32_bf16 v[84:87], v[208:211], v[20:23], v[84:87]\n"
  "ds_read_b64_tr_b16 v[204:205], v245 offset:9240\n"
  "buffer_store_dwordx4 v[96:99], v249, %5, s43 offen offset:0 sc1\n"
  "v_mfma_f32_16x16x32_bf16 v[84:87], v[212:215], v[28:31], v[84:87]\n"
  "ds_read_b64_tr_b16 v[206:207], v245 offset:9496\n"
  "buffer_store_dwordx4 v[100:103], v249, %5, s43 offen offset:1024 sc1\n"
  "v_mfma_f32_16x16x32_bf16 v[80:83], v[208:211], v[16:19], v[80:83]\n"
  "s_barrier\n"
  "ds_read_b64_tr_b16 v[192:193], v244 offset:17376\n"
  "s_add_u32 s43, s43, 8192\n"
  "v_mfma_f32_16x16x32_bf16 v[80:83], v[212:215], v[24:27], v[80:83]\n"
  "ds_read_b64_tr_b16 v[194:195], v244 offset:17632\n"
  "s_waitcnt lgkmcnt(6)\n"
  "v_mfma_f32_16x16x32_bf16 v[88:91], v[216:219], v[0:3], 0\n"
  "ds_read_b64_tr_b16 v[196:197], v244 offset:17888\n"
  "v_mfma_f32_16x16x32_bf16 v[88:91], v[220:223], v[8:11], v[88:91]\n"
  "ds_read_b64_tr_b16 v[198:199], v244 offset:18144\n"
  "v_mfma_f32_16x16x32_bf16 v[92:95], v[216:219], v[4:7], 0\n"
  "ds_read_b64_tr_b16 v[208:209], v244 offset:26096\n"
  "v_mfma_f32_16x16x32_bf16 v[92:95], v[220:223], v[12:15], v[92:95]\n"
  "ds_read_b64_tr_b16 v[210:211], v244 offset:26352\n"
  "s_waitcnt lgkmcnt(6)\n"
  "v_mfma_f32_16x16x32_bf16 v[92:95], v[200:203], v[20:23], v[92:95]\n"
  "ds_read_b64_tr_b16 v[212:213], v244 offset:26608\n"
  "v_mfma_f32_16x16x32_bf16 v[92:95], v[204:207], v[28:31], v[92:95]\n"
  "ds_read_b64_tr_b16 v[214:215], v244 offset:26864\n"
  "v_mfma_f32_16x16x32_bf16 v[88:91], v[200:203], v[16:19], v[88:91]\n"
  "ds_read_b64_tr_b16 v[216:217], v244 offset:17384\n"
  "v_mfma_f32_16x16x32_bf16 v[88:91], v[204:207], v[24:27], v[88:91]\n"
  "ds_read_b64_tr_b16 v[218:219], v244 offset:17640\n"
  "s_waitcnt lgkmcnt(6)\n"
  "v_mfma_f32_16x16x32_bf16 v[64:67], v[192:195], v[160:163], v[64:67]\n"
  "ds_read_b64_tr_b16 v[220:221], v244 offset:17896\n"
  "v_mfma_f32_16x16x32_bf16 v[64:67], v[196:199], v[168:171], v[64:67]\n"
  "ds_read_b64_tr_b16 v[222:223], v244 offset:18152\n"
  "v_mfma_f32_16x16x32_bf16 v[68:71], v[192:195], v[164:167], v[68:71]\n"
  "ds_read_b64_tr_b16 v[200:201], v244 offset:26104\n"
  "v_mfma_f32_16x16x32_bf16 v[68:71], v[196:199], v[172:175], v[68:71]\n"
  "ds_read_b64_tr_b16 v[202:203], v244 offset:26360\n"
  "s_waitcnt lgkmcnt(6)\n"
  "v_mfma_f32_16x16x32_bf16 v[68:71], v[208:211], v[180:183], v[68:71]\n"
  "ds_read_b64_tr_b16 v[204:205], v244 offset:26616\n"
  "v_mfma_f32_16x16x32_bf16 v[68:71], v[212:215], v[188:191], v[68:71]\n"
  "ds_read_b64_tr_b16 v[206:207], v244 offset:26872\n"
  "v_mfma_f32_16x16x32_bf16 v[64:67], v[208:211], v[176:179], v[64:67]\n"
  "ds_read_b64_tr_b16 v[192:193], v245 offset:17376\n"
  "v_mfma_f32_16x16x32_bf16 v[64:67], v[212:215], v[184:187], v[64:67]\n"
  "ds_read_b64_tr_b16 v[194:195], v245 offset:17632\n"
  "s_waitcnt lgkmcnt(6)\n"
  "v_mfma_f32_16x16x32_bf16 v[72:75], v[216:219], v[160:163], v[72:75]\n"
  "ds_read_b64_tr_b16 v[196:197], v245 offset:17888\n"
  "v_mfma_f32_16x16x32_bf16 v[72:75], v[220:223], v[168:171], v[72:75]\n"
  "ds_read_b64_tr_b16 v[198:199], v245 offset:18144\n"
  "v_mfma_f32_16x16x32_bf16 v[76:79], v[216:219], v[164:167], v[76:79]\n"
  "ds_read_b64_tr_b16 v[208:209], v245 offset:26096\n"
  "v_mul_f32 v64, v246, v64\n"
  "v_mfma_f32_16x16x32_bf16 v[76:79], v[220:223], v[172:175], v[76:79]\n"
  "ds_read_b64_tr_b16 v[210:211], v245 offset:26352\n"
  "s_waitcnt lgkmcnt(6)\n"
  "v_mfma_f32_16x16x32_bf16 v[76:79], v[200:203], v[180:183], v[76:79]\n"
  "ds_read_b64_tr_b16 v[212:213], v245 offset:26608\n"
  "v_mul_f32 v65, v246, v65\n"
  "v_mul_f32 v66, v246, v66\n"
  "v_mfma_f32_16x16x32_bf16 v[76:79], v[204:207], v[188:191], v[76:79]\n"
  "ds_read_b64_tr_b16 v[214:215], v245 offset:26864\n"
  "v_mul_f32 v67, v246, v67\n"
  "v_mfma_f32_16x16x32_bf16 v[72:75], v[200:203], v[176:179], v[72:75]\n"
  "ds_read_b64_tr_b16 v[216:217], v245 offset:17384\n"
  "v_mul_f32 v68, v247, v68\n"
  "v_mfma_f32_16x16x32_bf16 v[72:75], v[204:207], v[184:187], v[72:75]\n"
  "ds_read_b64_tr_b16 v[218:219], v245 offset:17640\n"
  "s_waitcnt lgkmcnt(6)\n"
  "v_mfma_f32_16x16x32_bf16 v[80:83], v[192:195], v[32:35], v[80:83]\n"
  "ds_read_b64_tr_b16 v[220:221], v245 offset:17896\n"
  "v_mul_f32 v69, v247, v69\n"
  "v_mfma_f32_16x16x32_bf16 v[80:83], v[196:199], v[40:43], v[80:83]\n"
  "ds_read_b64_tr_b16 v[222:223], v245 offset:18152\n"
  "v_mul_f32 v70, v247, v70\n"
  "v_mfma_f32_16x16x32_bf16 v[84:87], v[192:195], v[36:39], v[84:87]\n"
  "ds_read_b64_tr_b16 v[200:201], v245 offset:26104\n"
  "v_mul_f32 v71, v247, v71\n"
  "v_mfma_f32_16x16x32_bf16 v[84:87], v[196:199], v[44:47], v[84:87]\n"
  "ds_read_b64_tr_b16 v[202:203], v245 offset:26360\n"
  "s_waitcnt lgkmcnt(6)\n"
  "v_mfma_f32_16x16x32_bf16 v[84:87], v[208:211], v[52:55], v[84:87]\n"
  "ds_read_b64_tr_b16 v[204:205], v245 offset:26616\n"
  "v_mul_f32 v72, v246, v72\n"
  "v_mfma_f32_16x16x32_bf16 v[84:87], v[212:215], v[60:63], v[84:87]\n"
  "ds_read_b64_tr_b16 v[206:207], v245 offset:26872\n"
  "v_mul_f32 v73, v246, v73\n"
  "v_mfma_f32_16x16x32_bf16 v[80:83], v[208:211], v[48:51], v[80:83]\n"
  "s_barrier\n"
  "ds_read_b64_tr_b16 v[192:193], v245 offset:34752\n"
  "v_mfma_f32_16x16x32_bf16 v[80:83], v[212:215], v[56:59], v[80:83]\n"
  "ds_read_b64_tr_b16 v[194:195], v245 offset:35008\n"
  "s_waitcnt lgkmcnt(6)\n"
  "v_mfma_f32_16x16x32_bf16 v[88:91], v[216:219], v[32:35], v[88:91]\n"
  "ds_read_b64_tr_b16 v[196:197], v245 offset:35264\n"
  "v_mul_f32 v74, v246, v74\n"
  "v_mul_f32 v75, v246, v75\n"
  "v_mfma_f32_16x16x32_bf16 v[88:91], v[220:223], v[40:43], v[88:91]\n"
  "ds_read_b64_tr_b16 v[198:199], v245 offset:35520\n"
  "v_mul_f32 v76, v247, v76\n"
  "v_mul_f32 v77, v247, v77\n"
  "v_mfma_f32_16x16x32_bf16 v[92:95], v[216:219], v[36:39], v[92:95]\n"
  "ds_read_b64_tr_b16 v[208:209], v245 offset:43472\n"
  "v_mul_f32 v78, v247, v78\n"
  "v_mul_f32 v79, v247, v79\n"
  "v_mfma_f32_16x16x32_bf16 v[92:95], v[220:223], v[44:47], v[92:95]\n"
  "ds_read_b64_tr_b16 v[210:211], v245 offset:43728\n"
  "v_fma_f32 v80, v80, v122, v64\n"
  "s_waitcnt lgkmcnt(6)\n"
  "v_mfma_f32_16x16x32_bf16 v[92:95], v[200:203], v[52:55], v[92:95]\n"
  "ds_read_b64_tr_b16 v[212:213], v245 offset:43984\n"
  "v_fma_f32 v81, v81, v122, v65\n"
  "v_fma_f32 v82, v82, v122, v66\n"
  "v_mfma_f32_16x16x32_bf16 v[92:95], v[204:207], v[60:63], v[92:95]\n"
  "ds_read_b64_tr_b16 v[214:215], v245 offset:44240\n"
  "v_fma_f32 v83, v83, v122, v67\n"
  "v_fma_f32 v84, v84, v123, v68\n"
  "v_mfma_f32_16x16x32_bf16 v[88:91], v[200:203], v[48:51], v[88:91]\n"
  "ds_read_b64_tr_b16 v[216:217], v245 offset:34760\n"
  "v_fma_f32 v85, v85, v123, v69\n"
  "v_fma_f32 v86, v86, v123, v70\n"
  "v_mfma_f32_16x16x32_bf16 v[88:91], v[204:207], v[56:59], v[88:91]\n"
  "ds_read_b64_tr_b16 v[218:219], v245 offset:35016\n"
  "v_fma_f32 v87, v87, v123, v71\n"
  "s_waitcnt lgkmcnt(6)\n"
  "v_mfma_f32_16x16x32_bf16 v[64:67], v[192:195], v[128:131], 0\n"
  "ds_read_b64_tr_b16 v[220:221], v245 offset:35272\n"
  "v_mfma_f32_16x16x32_bf16 v[64:67], v[196:199], v[136:139], v[64:67]\n"
  "ds_read_b64_tr_b16 v[222:223], v245 offset:35528\n"
  "v_mfma_f32_16x16x32_bf16 v[68:71], v[192:195], v[132:135], 0\n"
  "ds_read_b64_tr_b16 v[200:201], v245 offset:43480\n"
  "v_fma_f32 v88, v88, v122, v72\n"
  "v_mfma_f32_16x16x32_bf16 v[68:71], v[196:199], v[140:143], v[68:71]\n"
  "ds_read_b64_tr_b16 v[202:203], v245 offset:43736\n"
  "s_waitcnt lgkmcnt(6)\n"
  "v_mfma_f32_16x16x32_bf16 v[68:71], v[208:211], v[148:151], v[68:71]\n"
  "ds_read_b64_tr_b16 v[204:205], v245 offset:43992\n"
  "v_fma_f32 v89, v89, v122, v73\n"
  "v_fma_f32 v90, v90, v122, v74\n"
  "v_mfma_f32_16x16x32_bf16 v[68:71], v[212:215], v[156:159], v[68:71]\n"
  "ds_read_b64_tr_b16 v[206:207], v245 offset:44248\n"
  "v_fma_f32 v91, v91, v122, v75\n"
  "v_fma_f32 v92, v92, v123, v76\n"
  "v_mfma_f32_16x16x32_bf16 v[64:67], v[208:211], v[144:147], v[64:67]\n"
  "ds_read_b64_tr_b16 v[192:193], v245 offset:52128\n"
  "v_fma_f32 v93, v93, v123, v77\n"
  "v_fma_f32 v94, v94, v123, v78\n"
  "v_mfma_f32_16x16x32_bf16 v[64:67], v[212:215], v[152:155], v[64:67]\n"
  "ds_read_b64_tr_b16 v[194:195], v245 offset:52384\n"
  "v_fma_f32 v95, v95, v123, v79\n"
  "s_waitcnt lgkmcnt(6)\n"
  "v_mfma_f32_16x16x32_bf16 v[72:75], v[216:219], v[128:131], 0\n"
  "ds_read_b64_tr_b16 v[196:197], v245 offset:52640\n"
  "v_mfma_f32_16x16x32_bf16 v[72:75], v[220:223], v[136:139], v[72:75]\n"
  "ds_read_b64_tr_b16 v[198:199], v245 offset:52896\n"
  "v_cvt_pk_bf16_f32 v104, v80, v81\n"
  "v_mfma_f32_16x16x32_bf16 v[76:79], v[216:219], v[132:135], 0\n"
  "ds_read_b64_tr_b16 v[208:209], v245 offset:60848\n"
  "v_cvt_pk_bf16_f32 v105, v82, v83\n"
  "v_mfma_f32_16x16x32_bf16 v[76:79], v[220:223], v[140:143], v[76:79]\n"
  "ds_read_b64_tr_b16 v[210:211], v245 offset:61104\n"
  "s_waitcnt lgkmcnt(6)\n"
  "v_mfma_f32_16x16x32_bf16 v[76:79], v[200:203], v[148:151], v[76:79]\n"
  "ds_read_b64_tr_b16 v[212:213], v245 offset:61360\n"
  "v_cvt_pk_bf16_f32 v108, v84, v85\n"
  "v_mfma_f32_16x16x32_bf16 v[76:79], v[204:207], v[156:159], v[76:79]\n"
  "ds_read_b64_tr_b16 v[214:215], v245 offset:61616\n"
  "v_cvt_pk_bf16_f32 v109, v86, v87\n"
  "v_mfma_f32_16x16x32_bf16 v[72:75], v[200:203], v[144:147], v[72:75]\n"
  "ds_read_b64_tr_b16 v[216:217], v245 offset:52136\n"
  "v_cvt_pk_bf16_f32 v106, v88, v89\n"
  "v_mfma_f32_16x16x32_bf16 v[72:75], v[204:207], v[152:155], v[72:75]\n"
  "ds_read_b64_tr_b16 v[218:219], v245 offset:52392\n"
  "s_waitcnt lgkmcnt(6)\n"
  "v_mfma_f32_16x16x32_bf16 v[80:83], v[192:195], v[0:3], 0\n"
  "ds_read_b64_tr_b16 v[220:221], v245 offset:52648\n"
  "v_cvt_pk_bf16_f32 v107, v90, v91\n"
  "v_mfma_f32_16x16x32_bf16 v[80:83], v[196:199], v[8:11], v[80:83]\n"
  "ds_read_b64_tr_b16 v[222:223], v245 offset:52904\n"
  "v_cvt_pk_bf16_f32 v110, v92, v93\n"
  "v_mfma_f32_16x16x32_bf16 v[84:87], v[192:195], v[4:7], 0\n"
  "ds_read_b64_tr_b16 v[200:201], v245 offset:60856\n"
  "v_cvt_pk_bf16_f32 v111, v94, v95\n"
  "v_mfma_f32_16x16x32_bf16 v[84:87], v[196:199], v[12:15], v[84:87]\n"
  "ds_read_b64_tr_b16 v[202:203], v245 offset:61112\n"
  "s_waitcnt lgkmcnt(6)\n"
  "v_mfma_f32_16x16x32_bf16 v[84:87], v[208:211], v[20:23], v[84:87]\n"
  "ds_read_b64_tr_b16 v[204:205], v245 offset:61368\n"
  "v_mfma_f32_16x16x32_bf16 v[84:87], v[212:215], v[28:31], v[84:87]\n"
  "ds_read_b64_tr_b16 v[206:207], v245 offset:61624\n"
  "v_mfma_f32_16x16x32_bf16 v[80:83], v[208:211], v[16:19], v[80:83]\n"
  "s_waitcnt vmcnt(0)\n"
  "s_barrier\n"
  "ds_read_b64_tr_b16 v[192:193], v244 offset:0\n"
  "v_mfma_f32_16x16x32_bf16 v[80:83], v[212:215], v[24:27], v[80:83]\n"
  "ds_read_b64_tr_b16 v[194:195], v244 offset:256\n"
  "buffer_store_dwordx4 v[104:107], v249, %5, s43 offen offset:0 sc1\n"
  "s_waitcnt lgkmcnt(6)\n"
  "v_mfma_f32_16x16x32_bf16 v[88:91], v[216:219], v[0:3], 0\n"
  "ds_read_b64_tr_b16 v[196:197], v244 offset:512\n"
  "buffer_store_dwordx4 v[108:111], v249, %5, s43 offen offset:1024 sc1\n"
  "v_mfma_f32_16x16x32_bf16 v[88:91], v[220:223], v[8:11], v[88:91]\n"
  "ds_read_b64_tr_b16 v[198:199], v244 offset:768\n"
  "s_add_u32 s43, s43, 8192\n"
  "v_mfma_f32_16x16x32_bf16 v[92:95], v[216:219], v[4:7], 0\n"
  "ds_read_b64_tr_b16 v[208:209], v244 offset:8720\n"
  "v_mfma_f32_16x16x32_bf16 v[92:95], v[220:223], v[12:15], v[92:95]\n"
  "ds_read_b64_tr_b16 v[210:211], v244 offset:8976\n"
  "s_waitcnt lgkmcnt(6)\n"
  "v_mfma_f32_16x16x32_bf16 v[92:95], v[200:203], v[20:23], v[92:95]\n"
  "ds_read_b64_tr_b16 v[212:213], v244 offset:9232\n"
  "v_mfma_f32_16x16x32_bf16 v[92:95], v[204:207], v[28:31], v[92:95]\n"
  "ds_read_b64_tr_b16 v[214:215], v244 offset:9488\n"
  "v_mfma_f32_16x16x32_bf16 v[88:91], v[200:203], v[16:19], v[88:91]\n"
  "ds_read_b64_tr_b16 v[216:217], v244 offset:8\n"
  "v_mfma_f32_16x16x32_bf16 v[88:91], v[204:207], v[24:27], v[88:91]\n"
  "ds_read_b64_tr_b16 v[218:219], v244 offset:264\n"
  "s_waitcnt lgkmcnt(6)\n"
  "v_mfma_f32_16x16x32_bf16 v[64:67], v[192:195], v[160:163], v[64:67]\n"
  "ds_read_b64_tr_b16 v[220:221], v244 offset:520\n"
  "v_mfma_f32_16x16x32_bf16 v[64:67], v[196:199], v[168:171], v[64:67]\n"
  "ds_read_b64_tr_b16 v[222:223], v244 offset:776\n"
  "v_mfma_f32_16x16x32_bf16 v[68:71], v[192:195], v[164:167], v[68:71]\n"
  "ds_read_b64_tr_b16 v[200:201], v244 offset:8728\n"
  "v_mfma_f32_16x16x32_bf16 v[68:71], v[196:199], v[172:175], v[68:71]\n"
  "ds_read_b64_tr_b16 v[202:203], v244 offset:8984\n"
  "s_waitcnt lgkmcnt(6)\n"
  "v_mfma_f32_16x16x32_bf16 v[68:71], v[208:211], v[180:183], v[68:71]\n"
  "ds_read_b64_tr_b16 v[204:205], v244 offset:9240\n"
  "v_mfma_f32_16x16x32_bf16 v[68:71], v[212:215], v[188:191], v[68:71]\n"
  "ds_read_b64_tr_b16 v[206:207], v244 offset:9496\n"
  "v_mfma_f32_16x16x32_bf16 v[64:67], v[208:211], v[176:179], v[64:67]\n"
  "ds_read_b64_tr_b16 v[192:193], v244 offset:34752\n"
  "v_mfma_f32_16x16x32_bf16 v[64:67], v[212:215], v[184:187], v[64:67]\n"
  "ds_read_b64_tr_b16 v[194:195], v244 offset:35008\n"
  "s_waitcnt lgkmcnt(6)\n"
  "v_mfma_f32_16x16x32_bf16 v[72:75], v[216:219], v[160:163], v[72:75]\n"
  "ds_read_b64_tr_b16 v[196:197], v244 offset:35264\n"
  "v_mfma_f32_16x16x32_bf16 v[72:75], v[220:223], v[168:171], v[72:75]\n"
  "ds_read_b64_tr_b16 v[198:199], v244 offset:35520\n"
  "v_mfma_f32_16x16x32_bf16 v[76:79], v[216:219], v[164:167], v[76:79]\n"
  "ds_read_b64_tr_b16 v[208:209], v244 offset:43472\n"
  "v_mul_f32 v64, v246, v64\n"
  "v_mfma_f32_16x16x32_bf16 v[76:79], v[220:223], v[172:175], v[76:79]\n"
  "ds_read_b64_tr_b16 v[210:211], v244 offset:43728\n"
  "s_waitcnt lgkmcnt(6)\n"
  "v_mfma_f32_16x16x32_bf16 v[76:79], v[200:203], v[180:183], v[76:79]\n"
  "ds_read_b64_tr_b16 v[212:213], v244 offset:43984\n"
  "v_mul_f32 v65, v246, v65\n"
  "v_mul_f32 v66, v246, v66\n"
  "v_mfma_f32_16x16x32_bf16 v[76:79], v[204:207], v[188:191], v[76:79]\n"
  "ds_read_b64_tr_b16 v[214:215], v244 offset:44240\n"
  "v_mul_f32 v67, v246, v67\n"
  "v_mfma_f32_16x16x32_bf16 v[72:75], v[200:203], v[176:179], v[72:75]\n"
  "ds_read_b64_tr_b16 v[216:217], v244 offset:34760\n"
  "v_mul_f32 v68, v247, v68\n"
  "v_mfma_f32_16x16x32_bf16 v[72:75], v[204:207], v[184:187], v[72:75]\n"
  "ds_read_b64_tr_b16 v[218:219], v244 offset:35016\n"
  "s_waitcnt lgkmcnt(6)\n"
  "v_mfma_f32_16x16x32_bf16 v[80:83], v[192:195], v[32:35], v[80:83]\n"
  "ds_read_b64_tr_b16 v[220:221], v244 offset:35272\n"
  "v_mul_f32 v69, v247, v69\n"
  "v_mfma_f32_16x16x32_bf16 v[80:83], v[196:199], v[40:43], v[80:83]\n"
  "ds_read_b64_tr_b16 v[222:223], v244 offset:35528\n"
  "v_mul_f32 v70, v247, v70\n"
  "v_mfma_f32_16x16x32_bf16 v[84:87], v[192:195], v[36:39], v[84:87]\n"
  "ds_read_b64_tr_b16 v[200:201], v244 offset:43480\n"
  "v_mul_f32 v71, v247, v71\n"
  "v_mfma_f32_16x16x32_bf16 v[84:87], v[196:199], v[44:47], v[84:87]\n"
  "ds_read_b64_tr_b16 v[202:203], v244 offset:43736\n"
  "s_waitcnt lgkmcnt(6)\n"
  "v_mfma_f32_16x16x32_bf16 v[84:87], v[208:211], v[52:55], v[84:87]\n"
  "ds_read_b64_tr_b16 v[204:205], v244 offset:43992\n"
  "v_mul_f32 v72, v246, v72\n"
  "v_mfma_f32_16x16x32_bf16 v[84:87], v[212:215], v[60:63], v[84:87]\n"
  "ds_read_b64_tr_b16 v[206:207], v244 offset:44248\n"
  "v_mul_f32 v73, v246, v73\n"
  "v_mfma_f32_16x16x32_bf16 v[80:83], v[208:211], v[48:51], v[80:83]\n"
  "s_barrier\n"
  "ds_read_b64_tr_b16 v[192:193], v244 offset:52128\n"
  "v_mfma_f32_16x16x32_bf16 v[80:83], v[212:215], v[56:59], v[80:83]\n"
  "ds_read_b64_tr_b16 v[194:195], v244 offset:52384\n"
  "s_waitcnt lgkmcnt(6)\n"
  "v_mfma_f32_16x16x32_bf16 v[88:91], v[216:219], v[32:35], v[88:91]\n"
  "ds_read_b64_tr_b16 v[196:197], v244 offset:52640\n"
  "v_mul_f32 v74, v246, v74\n"
  "v_mul_f32 v75, v246, v75\n"
  "v_mfma_f32_16x16x32_bf16 v[88:91], v[220:223], v[40:43], v[88:91]\n"
  "ds_read_b64_tr_b16 v[198:199], v244 offset:52896\n"
  "v_mul_f32 v76, v247, v76\n"
  "v_mul_f32 v77, v247, v77\n"
  "v_mfma_f32_16x16x32_bf16 v[92:95], v[216:219], v[36:39], v[92:95]\n"
  "ds_read_b64_tr_b16 v[208:209], v244 offset:60848\n"
  "v_mul_f32 v78, v247, v78\n"
  "v_mul_f32 v79, v247, v79\n"
  "v_mfma_f32_16x16x32_bf16 v[92:95], v[220:223], v[44:47], v[92:95]\n"
  "ds_read_b64_tr_b16 v[210:211], v244 offset:61104\n"
  "v_fma_f32 v80, v80, v122, v64\n"
  "s_waitcnt lgkmcnt(6)\n"
  "v_mfma_f32_16x16x32_bf16 v[92:95], v[200:203], v[52:55], v[92:95]\n"
  "ds_read_b64_tr_b16 v[212:213], v244 offset:61360\n"
  "v_fma_f32 v81, v81, v122, v65\n"
  "v_fma_f32 v82, v82, v122, v66\n"
  "v_mfma_f32_16x16x32_bf16 v[92:95], v[204:207], v[60:63], v[92:95]\n"
  "ds_read_b64_tr_b16 v[214:215], v244 offset:61616\n"
  "v_fma_f32 v83, v83, v122, v67\n"
  "v_fma_f32 v84, v84, v123, v68\n"
  "v_mfma_f32_16x16x32_bf16 v[88:91], v[200:203], v[48:51], v[88:91]\n"
  "ds_read_b64_tr_b16 v[216:217], v244 offset:52136\n"
  "v_fma_f32 v85, v85, v123, v69\n"
  "v_fma_f32 v86, v86, v123, v70\n"
  "v_mfma_f32_16x16x32_bf16 v[88:91], v[204:207], v[56:59], v[88:91]\n"
  "ds_read_b64_tr_b16 v[218:219], v244 offset:52392\n"
  "v_fma_f32 v87, v87, v123, v71\n"
  "s_waitcnt lgkmcnt(6)\n"
  "v_mfma_f32_16x16x32_bf16 v[64:67], v[192:195], v[128:131], 0\n"
  "ds_read_b64_tr_b16 v[220:221], v244 offset:52648\n"
  "v_mfma_f32_16x16x32_bf16 v[64:67], v[196:199], v[136:139], v[64:67]\n"
  "ds_read_b64_tr_b16 v[222:223], v244 offset:52904\n"
  "v_mfma_f32_16x16x32_bf16 v[68:71], v[192:195], v[132:135], 0\n"
  "ds_read_b64_tr_b16 v[200:201], v244 offset:60856\n"
  "v_fma_f32 v88, v88, v122, v72\n"
  "v_mfma_f32_16x16x32_bf16 v[68:71], v[196:199], v[140:143], v[68:71]\n"
  "ds_read_b64_tr_b16 v[202:203], v244 offset:61112\n"
  "s_waitcnt lgkmcnt(6)\n"
  "v_mfma_f32_16x16x32_bf16 v[68:71], v[208:211], v[148:151], v[68:71]\n"
  "ds_read_b64_tr_b16 v[204:205], v244 offset:61368\n"
  "v_fma_f32 v89, v89, v122, v73\n"
  "v_fma_f32 v90, v90, v122, v74\n"
  "v_mfma_f32_16x16x32_bf16 v[68:71], v[212:215], v[156:159], v[68:71]\n"
  "ds_read_b64_tr_b16 v[206:207], v244 offset:61624\n"
  "v_fma_f32 v91, v91, v122, v75\n"
  "v_fma_f32 v92, v92, v123, v76\n"
  "v_mfma_f32_16x16x32_bf16 v[64:67], v[208:211], v[144:147], v[64:67]\n"
  "ds_read_b64_tr_b16 v[192:193], v245 offset:0\n"
  "v_fma_f32 v93, v93, v123, v77\n"
  "v_fma_f32 v94, v94, v123, v78\n"
  "v_mfma_f32_16x16x32_bf16 v[64:67], v[212:215], v[152:155], v[64:67]\n"
  "ds_read_b64_tr_b16 v[194:195], v245 offset:256\n"
  "v_fma_f32 v95, v95, v123, v79\n"
  "s_waitcnt lgkmcnt(6)\n"
  "v_mfma_f32_16x16x32_bf16 v[72:75], v[216:219], v[128:131], 0\n"
  "ds_read_b64_tr_b16 v[196:197], v245 offset:512\n"
  "v_mfma_f32_16x16x32_bf16 v[72:75], v[220:223], v[136:139], v[72:75]\n"
  "ds_read_b64_tr_b16 v[198:199], v245 offset:768\n"
  "v_cvt_pk_bf16_f32 v112, v80, v81\n"
  "v_mfma_f32_16x16x32_bf16 v[76:79], v[216:219], v[132:135], 0\n"
  "ds_read_b64_tr_b16 v[208:209], v245 offset:8720\n"
  "v_cvt_pk_bf16_f32 v113, v82, v83\n"
  "v_mfma_f32_16x16x32_bf16 v[76:79], v[220:223], v[140:143], v[76:79]\n"
  "ds_read_b64_tr_b16 v[210:211], v245 offset:8976\n"
  "s_waitcnt lgkmcnt(6)\n"
  "v_mfma_f32_16x16x32_bf16 v[76:79], v[200:203], v[148:151], v[76:79]\n"
  "ds_read_b64_tr_b16 v[212:213], v245 offset:9232\n"
  "v_cvt_pk_bf16_f32 v116, v84, v85\n"
  "v_mfma_f32_16x16x32_bf16 v[76:79], v[204:207], v[156:159], v[76:79]\n"
  "ds_read_b64_tr_b16 v[214:215], v245 offset:9488\n"
  "v_cvt_pk_bf16_f32 v117, v86, v87\n"
  "v_mfma_f32_16x16x32_bf16 v[72:75], v[200:203], v[144:147], v[72:75]\n"
  "ds_read_b64_tr_b16 v[216:217], v245 offset:8\n"
  "v_cvt_pk_bf16_f32 v114, v88, v89\n"
  "v_mfma_f32_16x16x32_bf16 v[72:75], v[204:207], v[152:155], v[72:75]\n"
  "ds_read_b64_tr_b16 v[218:219], v245 offset:264\n"
  "s_waitcnt lgkmcnt(6)\n"
  "v_mfma_f32_16x16x32_bf16 v[80:83], v[192:195], v[0:3], 0\n"
  "ds_read_b64_tr_b16 v[220:221], v245 offset:520\n"
  "v_cvt_pk_bf16_f32 v115, v90, v91\n"
  "v_mfma_f32_16x16x32_bf16 v[80:83], v[196:199], v[8:11], v[80:83]\n"
  "ds_read_b64_tr_b16 v[222:223], v245 offset:776\n"
  "v_cvt_pk_bf16_f32 v118, v92, v93\n"
  "v_mfma_f32_16x16x32_bf16 v[84:87], v[192:195], v[4:7], 0\n"
  "ds_read_b64_tr_b16 v[200:201], v245 offset:8728\n"
  "v_cvt_pk_bf16_f32 v119, v94, v95\n"
  "v_mfma_f32_16x16x32_bf16 v[84:87], v[196:199], v[12:15], v[84:87]\n"
  "ds_read_b64_tr_b16 v[202:203], v245 offset:8984\n"
  "s_waitcnt lgkmcnt(6)\n"
  "v_mfma_f32_16x16x32_bf16 v[84:87], v[208:211], v[20:23], v[84:87]\n"
  "ds_read_b64_tr_b16 v[204:205], v245 offset:9240\n"
  "buffer_store_dwordx4 v[112:115], v249, %5, s43 offen offset:0 sc1\n"
  "v_mfma_f32_16x16x32_bf16 v[84:87], v[212:215], v[28:31], v[84:87]\n"
  "ds_read_b64_tr_b16 v[206:207], v245 offset:9496\n"
  "buffer_store_dwordx4 v[116:119], v249, %5, s43 offen offset:1024 sc1\n"
  "v_mfma_f32_16x16x32_bf16 v[80:83], v[208:211], v[16:19], v[80:83]\n"
  "s_barrier\n"
  "ds_read_b64_tr_b16 v[192:193], v244 offset:17376\n"
  "s_add_u32 s43, s43, 8192\n"
  "v_mfma_f32_16x16x32_bf16 v[80:83], v[212:215], v[24:27], v[80:83]\n"
  "ds_read_b64_tr_b16 v[194:195], v244 offset:17632\n"
  "s_waitcnt lgkmcnt(6)\n"
  "v_mfma_f32_16x16x32_bf16 v[88:91], v[216:219], v[0:3], 0\n"
  "ds_read_b64_tr_b16 v[196:197], v244 offset:17888\n"
  "v_mfma_f32_16x16x32_bf16 v[88:91], v[220:223], v[8:11], v[88:91]\n"
  "ds_read_b64_tr_b16 v[198:199], v244 offset:18144\n"
  "v_mfma_f32_16x16x32_bf16 v[92:95], v[216:219], v[4:7], 0\n"
  "ds_read_b64_tr_b16 v[208:209], v244 offset:26096\n"
  "v_mfma_f32_16x16x32_bf16 v[92:95], v[220:223], v[12:15], v[92:95]\n"
  "ds_read_b64_tr_b16 v[210:211], v244 offset:26352\n"
  "s_waitcnt lgkmcnt(6)\n"
  "v_mfma_f32_16x16x32_bf16 v[92:95], v[200:203], v[20:23], v[92:95]\n"
  "ds_read_b64_tr_b16 v[212:213], v244 offset:26608\n"
  "v_mfma_f32_16x16x32_bf16 v[92:95], v[204:207], v[28:31], v[92:95]\n"
  "ds_read_b64_tr_b16 v[214:215], v244 offset:26864\n"
  "v_mfma_f32_16x16x32_bf16 v[88:91], v[200:203], v[16:19], v[88:91]\n"
  "ds_read_b64_tr_b16 v[216:217], v244 offset:17384\n"
  "v_mfma_f32_16x16x32_bf16 v[88:91], v[204:207], v[24:27], v[88:91]\n"
  "ds_read_b64_tr_b16 v[218:219], v244 offset:17640\n"
  "s_waitcnt lgkmcnt(6)\n"
  "v_mfma_f32_16x16x32_bf16 v[64:67], v[192:195], v[160:163], v[64:67]\n"
  "ds_read_b64_tr_b16 v[220:221], v244 offset:17896\n"
  "v_mfma_f32_16x16x32_bf16 v[64:67], v[196:199], v[168:171], v[64:67]\n"
  "ds_read_b64_tr_b16 v[222:223], v244 offset:18152\n"
  "v_mfma_f32_16x16x32_bf16 v[68:71], v[192:195], v[164:167], v[68:71]\n"
  "ds_read_b64_tr_b16 v[200:201], v244 offset:26104\n"
  "v_mfma_f32_16x16x32_bf16 v[68:71], v[196:199], v[172:175], v[68:71]\n"
  "ds_read_b64_tr_b16 v[202:203], v244 offset:26360\n"
  "s_waitcnt lgkmcnt(6)\n"
  "v_mfma_f32_16x16x32_bf16 v[68:71], v[208:211], v[180:183], v[68:71]\n"
  "ds_read_b64_tr_b16 v[204:205], v244 offset:26616\n"
  "v_mfma_f32_16x16x32_bf16 v[68:71], v[212:215], v[188:191], v[68:71]\n"
  "ds_read_b64_tr_b16 v[206:207], v244 offset:26872\n"
  "v_mfma_f32_16x16x32_bf16 v[64:67], v[208:211], v[176:179], v[64:67]\n"
  "ds_read_b64_tr_b16 v[192:193], v245 offset:17376\n"
  "v_mfma_f32_16x16x32_bf16 v[64:67], v[212:215], v[184:187], v[64:67]\n"
  "ds_read_b64_tr_b16 v[194:195], v245 offset:17632\n"
  "s_waitcnt lgkmcnt(6)\n"
  "v_mfma_f32_16x16x32_bf16 v[72:75], v[216:219], v[160:163], v[72:75]\n"
  "ds_read_b64_tr_b16 v[196:197], v245 offset:17888\n"
  "v_mfma_f32_16x16x32_bf16 v[72:75], v[220:223], v[168:171], v[72:75]\n"
  "ds_read_b64_tr_b16 v[198:199], v245 offset:18144\n"
  "v_mfma_f32_16x16x32_bf16 v[76:79], v[216:219], v[164:167], v[76:79]\n"
  "ds_read_b64_tr_b16 v[208:209], v245 offset:26096\n"
  "v_mul_f32 v64, v246, v64\n"
  "v_mfma_f32_16x16x32_bf16 v[76:79], v[220:223], v[172:175], v[76:79]\n"
  "ds_read_b64_tr_b16 v[210:211], v245 offset:26352\n"
  "s_waitcnt lgkmcnt(6)\n"
  "v_mfma_f32_16x16x32_bf16 v[76:79], v[200:203], v[180:183], v[76:79]\n"
  "ds_read_b64_tr_b16 v[212:213], v245 offset:26608\n"
  "v_mul_f32 v65, v246, v65\n"
  "v_mul_f32 v66, v246, v66\n"
  "v_mfma_f32_16x16x32_bf16 v[76:79], v[204:207], v[188:191], v[76:79]\n"
  "ds_read_b64_tr_b16 v[214:215], v245 offset:26864\n"
  "v_mul_f32 v67, v246, v67\n"
  "v_mfma_f32_16x16x32_bf16 v[72:75], v[200:203], v[176:179], v[72:75]\n"
  "ds_read_b64_tr_b16 v[216:217], v245 offset:17384\n"
  "v_mul_f32 v68, v247, v68\n"
  "v_mfma_f32_16x16x32_bf16 v[72:75], v[204:207], v[184:187], v[72:75]\n"
  "ds_read_b64_tr_b16 v[218:219], v245 offset:17640\n"
  "s_waitcnt lgkmcnt(6)\n"
  "v_mfma_f32_16x16x32_bf16 v[80:83], v[192:195], v[32:35], v[80:83]\n"
  "ds_read_b64_tr_b16 v[220:221], v245 offset:17896\n"
  "v_mul_f32 v69, v247, v69\n"
  "v_mfma_f32_16x16x32_bf16 v[80:83], v[196:199], v[40:43], v[80:83]\n"
  "ds_read_b64_tr_b16 v[222:223], v245 offset:18152\n"
  "v_mul_f32 v70, v247, v70\n"
  "v_mfma_f32_16x16x32_bf16 v[84:87], v[192:195], v[36:39], v[84:87]\n"
  "ds_read_b64_tr_b16 v[200:201], v245 offset:26104\n"
  "v_mul_f32 v71, v247, v71\n"
  "v_mfma_f32_16x16x32_bf16 v[84:87], v[196:199], v[44:47], v[84:87]\n"
  "ds_read_b64_tr_b16 v[202:203], v245 offset:26360\n"
  "s_waitcnt lgkmcnt(6)\n"
  "v_mfma_f32_16x16x32_bf16 v[84:87], v[208:211], v[52:55], v[84:87]\n"
  "ds_read_b64_tr_b16 v[204:205], v245 offset:26616\n"
  "v_mul_f32 v72, v246, v72\n"
  "v_mfma_f32_16x16x32_bf16 v[84:87], v[212:215], v[60:63], v[84:87]\n"
  "ds_read_b64_tr_b16 v[206:207], v245 offset:26872\n"
  "v_mul_f32 v73, v246, v73\n"
  "v_mfma_f32_16x16x32_bf16 v[80:83], v[208:211], v[48:51], v[80:83]\n"
  "v_mfma_f32_16x16x32_bf16 v[80:83], v[212:215], v[56:59], v[80:83]\n"
  "s_waitcnt lgkmcnt(4)\n"
  "v_mfma_f32_16x16x32_bf16 v[88:91], v[216:219], v[32:35], v[88:91]\n"
  "v_mul_f32 v74, v246, v74\n"
  "v_mul_f32 v75, v246, v75\n"
  "v_mfma_f32_16x16x32_bf16 v[88:91], v[220:223], v[40:43], v[88:91]\n"
  "v_mul_f32 v76, v247, v76\n"
  "v_mul_f32 v77, v247, v77\n"
  "v_mfma_f32_16x16x32_bf16 v[92:95], v[216:219], v[36:39], v[92:95]\n"
  "v_mul_f32 v78, v247, v78\n"
  "v_mul_f32 v79, v247, v79\n"
  "v_mfma_f32_16x16x32_bf16 v[92:95], v[220:223], v[44:47], v[92:95]\n"
  "v_fma_f32 v80, v80, v122, v64\n"
  "s_waitcnt lgkmcnt(0)\n"
  "v_mfma_f32_16x16x32_bf16 v[92:95], v[200:203], v[52:55], v[92:95]\n"
  "v_fma_f32 v81, v81, v122, v65\n"
  "v_fma_f32 v82, v82, v122, v66\n"
  "v_mfma_f32_16x16x32_bf16 v[92:95], v[204:207], v[60:63], v[92:95]\n"
  "v_fma_f32 v83, v83, v122, v67\n"
  "v_fma_f32 v84, v84, v123, v68\n"
  "v_mfma_f32_16x16x32_bf16 v[88:91], v[200:203], v[48:51], v[88:91]\n"
  "v_fma_f32 v85, v85, v123, v69\n"
  "v_fma_f32 v86, v86, v123, v70\n"
  "v_mfma_f32_16x16x32_bf16 v[88:91], v[204:207], v[56:59], v[88:91]\n"
  "v_fma_f32 v87, v87, v123, v71\n"
  "s_nop 6\n"
  "v_fma_f32 v88, v88, v122, v72\n"
  "v_fma_f32 v89, v89, v122, v73\n"
  "v_fma_f32 v90, v90, v122, v74\n"
  "v_fma_f32 v91, v91, v122, v75\n"
  "v_fma_f32 v92, v92, v123, v76\n"
  "v_fma_f32 v93, v93, v123, v77\n"
  "v_fma_f32 v94, v94, v123, v78\n"
  "v_fma_f32 v95, v95, v123, v79\n"
  "v_cvt_pk_bf16_f32 v96, v80, v81\n"
  "v_cvt_pk_bf16_f32 v97, v82, v83\n"
  "v_cvt_pk_bf16_f32 v100, v84, v85\n"
  "v_cvt_pk_bf16_f32 v101, v86, v87\n"
  "v_cvt_pk_bf16_f32 v98, v88, v89\n"
  "v_cvt_pk_bf16_f32 v99, v90, v91\n"
  "v_cvt_pk_bf16_f32 v102, v92, v93\n"
  "v_cvt_pk_bf16_f32 v103, v94, v95\n"
  "buffer_store_dwordx4 v[96:99], v249, %5, s43 offen offset:0 sc1\n"
  "buffer_store_dwordx4 v[100:103], v249, %5, s43 offen offset:1024 sc1\n"
  "s_setprio 0\n"
  "s_branch .Lcons_end\n"
  ".Lrare_pro_1_0:\n"
  "s_nop 15\n"
  "v_max3_f32 v248, v200, v201, v202\n"
  "v_max_f32 v248, v248, v203\n"
  "v_mov_b32 v249, v248\n"
  "s_nop 1\n"
  "v_permlane32_swap_b32 v248, v249\n"
  "v_max_f32 v248, v248, v249\n"
  "v_mov_b32 v249, v248\n"
  "s_nop 1\n"
  "v_permlane16_swap_b32 v248, v249\n"
  "v_max_f32 v248, v248, v249\n"
  "v_ceil_f32 v248, v248\n"
  "v_max_f32 v248, 0, v248\n"
  "v_cvt_i32_f32 v249, v248\n"
  "v_sub_u32 v249, 0, v249\n"
  "v_sub_f32 v200, v200, v248\n"
  "v_sub_f32 v201, v201, v248\n"
  "v_sub_f32 v202, v202, v248\n"
  "v_sub_f32 v203, v203, v248\n"
  "v_sub_f32 v208, v208, v248\n"
  "v_sub_f32 v209, v209, v248\n"
  "v_sub_f32 v210, v210, v248\n"
  "v_sub_f32 v211, v211, v248\n"
  "v_sub_f32 v216, v216, v248\n"
  "v_sub_f32 v217, v217, v248\n"
  "v_sub_f32 v218, v218, v248\n"
  "v_sub_f32 v219, v219, v248\n"
  "v_lshlrev_b32 v250, 16, v192\n"
  "v_and_b32 v251, 0xffff0000, v192\n"
  "v_ldexp_f32 v250, v250, v249\n"
  "v_ldexp_f32 v251, v251, v249\n"
  "v_cvt_pk_bf16_f32 v192, v250, v251\n"
  "v_lshlrev_b32 v250, 16, v193\n"
  "v_and_b32 v251, 0xffff0000, v193\n"
  "v_ldexp_f32 v250, v250, v249\n"
  "v_ldexp_f32 v251, v251, v249\n"
  "v_cvt_pk_bf16_f32 v193, v250, v251\n"
  "v_lshlrev_b32 v250, 16, v194\n"
  "v_and_b32 v251, 0xffff0000, v194\n"
  "v_ldexp_f32 v250, v250, v249\n"
  "v_ldexp_f32 v251, v251, v249\n"
  "v_cvt_pk_bf16_f32 v194, v250, v251\n"
  "v_lshlrev_b32 v250, 16, v195\n"
  "v_and_b32 v251, 0xffff0000, v195\n"
  "v_ldexp_f32 v250, v250, v249\n"
  "v_ldexp_f32 v251, v251, v249\n"
  "v_cvt_pk_bf16_f32 v195, v250, v251\n"
  "v_ldexp_f32 v246, v246, v249\n"
  "v_ldexp_f32 v0, v0, v249\n"
  "v_ldexp_f32 v1, v1, v249\n"
  "v_ldexp_f32 v2, v2, v249\n"
  "v_ldexp_f32 v3, v3, v249\n"
  "v_ldexp_f32 v8, v8, v249\n"
  "v_ldexp_f32 v9, v9, v249\n"
  "v_ldexp_f32 v10, v10, v249\n"
  "v_ldexp_f32 v11, v11, v249\n"
  "v_ldexp_f32 v16, v16, v249\n"
  "v_ldexp_f32 v17, v17, v249\n"
  "v_ldexp_f32 v18, v18, v249\n"
  "v_ldexp_f32 v19, v19, v249\n"
  "v_ldexp_f32 v24, v24, v249\n"
  "v_ldexp_f32 v25, v25, v249\n"
  "v_ldexp_f32 v26, v26, v249\n"
  "v_ldexp_f32 v27, v27, v249\n"
  "v_ldexp_f32 v32, v32, v249\n"
  "v_ldexp_f32 v33, v33, v249\n"
  "v_ldexp_f32 v34, v34, v249\n"
  "v_ldexp_f32 v35, v35, v249\n"
  "v_ldexp_f32 v40, v40, v249\n"
  "v_ldexp_f32 v41, v41, v249\n"
  "v_ldexp_f32 v42, v42, v249\n"
  "v_ldexp_f32 v43, v43, v249\n"
  "v_ldexp_f32 v48, v48, v249\n"
  "v_ldexp_f32 v49, v49, v249\n"
  "v_ldexp_f32 v50, v50, v249\n"
  "v_ldexp_f32 v51, v51, v249\n"
  "v_ldexp_f32 v56, v56, v249\n"
  "v_ldexp_f32 v57, v57, v249\n"
  "v_ldexp_f32 v58, v58, v249\n"
  "v_ldexp_f32 v59, v59, v249\n"
  "v_ldexp_f32 v64, v64, v249\n"
  "v_ldexp_f32 v65, v65, v249\n"
  "v_ldexp_f32 v66, v66, v249\n"
  "v_ldexp_f32 v67, v67, v249\n"
  "v_ldexp_f32 v72, v72, v249\n"
  "v_ldexp_f32 v73, v73, v249\n"
  "v_ldexp_f32 v74, v74, v249\n"
  "v_ldexp_f32 v75, v75, v249\n"
  "v_ldexp_f32 v80, v80, v249\n"
  "v_ldexp_f32 v81, v81, v249\n"
  "v_ldexp_f32 v82, v82, v249\n"
  "v_ldexp_f32 v83, v83, v249\n"
  "v_ldexp_f32 v88, v88, v249\n"
  "v_ldexp_f32 v89, v89, v249\n"
  "v_ldexp_f32 v90, v90, v249\n"
  "v_ldexp_f32 v91, v91, v249\n"
  "v_ldexp_f32 v96, v96, v249\n"
  "v_ldexp_f32 v97, v97, v249\n"
  "v_ldexp_f32 v98, v98, v249\n"
  "v_ldexp_f32 v99, v99, v249\n"
  "v_ldexp_f32 v104, v104, v249\n"
  "v_ldexp_f32 v105, v105, v249\n"
  "v_ldexp_f32 v106, v106, v249\n"
  "v_ldexp_f32 v107, v107, v249\n"
  "v_ldexp_f32 v112, v112, v249\n"
  "v_ldexp_f32 v113, v113, v249\n"
  "v_ldexp_f32 v114, v114, v249\n"
  "v_ldexp_f32 v115, v115, v249\n"
  "v_ldexp_f32 v120, v120, v249\n"
  "v_ldexp_f32 v121, v121, v249\n"
  "v_ldexp_f32 v122, v122, v249\n"
  "v_ldexp_f32 v123, v123, v249\n"
  "v_max3_f32 v248, v204, v205, v206\n"
  "v_max_f32 v248, v248, v207\n"
  "v_mov_b32 v249, v248\n"
  "s_nop 1\n"
  "v_permlane32_swap_b32 v248, v249\n"
  "v_max_f32 v248, v248, v249\n"
  "v_mov_b32 v249, v248\n"
  "s_nop 1\n"
  "v_permlane16_swap_b32 v248, v249\n"
  "v_max_f32 v248, v248, v249\n"
  "v_ceil_f32 v248, v248\n"
  "v_max_f32 v248, 0, v248\n"
  "v_cvt_i32_f32 v249, v248\n"
  "v_sub_u32 v249, 0, v249\n"
  "v_sub_f32 v204, v204, v248\n"
  "v_sub_f32 v205, v205, v248\n"
  "v_sub_f32 v206, v206, v248\n"
  "v_sub_f32 v207, v207, v248\n"
  "v_sub_f32 v212, v212, v248\n"
  "v_sub_f32 v213, v213, v248\n"
  "v_sub_f32 v214, v214, v248\n"
  "v_sub_f32 v215, v215, v248\n"
  "v_sub_f32 v220, v220, v248\n"
  "v_sub_f32 v221, v221, v248\n"
  "v_sub_f32 v222, v222, v248\n"
  "v_sub_f32 v223, v223, v248\n"
  "v_lshlrev_b32 v250, 16, v196\n"
  "v_and_b32 v251, 0xffff0000, v196\n"
  "v_ldexp_f32 v250, v250, v249\n"
  "v_ldexp_f32 v251, v251, v249\n"
  "v_cvt_pk_bf16_f32 v196, v250, v251\n"
  "v_lshlrev_b32 v250, 16, v197\n"
  "v_and_b32 v251, 0xffff0000, v197\n"
  "v_ldexp_f32 v250, v250, v249\n"
  "v_ldexp_f32 v251, v251, v249\n"
  "v_cvt_pk_bf16_f32 v197, v250, v251\n"
  "v_lshlrev_b32 v250, 16, v198\n"
  "v_and_b32 v251, 0xffff0000, v198\n"
  "v_ldexp_f32 v250, v250, v249\n"
  "v_ldexp_f32 v251, v251, v249\n"
  "v_cvt_pk_bf16_f32 v198, v250, v251\n"
  "v_lshlrev_b32 v250, 16, v199\n"
  "v_and_b32 v251, 0xffff0000, v199\n"
  "v_ldexp_f32 v250, v250, v249\n"
  "v_ldexp_f32 v251, v251, v249\n"
  "v_cvt_pk_bf16_f32 v199, v250, v251\n"
  "v_ldexp_f32 v247, v247, v249\n"
  "v_ldexp_f32 v4, v4, v249\n"
  "v_ldexp_f32 v5, v5, v249\n"
  "v_ldexp_f32 v6, v6, v249\n"
  "v_ldexp_f32 v7, v7, v249\n"
  "v_ldexp_f32 v12, v12, v249\n"
  "v_ldexp_f32 v13, v13, v249\n"
  "v_ldexp_f32 v14, v14, v249\n"
  "v_ldexp_f32 v15, v15, v249\n"
  "v_ldexp_f32 v20, v20, v249\n"
  "v_ldexp_f32 v21, v21, v249\n"
  "v_ldexp_f32 v22, v22, v249\n"
  "v_ldexp_f32 v23, v23, v249\n"
  "v_ldexp_f32 v28, v28, v249\n"
  "v_ldexp_f32 v29, v29, v249\n"
  "v_ldexp_f32 v30, v30, v249\n"
  "v_ldexp_f32 v31, v31, v249\n"
  "v_ldexp_f32 v36, v36, v249\n"
  "v_ldexp_f32 v37, v37, v249\n"
  "v_ldexp_f32 v38, v38, v249\n"
  "v_ldexp_f32 v39, v39, v249\n"
  "v_ldexp_f32 v44, v44, v249\n"
  "v_ldexp_f32 v45, v45, v249\n"
  "v_ldexp_f32 v46, v46, v249\n"
  "v_ldexp_f32 v47, v47, v249\n"
  "v_ldexp_f32 v52, v52, v249\n"
  "v_ldexp_f32 v53, v53, v249\n"
  "v_ldexp_f32 v54, v54, v249\n"
  "v_ldexp_f32 v55, v55, v249\n"
  "v_ldexp_f32 v60, v60, v249\n"
  "v_ldexp_f32 v61, v61, v249\n"
  "v_ldexp_f32 v62, v62, v249\n"
  "v_ldexp_f32 v63, v63, v249\n"
  "v_ldexp_f32 v68, v68, v249\n"
  "v_ldexp_f32 v69, v69, v249\n"
  "v_ldexp_f32 v70, v70, v249\n"
  "v_ldexp_f32 v71, v71, v249\n"
  "v_ldexp_f32 v76, v76, v249\n"
  "v_ldexp_f32 v77, v77, v249\n"
  "v_ldexp_f32 v78, v78, v249\n"
  "v_ldexp_f32 v79, v79, v249\n"
  "v_ldexp_f32 v84, v84, v249\n"
  "v_ldexp_f32 v85, v85, v249\n"
  "v_ldexp_f32 v86, v86, v249\n"
  "v_ldexp_f32 v87, v87, v249\n"
  "v_ldexp_f32 v92, v92, v249\n"
  "v_ldexp_f32 v93, v93, v249\n"
  "v_ldexp_f32 v94, v94, v249\n"
  "v_ldexp_f32 v95, v95, v249\n"
  "v_ldexp_f32 v100, v100, v249\n"
  "v_ldexp_f32 v101, v101, v249\n"
  "v_ldexp_f32 v102, v102, v249\n"
  "v_ldexp_f32 v103, v103, v249\n"
  "v_ldexp_f32 v108, v108, v249\n"
  "v_ldexp_f32 v109, v109, v249\n"
  "v_ldexp_f32 v110, v110, v249\n"
  "v_ldexp_f32 v111, v111, v249\n"
  "v_ldexp_f32 v116, v116, v249\n"
  "v_ldexp_f32 v117, v117, v249\n"
  "v_ldexp_f32 v118, v118, v249\n"
  "v_ldexp_f32 v119, v119, v249\n"
  "v_ldexp_f32 v124, v124, v249\n"
  "v_ldexp_f32 v125, v125, v249\n"
  "v_ldexp_f32 v126, v126, v249\n"
  "v_ldexp_f32 v127, v127, v249\n"
  "s_nop 3\n"
  "s_branch .Lback_pro_1_0\n"
  ".Lrare_pro_1_1:\n"
  "s_nop 15\n"
  "v_max3_f32 v248, v208, v209, v210\n"
  "v_max_f32 v248, v248, v211\n"
  "v_mov_b32 v249, v248\n"
  "s_nop 1\n"
  "v_permlane32_swap_b32 v248, v249\n"
  "v_max_f32 v248, v248, v249\n"
  "v_mov_b32 v249, v248\n"
  "s_nop 1\n"
  "v_permlane16_swap_b32 v248, v249\n"
  "v_max_f32 v248, v248, v249\n"
  "v_ceil_f32 v248, v248\n"
  "v_max_f32 v248, 0, v248\n"
  "v_cvt_i32_f32 v249, v248\n"
  "v_sub_u32 v249, 0, v249\n"
  "v_sub_f32 v208, v208, v248\n"
  "v_sub_f32 v209, v209, v248\n"
  "v_sub_f32 v210, v210, v248\n"
  "v_sub_f32 v211, v211, v248\n"
  "v_sub_f32 v216, v216, v248\n"
  "v_sub_f32 v217, v217, v248\n"
  "v_sub_f32 v218, v218, v248\n"
  "v_sub_f32 v219, v219, v248\n"
  "v_lshlrev_b32 v250, 16, v192\n"
  "v_and_b32 v251, 0xffff0000, v192\n"
  "v_ldexp_f32 v250, v250, v249\n"
  "v_ldexp_f32 v251, v251, v249\n"
  "v_cvt_pk_bf16_f32 v192, v250, v251\n"
  "v_lshlrev_b32 v250, 16, v193\n"
  "v_and_b32 v251, 0xffff0000, v193\n"
  "v_ldexp_f32 v250, v250, v249\n"
  "v_ldexp_f32 v251, v251, v249\n"
  "v_cvt_pk_bf16_f32 v193, v250, v251\n"
  "v_lshlrev_b32 v250, 16, v194\n"
  "v_and_b32 v251, 0xffff0000, v194\n"
  "v_ldexp_f32 v250, v250, v249\n"
  "v_ldexp_f32 v251, v251, v249\n"
  "v_cvt_pk_bf16_f32 v194, v250, v251\n"
  "v_lshlrev_b32 v250, 16, v195\n"
  "v_and_b32 v251, 0xffff0000, v195\n"
  "v_ldexp_f32 v250, v250, v249\n"
  "v_ldexp_f32 v251, v251, v249\n"
  "v_cvt_pk_bf16_f32 v195, v250, v251\n"
  "v_lshlrev_b32 v250, 16, v200\n"
  "v_and_b32 v251, 0xffff0000, v200\n"
  "v_ldexp_f32 v250, v250, v249\n"
  "v_ldexp_f32 v251, v251, v249\n"
  "v_cvt_pk_bf16_f32 v200, v250, v251\n"
  "v_lshlrev_b32 v250, 16, v201\n"
  "v_and_b32 v251, 0xffff0000, v201\n"
  "v_ldexp_f32 v250, v250, v249\n"
  "v_ldexp_f32 v251, v251, v249\n"
  "v_cvt_pk_bf16_f32 v201, v250, v251\n"
  "v_ldexp_f32 v246, v246, v249\n"
  "v_ldexp_f32 v0, v0, v249\n"
  "v_ldexp_f32 v1, v1, v249\n"
  "v_ldexp_f32 v2, v2, v249\n"
  "v_ldexp_f32 v3, v3, v249\n"
  "v_ldexp_f32 v8, v8, v249\n"
  "v_ldexp_f32 v9, v9, v249\n"
  "v_ldexp_f32 v10, v10, v249\n"
  "v_ldexp_f32 v11, v11, v249\n"
  "v_ldexp_f32 v16, v16, v249\n"
  "v_ldexp_f32 v17, v17, v249\n"
  "v_ldexp_f32 v18, v18, v249\n"
  "v_ldexp_f32 v19, v19, v249\n"
  "v_ldexp_f32 v24, v24, v249\n"
  "v_ldexp_f32 v25, v25, v249\n"
  "v_ldexp_f32 v26, v26, v249\n"
  "v_ldexp_f32 v27, v27, v249\n"
  "v_ldexp_f32 v32, v32, v249\n"
  "v_ldexp_f32 v33, v33, v249\n"
  "v_ldexp_f32 v34, v34, v249\n"
  "v_ldexp_f32 v35, v35, v249\n"
  "v_ldexp_f32 v40, v40, v249\n"
  "v_ldexp_f32 v41, v41, v249\n"
  "v_ldexp_f32 v42, v42, v249\n"
  "v_ldexp_f32 v43, v43, v249\n"
  "v_ldexp_f32 v48, v48, v249\n"
  "v_ldexp_f32 v49, v49, v249\n"
  "v_ldexp_f32 v50, v50, v249\n"
  "v_ldexp_f32 v51, v51, v249\n"
  "v_ldexp_f32 v56, v56, v249\n"
  "v_ldexp_f32 v57, v57, v249\n"
  "v_ldexp_f32 v58, v58, v249\n"
  "v_ldexp_f32 v59, v59, v249\n"
  "v_ldexp_f32 v64, v64, v249\n"
  "v_ldexp_f32 v65, v65, v249\n"
  "v_ldexp_f32 v66, v66, v249\n"
  "v_ldexp_f32 v67, v67, v249\n"
  "v_ldexp_f32 v72, v72, v249\n"
  "v_ldexp_f32 v73, v73, v249\n"
  "v_ldexp_f32 v74, v74, v249\n"
  "v_ldexp_f32 v75, v75, v249\n"
  "v_ldexp_f32 v80, v80, v249\n"
  "v_ldexp_f32 v81, v81, v249\n"
  "v_ldexp_f32 v82, v82, v249\n"
  "v_ldexp_f32 v83, v83, v249\n"
  "v_ldexp_f32 v88, v88, v249\n"
  "v_ldexp_f32 v89, v89, v249\n"
  "v_ldexp_f32 v90, v90, v249\n"
  "v_ldexp_f32 v91, v91, v249\n"
  "v_ldexp_f32 v96, v96, v249\n"
  "v_ldexp_f32 v97, v97, v249\n"
  "v_ldexp_f32 v98, v98, v249\n"
  "v_ldexp_f32 v99, v99, v249\n"
  "v_ldexp_f32 v104, v104, v249\n"
  "v_ldexp_f32 v105, v105, v249\n"
  "v_ldexp_f32 v106, v106, v249\n"
  "v_ldexp_f32 v107, v107, v249\n"
  "v_ldexp_f32 v112, v112, v249\n"
  "v_ldexp_f32 v113, v113, v249\n"
  "v_ldexp_f32 v114, v114, v249\n"
  "v_ldexp_f32 v115, v115, v249\n"
  "v_ldexp_f32 v120, v120, v249\n"
  "v_ldexp_f32 v121, v121, v249\n"
  "v_ldexp_f32 v122, v122, v249\n"
  "v_ldexp_f32 v123, v123, v249\n"
  "v_max3_f32 v248, v212, v213, v214\n"
  "v_max_f32 v248, v248, v215\n"
  "v_mov_b32 v249, v248\n"
  "s_nop 1\n"
  "v_permlane32_swap_b32 v248, v249\n"
  "v_max_f32 v248, v248, v249\n"
  "v_mov_b32 v249, v248\n"
  "s_nop 1\n"
  "v_permlane16_swap_b32 v248, v249\n"
  "v_max_f32 v248, v248, v249\n"
  "v_ceil_f32 v248, v248\n"
  "v_max_f32 v248, 0, v248\n"
  "v_cvt_i32_f32 v249, v248\n"
  "v_sub_u32 v249, 0, v249\n"
  "v_sub_f32 v212, v212, v248\n"
  "v_sub_f32 v213, v213, v248\n"
  "v_sub_f32 v214, v214, v248\n"
  "v_sub_f32 v215, v215, v248\n"
  "v_sub_f32 v220, v220, v248\n"
  "v_sub_f32 v221, v221, v248\n"
  "v_sub_f32 v222, v222, v248\n"
  "v_sub_f32 v223, v223, v248\n"
  "v_lshlrev_b32 v250, 16, v196\n"
  "v_and_b32 v251, 0xffff0000, v196\n"
  "v_ldexp_f32 v250, v250, v249\n"
  "v_ldexp_f32 v251, v251, v249\n"
  "v_cvt_pk_bf16_f32 v196, v250, v251\n"
  "v_lshlrev_b32 v250, 16, v197\n"
  "v_and_b32 v251, 0xffff0000, v197\n"
  "v_ldexp_f32 v250, v250, v249\n"
  "v_ldexp_f32 v251, v251, v249\n"
  "v_cvt_pk_bf16_f32 v197, v250, v251\n"
  "v_lshlrev_b32 v250, 16, v198\n"
  "v_and_b32 v251, 0xffff0000, v198\n"
  "v_ldexp_f32 v250, v250, v249\n"
  "v_ldexp_f32 v251, v251, v249\n"
  "v_cvt_pk_bf16_f32 v198, v250, v251\n"
  "v_lshlrev_b32 v250, 16, v199\n"
  "v_and_b32 v251, 0xffff0000, v199\n"
  "v_ldexp_f32 v250, v250, v249\n"
  "v_ldexp_f32 v251, v251, v249\n"
  "v_cvt_pk_bf16_f32 v199, v250, v251\n"
  "v_lshlrev_b32 v250, 16, v204\n"
  "v_and_b32 v251, 0xffff0000, v204\n"
  "v_ldexp_f32 v250, v250, v249\n"
  "v_ldexp_f32 v251, v251, v249\n"
  "v_cvt_pk_bf16_f32 v204, v250, v251\n"
  "v_lshlrev_b32 v250, 16, v205\n"
  "v_and_b32 v251, 0xffff0000, v205\n"
  "v_ldexp_f32 v250, v250, v249\n"
  "v_ldexp_f32 v251, v251, v249\n"
  "v_cvt_pk_bf16_f32 v205, v250, v251\n"
  "v_ldexp_f32 v247, v247, v249\n"
  "v_ldexp_f32 v4, v4, v249\n"
  "v_ldexp_f32 v5, v5, v249\n"
  "v_ldexp_f32 v6, v6, v249\n"
  "v_ldexp_f32 v7, v7, v249\n"
  "v_ldexp_f32 v12, v12, v249\n"
  "v_ldexp_f32 v13, v13, v249\n"
  "v_ldexp_f32 v14, v14, v249\n"
  "v_ldexp_f32 v15, v15, v249\n"
  "v_ldexp_f32 v20, v20, v249\n"
  "v_ldexp_f32 v21, v21, v249\n"
  "v_ldexp_f32 v22, v22, v249\n"
  "v_ldexp_f32 v23, v23, v249\n"
  "v_ldexp_f32 v28, v28, v249\n"
  "v_ldexp_f32 v29, v29, v249\n"
  "v_ldexp_f32 v30, v30, v249\n"
  "v_ldexp_f32 v31, v31, v249\n"
  "v_ldexp_f32 v36, v36, v249\n"
  "v_ldexp_f32 v37, v37, v249\n"
  "v_ldexp_f32 v38, v38, v249\n"
  "v_ldexp_f32 v39, v39, v249\n"
  "v_ldexp_f32 v44, v44, v249\n"
  "v_ldexp_f32 v45, v45, v249\n"
  "v_ldexp_f32 v46, v46, v249\n"
  "v_ldexp_f32 v47, v47, v249\n"
  "v_ldexp_f32 v52, v52, v249\n"
  "v_ldexp_f32 v53, v53, v249\n"
  "v_ldexp_f32 v54, v54, v249\n"
  "v_ldexp_f32 v55, v55, v249\n"
  "v_ldexp_f32 v60, v60, v249\n"
  "v_ldexp_f32 v61, v61, v249\n"
  "v_ldexp_f32 v62, v62, v249\n"
  "v_ldexp_f32 v63, v63, v249\n"
  "v_ldexp_f32 v68, v68, v249\n"
  "v_ldexp_f32 v69, v69, v249\n"
  "v_ldexp_f32 v70, v70, v249\n"
  "v_ldexp_f32 v71, v71, v249\n"
  "v_ldexp_f32 v76, v76, v249\n"
  "v_ldexp_f32 v77, v77, v249\n"
  "v_ldexp_f32 v78, v78, v249\n"
  "v_ldexp_f32 v79, v79, v249\n"
  "v_ldexp_f32 v84, v84, v249\n"
  "v_ldexp_f32 v85, v85, v249\n"
  "v_ldexp_f32 v86, v86, v249\n"
  "v_ldexp_f32 v87, v87, v249\n"
  "v_ldexp_f32 v92, v92, v249\n"
  "v_ldexp_f32 v93, v93, v249\n"
  "v_ldexp_f32 v94, v94, v249\n"
  "v_ldexp_f32 v95, v95, v249\n"
  "v_ldexp_f32 v100, v100, v249\n"
  "v_ldexp_f32 v101, v101, v249\n"
  "v_ldexp_f32 v102, v102, v249\n"
  "v_ldexp_f32 v103, v103, v249\n"
  "v_ldexp_f32 v108, v108, v249\n"
  "v_ldexp_f32 v109, v109, v249\n"
  "v_ldexp_f32 v110, v110, v249\n"
  "v_ldexp_f32 v111, v111, v249\n"
  "v_ldexp_f32 v116, v116, v249\n"
  "v_ldexp_f32 v117, v117, v249\n"
  "v_ldexp_f32 v118, v118, v249\n"
  "v_ldexp_f32 v119, v119, v249\n"
  "v_ldexp_f32 v124, v124, v249\n"
  "v_ldexp_f32 v125, v125, v249\n"
  "v_ldexp_f32 v126, v126, v249\n"
  "v_ldexp_f32 v127, v127, v249\n"
  "s_nop 3\n"
  "s_branch .Lback_pro_1_1\n"
  ".Lrare_lp_0_0:\n"
  "s_nop 15\n"
  "v_max3_f32 v248, v192, v193, v194\n"
  "v_max_f32 v248, v248, v195\n"
  "v_mov_b32 v249, v248\n"
  "s_nop 1\n"
  "v_permlane32_swap_b32 v248, v249\n"
  "v_max_f32 v248, v248, v249\n"
  "v_mov_b32 v249, v248\n"
  "s_nop 1\n"
  "v_permlane16_swap_b32 v248, v249\n"
  "v_max_f32 v248, v248, v249\n"
  "v_ceil_f32 v248, v248\n"
  "v_max_f32 v248, 0, v248\n"
  "v_cvt_i32_f32 v249, v248\n"
  "v_sub_u32 v249, 0, v249\n"
  "v_sub_f32 v192, v192, v248\n"
  "v_sub_f32 v193, v193, v248\n"
  "v_sub_f32 v194, v194, v248\n"
  "v_sub_f32 v195, v195, v248\n"
  "v_sub_f32 v208, v208, v248\n"
  "v_sub_f32 v209, v209, v248\n"
  "v_sub_f32 v210, v210, v248\n"
  "v_sub_f32 v211, v211, v248\n"
  "v_sub_f32 v216, v216, v248\n"
  "v_sub_f32 v217, v217, v248\n"
  "v_sub_f32 v218, v218, v248\n"
  "v_sub_f32 v219, v219, v248\n"
  "v_lshlrev_b32 v250, 16, v200\n"
  "v_and_b32 v251, 0xffff0000, v200\n"
  "v_ldexp_f32 v250, v250, v249\n"
  "v_ldexp_f32 v251, v251, v249\n"
  "v_cvt_pk_bf16_f32 v200, v250, v251\n"
  "v_lshlrev_b32 v250, 16, v201\n"
  "v_and_b32 v251, 0xffff0000, v201\n"
  "v_ldexp_f32 v250, v250, v249\n"
  "v_ldexp_f32 v251, v251, v249\n"
  "v_cvt_pk_bf16_f32 v201, v250, v251\n"
  "v_lshlrev_b32 v250, 16, v202\n"
  "v_and_b32 v251, 0xffff0000, v202\n"
  "v_ldexp_f32 v250, v250, v249\n"
  "v_ldexp_f32 v251, v251, v249\n"
  "v_cvt_pk_bf16_f32 v202, v250, v251\n"
  "v_lshlrev_b32 v250, 16, v203\n"
  "v_and_b32 v251, 0xffff0000, v203\n"
  "v_ldexp_f32 v250, v250, v249\n"
  "v_ldexp_f32 v251, v251, v249\n"
  "v_cvt_pk_bf16_f32 v203, v250, v251\n"
  "v_ldexp_f32 v246, v246, v249\n"
  "v_ldexp_f32 v0, v0, v249\n"
  "v_ldexp_f32 v1, v1, v249\n"
  "v_ldexp_f32 v2, v2, v249\n"
  "v_ldexp_f32 v3, v3, v249\n"
  "v_ldexp_f32 v8, v8, v249\n"
  "v_ldexp_f32 v9, v9, v249\n"
  "v_ldexp_f32 v10, v10, v249\n"
  "v_ldexp_f32 v11, v11, v249\n"
  "v_ldexp_f32 v16, v16, v249\n"
  "v_ldexp_f32 v17, v17, v249\n"
  "v_ldexp_f32 v18, v18, v249\n"
  "v_ldexp_f32 v19, v19, v249\n"
  "v_ldexp_f32 v24, v24, v249\n"
  "v_ldexp_f32 v25, v25, v249\n"
  "v_ldexp_f32 v26, v26, v249\n"
  "v_ldexp_f32 v27, v27, v249\n"
  "v_ldexp_f32 v32, v32, v249\n"
  "v_ldexp_f32 v33, v33, v249\n"
  "v_ldexp_f32 v34, v34, v249\n"
  "v_ldexp_f32 v35, v35, v249\n"
  "v_ldexp_f32 v40, v40, v249\n"
  "v_ldexp_f32 v41, v41, v249\n"
  "v_ldexp_f32 v42, v42, v249\n"
  "v_ldexp_f32 v43, v43, v249\n"
  "v_ldexp_f32 v48, v48, v249\n"
  "v_ldexp_f32 v49, v49, v249\n"
  "v_ldexp_f32 v50, v50, v249\n"
  "v_ldexp_f32 v51, v51, v249\n"
  "v_ldexp_f32 v56, v56, v249\n"
  "v_ldexp_f32 v57, v57, v249\n"
  "v_ldexp_f32 v58, v58, v249\n"
  "v_ldexp_f32 v59, v59, v249\n"
  "v_ldexp_f32 v64, v64, v249\n"
  "v_ldexp_f32 v65, v65, v249\n"
  "v_ldexp_f32 v66, v66, v249\n"
  "v_ldexp_f32 v67, v67, v249\n"
  "v_ldexp_f32 v72, v72, v249\n"
  "v_ldexp_f32 v73, v73, v249\n"
  "v_ldexp_f32 v74, v74, v249\n"
  "v_ldexp_f32 v75, v75, v249\n"
  "v_ldexp_f32 v80, v80, v249\n"
  "v_ldexp_f32 v81, v81, v249\n"
  "v_ldexp_f32 v82, v82, v249\n"
  "v_ldexp_f32 v83, v83, v249\n"
  "v_ldexp_f32 v88, v88, v249\n"
  "v_ldexp_f32 v89, v89, v249\n"
  "v_ldexp_f32 v90, v90, v249\n"
  "v_ldexp_f32 v91, v91, v249\n"
  "v_ldexp_f32 v96, v96, v249\n"
  "v_ldexp_f32 v97, v97, v249\n"
  "v_ldexp_f32 v98, v98, v249\n"
  "v_ldexp_f32 v99, v99, v249\n"
  "v_ldexp_f32 v104, v104, v249\n"
  "v_ldexp_f32 v105, v105, v249\n"
  "v_ldexp_f32 v106, v106, v249\n"
  "v_ldexp_f32 v107, v107, v249\n"
  "v_ldexp_f32 v112, v112, v249\n"
  "v_ldexp_f32 v113, v113, v249\n"
  "v_ldexp_f32 v114, v114, v249\n"
  "v_ldexp_f32 v115, v115, v249\n"
  "v_ldexp_f32 v120, v120, v249\n"
  "v_ldexp_f32 v121, v121, v249\n"
  "v_ldexp_f32 v122, v122, v249\n"
  "v_ldexp_f32 v123, v123, v249\n"
  "v_max3_f32 v248, v196, v197, v198\n"
  "v_max_f32 v248, v248, v199\n"
  "v_mov_b32 v249, v248\n"
  "s_nop 1\n"
  "v_permlane32_swap_b32 v248, v249\n"
  "v_max_f32 v248, v248, v249\n"
  "v_mov_b32 v249, v248\n"
  "s_nop 1\n"
  "v_permlane16_swap_b32 v248, v249\n"
  "v_max_f32 v248, v248, v249\n"
  "v_ceil_f32 v248, v248\n"
  "v_max_f32 v248, 0, v248\n"
  "v_cvt_i32_f32 v249, v248\n"
  "v_sub_u32 v249, 0, v249\n"
  "v_sub_f32 v196, v196, v248\n"
  "v_sub_f32 v197, v197, v248\n"
  "v_sub_f32 v198, v198, v248\n"
  "v_sub_f32 v199, v199, v248\n"
  "v_sub_f32 v212, v212, v248\n"
  "v_sub_f32 v213, v213, v248\n"
  "v_sub_f32 v214, v214, v248\n"
  "v_sub_f32 v215, v215, v248\n"
  "v_sub_f32 v220, v220, v248\n"
  "v_sub_f32 v221, v221, v248\n"
  "v_sub_f32 v222, v222, v248\n"
  "v_sub_f32 v223, v223, v248\n"
  "v_lshlrev_b32 v250, 16, v204\n"
  "v_and_b32 v251, 0xffff0000, v204\n"
  "v_ldexp_f32 v250, v250, v249\n"
  "v_ldexp_f32 v251, v251, v249\n"
  "v_cvt_pk_bf16_f32 v204, v250, v251\n"
  "v_lshlrev_b32 v250, 16, v205\n"
  "v_and_b32 v251, 0xffff0000, v205\n"
  "v_ldexp_f32 v250, v250, v249\n"
  "v_ldexp_f32 v251, v251, v249\n"
  "v_cvt_pk_bf16_f32 v205, v250, v251\n"
  "v_lshlrev_b32 v250, 16, v206\n"
  "v_and_b32 v251, 0xffff0000, v206\n"
  "v_ldexp_f32 v250, v250, v249\n"
  "v_ldexp_f32 v251, v251, v249\n"
  "v_cvt_pk_bf16_f32 v206, v250, v251\n"
  "v_lshlrev_b32 v250, 16, v207\n"
  "v_and_b32 v251, 0xffff0000, v207\n"
  "v_ldexp_f32 v250, v250, v249\n"
  "v_ldexp_f32 v251, v251, v249\n"
  "v_cvt_pk_bf16_f32 v207, v250, v251\n"
  "v_ldexp_f32 v247, v247, v249\n"
  "v_ldexp_f32 v4, v4, v249\n"
  "v_ldexp_f32 v5, v5, v249\n"
  "v_ldexp_f32 v6, v6, v249\n"
  "v_ldexp_f32 v7, v7, v249\n"
  "v_ldexp_f32 v12, v12, v249\n"
  "v_ldexp_f32 v13, v13, v249\n"
  "v_ldexp_f32 v14, v14, v249\n"
  "v_ldexp_f32 v15, v15, v249\n"
  "v_ldexp_f32 v20, v20, v249\n"
  "v_ldexp_f32 v21, v21, v249\n"
  "v_ldexp_f32 v22, v22, v249\n"
  "v_ldexp_f32 v23, v23, v249\n"
  "v_ldexp_f32 v28, v28, v249\n"
  "v_ldexp_f32 v29, v29, v249\n"
  "v_ldexp_f32 v30, v30, v249\n"
  "v_ldexp_f32 v31, v31, v249\n"
  "v_ldexp_f32 v36, v36, v249\n"
  "v_ldexp_f32 v37, v37, v249\n"
  "v_ldexp_f32 v38, v38, v249\n"
  "v_ldexp_f32 v39, v39, v249\n"
  "v_ldexp_f32 v44, v44, v249\n"
  "v_ldexp_f32 v45, v45, v249\n"
  "v_ldexp_f32 v46, v46, v249\n"
  "v_ldexp_f32 v47, v47, v249\n"
  "v_ldexp_f32 v52, v52, v249\n"
  "v_ldexp_f32 v53, v53, v249\n"
  "v_ldexp_f32 v54, v54, v249\n"
  "v_ldexp_f32 v55, v55, v249\n"
  "v_ldexp_f32 v60, v60, v249\n"
  "v_ldexp_f32 v61, v61, v249\n"
  "v_ldexp_f32 v62, v62, v249\n"
  "v_ldexp_f32 v63, v63, v249\n"
  "v_ldexp_f32 v68, v68, v249\n"
  "v_ldexp_f32 v69, v69, v249\n"
  "v_ldexp_f32 v70, v70, v249\n"
  "v_ldexp_f32 v71, v71, v249\n"
  "v_ldexp_f32 v76, v76, v249\n"
  "v_ldexp_f32 v77, v77, v249\n"
  "v_ldexp_f32 v78, v78, v249\n"
  "v_ldexp_f32 v79, v79, v249\n"
  "v_ldexp_f32 v84, v84, v249\n"
  "v_ldexp_f32 v85, v85, v249\n"
  "v_ldexp_f32 v86, v86, v249\n"
  "v_ldexp_f32 v87, v87, v249\n"
  "v_ldexp_f32 v92, v92, v249\n"
  "v_ldexp_f32 v93, v93, v249\n"
  "v_ldexp_f32 v94, v94, v249\n"
  "v_ldexp_f32 v95, v95, v249\n"
  "v_ldexp_f32 v100, v100, v249\n"
  "v_ldexp_f32 v101, v101, v249\n"
  "v_ldexp_f32 v102, v102, v249\n"
  "v_ldexp_f32 v103, v103, v249\n"
  "v_ldexp_f32 v108, v108, v249\n"
  "v_ldexp_f32 v109, v109, v249\n"
  "v_ldexp_f32 v110, v110, v249\n"
  "v_ldexp_f32 v111, v111, v249\n"
  "v_ldexp_f32 v116, v116, v249\n"
  "v_ldexp_f32 v117, v117, v249\n"
  "v_ldexp_f32 v118, v118, v249\n"
  "v_ldexp_f32 v119, v119, v249\n"
  "v_ldexp_f32 v124, v124, v249\n"
  "v_ldexp_f32 v125, v125, v249\n"
  "v_ldexp_f32 v126, v126, v249\n"
  "v_ldexp_f32 v127, v127, v249\n"
  "s_nop 3\n"
  "s_branch .Lback_lp_0_0\n"
  ".Lrare_lp_0_1:\n"
  "s_nop 15\n"
  "v_max3_f32 v248, v208, v209, v210\n"
  "v_max_f32 v248, v248, v211\n"
  "v_mov_b32 v249, v248\n"
  "s_nop 1\n"
  "v_permlane32_swap_b32 v248, v249\n"
  "v_max_f32 v248, v248, v249\n"
  "v_mov_b32 v249, v248\n"
  "s_nop 1\n"
  "v_permlane16_swap_b32 v248, v249\n"
  "v_max_f32 v248, v248, v249\n"
  "v_ceil_f32 v248, v248\n"
  "v_max_f32 v248, 0, v248\n"
  "v_cvt_i32_f32 v249, v248\n"
  "v_sub_u32 v249, 0, v249\n"
  "v_sub_f32 v208, v208, v248\n"
  "v_sub_f32 v209, v209, v248\n"
  "v_sub_f32 v210, v210, v248\n"
  "v_sub_f32 v211, v211, v248\n"
  "v_sub_f32 v216, v216, v248\n"
  "v_sub_f32 v217, v217, v248\n"
  "v_sub_f32 v218, v218, v248\n"
  "v_sub_f32 v219, v219, v248\n"
  "v_lshlrev_b32 v250, 16, v200\n"
  "v_and_b32 v251, 0xffff0000, v200\n"
  "v_ldexp_f32 v250, v250, v249\n"
  "v_ldexp_f32 v251, v251, v249\n"
  "v_cvt_pk_bf16_f32 v200, v250, v251\n"
  "v_lshlrev_b32 v250, 16, v201\n"
  "v_and_b32 v251, 0xffff0000, v201\n"
  "v_ldexp_f32 v250, v250, v249\n"
  "v_ldexp_f32 v251, v251, v249\n"
  "v_cvt_pk_bf16_f32 v201, v250, v251\n"
  "v_lshlrev_b32 v250, 16, v202\n"
  "v_and_b32 v251, 0xffff0000, v202\n"
  "v_ldexp_f32 v250, v250, v249\n"
  "v_ldexp_f32 v251, v251, v249\n"
  "v_cvt_pk_bf16_f32 v202, v250, v251\n"
  "v_lshlrev_b32 v250, 16, v203\n"
  "v_and_b32 v251, 0xffff0000, v203\n"
  "v_ldexp_f32 v250, v250, v249\n"
  "v_ldexp_f32 v251, v251, v249\n"
  "v_cvt_pk_bf16_f32 v203, v250, v251\n"
  "v_lshlrev_b32 v250, 16, v192\n"
  "v_and_b32 v251, 0xffff0000, v192\n"
  "v_ldexp_f32 v250, v250, v249\n"
  "v_ldexp_f32 v251, v251, v249\n"
  "v_cvt_pk_bf16_f32 v192, v250, v251\n"
  "v_lshlrev_b32 v250, 16, v193\n"
  "v_and_b32 v251, 0xffff0000, v193\n"
  "v_ldexp_f32 v250, v250, v249\n"
  "v_ldexp_f32 v251, v251, v249\n"
  "v_cvt_pk_bf16_f32 v193, v250, v251\n"
  "v_ldexp_f32 v246, v246, v249\n"
  "v_ldexp_f32 v0, v0, v249\n"
  "v_ldexp_f32 v1, v1, v249\n"
  "v_ldexp_f32 v2, v2, v249\n"
  "v_ldexp_f32 v3, v3, v249\n"
  "v_ldexp_f32 v8, v8, v249\n"
  "v_ldexp_f32 v9, v9, v249\n"
  "v_ldexp_f32 v10, v10, v249\n"
  "v_ldexp_f32 v11, v11, v249\n"
  "v_ldexp_f32 v16, v16, v249\n"
  "v_ldexp_f32 v17, v17, v249\n"
  "v_ldexp_f32 v18, v18, v249\n"
  "v_ldexp_f32 v19, v19, v249\n"
  "v_ldexp_f32 v24, v24, v249\n"
  "v_ldexp_f32 v25, v25, v249\n"
  "v_ldexp_f32 v26, v26, v249\n"
  "v_ldexp_f32 v27, v27, v249\n"
  "v_ldexp_f32 v32, v32, v249\n"
  "v_ldexp_f32 v33, v33, v249\n"
  "v_ldexp_f32 v34, v34, v249\n"
  "v_ldexp_f32 v35, v35, v249\n"
  "v_ldexp_f32 v40, v40, v249\n"
  "v_ldexp_f32 v41, v41, v249\n"
  "v_ldexp_f32 v42, v42, v249\n"
  "v_ldexp_f32 v43, v43, v249\n"
  "v_ldexp_f32 v48, v48, v249\n"
  "v_ldexp_f32 v49, v49, v249\n"
  "v_ldexp_f32 v50, v50, v249\n"
  "v_ldexp_f32 v51, v51, v249\n"
  "v_ldexp_f32 v56, v56, v249\n"
  "v_ldexp_f32 v57, v57, v249\n"
  "v_ldexp_f32 v58, v58, v249\n"
  "v_ldexp_f32 v59, v59, v249\n"
  "v_ldexp_f32 v64, v64, v249\n"
  "v_ldexp_f32 v65, v65, v249\n"
  "v_ldexp_f32 v66, v66, v249\n"
  "v_ldexp_f32 v67, v67, v249\n"
  "v_ldexp_f32 v72, v72, v249\n"
  "v_ldexp_f32 v73, v73, v249\n"
  "v_ldexp_f32 v74, v74, v249\n"
  "v_ldexp_f32 v75, v75, v249\n"
  "v_ldexp_f32 v80, v80, v249\n"
  "v_ldexp_f32 v81, v81, v249\n"
  "v_ldexp_f32 v82, v82, v249\n"
  "v_ldexp_f32 v83, v83, v249\n"
  "v_ldexp_f32 v88, v88, v249\n"
  "v_ldexp_f32 v89, v89, v249\n"
  "v_ldexp_f32 v90, v90, v249\n"
  "v_ldexp_f32 v91, v91, v249\n"
  "v_ldexp_f32 v96, v96, v249\n"
  "v_ldexp_f32 v97, v97, v249\n"
  "v_ldexp_f32 v98, v98, v249\n"
  "v_ldexp_f32 v99, v99, v249\n"
  "v_ldexp_f32 v104, v104, v249\n"
  "v_ldexp_f32 v105, v105, v249\n"
  "v_ldexp_f32 v106, v106, v249\n"
  "v_ldexp_f32 v107, v107, v249\n"
  "v_ldexp_f32 v112, v112, v249\n"
  "v_ldexp_f32 v113, v113, v249\n"
  "v_ldexp_f32 v114, v114, v249\n"
  "v_ldexp_f32 v115, v115, v249\n"
  "v_ldexp_f32 v120, v120, v249\n"
  "v_ldexp_f32 v121, v121, v249\n"
  "v_ldexp_f32 v122, v122, v249\n"
  "v_ldexp_f32 v123, v123, v249\n"
  "v_max3_f32 v248, v212, v213, v214\n"
  "v_max_f32 v248, v248, v215\n"
  "v_mov_b32 v249, v248\n"
  "s_nop 1\n"
  "v_permlane32_swap_b32 v248, v249\n"
  "v_max_f32 v248, v248, v249\n"
  "v_mov_b32 v249, v248\n"
  "s_nop 1\n"
  "v_permlane16_swap_b32 v248, v249\n"
  "v_max_f32 v248, v248, v249\n"
  "v_ceil_f32 v248, v248\n"
  "v_max_f32 v248, 0, v248\n"
  "v_cvt_i32_f32 v249, v248\n"
  "v_sub_u32 v249, 0, v249\n"
  "v_sub_f32 v212, v212, v248\n"
  "v_sub_f32 v213, v213, v248\n"
  "v_sub_f32 v214, v214, v248\n"
  "v_sub_f32 v215, v215, v248\n"
  "v_sub_f32 v220, v220, v248\n"
  "v_sub_f32 v221, v221, v248\n"
  "v_sub_f32 v222, v222, v248\n"
  "v_sub_f32 v223, v223, v248\n"
  "v_lshlrev_b32 v250, 16, v204\n"
  "v_and_b32 v251, 0xffff0000, v204\n"
  "v_ldexp_f32 v250, v250, v249\n"
  "v_ldexp_f32 v251, v251, v249\n"
  "v_cvt_pk_bf16_f32 v204, v250, v251\n"
  "v_lshlrev_b32 v250, 16, v205\n"
  "v_and_b32 v251, 0xffff0000, v205\n"
  "v_ldexp_f32 v250, v250, v249\n"
  "v_ldexp_f32 v251, v251, v249\n"
  "v_cvt_pk_bf16_f32 v205, v250, v251\n"
  "v_lshlrev_b32 v250, 16, v206\n"
  "v_and_b32 v251, 0xffff0000, v206\n"
  "v_ldexp_f32 v250, v250, v249\n"
  "v_ldexp_f32 v251, v251, v249\n"
  "v_cvt_pk_bf16_f32 v206, v250, v251\n"
  "v_lshlrev_b32 v250, 16, v207\n"
  "v_and_b32 v251, 0xffff0000, v207\n"
  "v_ldexp_f32 v250, v250, v249\n"
  "v_ldexp_f32 v251, v251, v249\n"
  "v_cvt_pk_bf16_f32 v207, v250, v251\n"
  "v_lshlrev_b32 v250, 16, v196\n"
  "v_and_b32 v251, 0xffff0000, v196\n"
  "v_ldexp_f32 v250, v250, v249\n"
  "v_ldexp_f32 v251, v251, v249\n"
  "v_cvt_pk_bf16_f32 v196, v250, v251\n"
  "v_lshlrev_b32 v250, 16, v197\n"
  "v_and_b32 v251, 0xffff0000, v197\n"
  "v_ldexp_f32 v250, v250, v249\n"
  "v_ldexp_f32 v251, v251, v249\n"
  "v_cvt_pk_bf16_f32 v197, v250, v251\n"
  "v_ldexp_f32 v247, v247, v249\n"
  "v_ldexp_f32 v4, v4, v249\n"
  "v_ldexp_f32 v5, v5, v249\n"
  "v_ldexp_f32 v6, v6, v249\n"
  "v_ldexp_f32 v7, v7, v249\n"
  "v_ldexp_f32 v12, v12, v249\n"
  "v_ldexp_f32 v13, v13, v249\n"
  "v_ldexp_f32 v14, v14, v249\n"
  "v_ldexp_f32 v15, v15, v249\n"
  "v_ldexp_f32 v20, v20, v249\n"
  "v_ldexp_f32 v21, v21, v249\n"
  "v_ldexp_f32 v22, v22, v249\n"
  "v_ldexp_f32 v23, v23, v249\n"
  "v_ldexp_f32 v28, v28, v249\n"
  "v_ldexp_f32 v29, v29, v249\n"
  "v_ldexp_f32 v30, v30, v249\n"
  "v_ldexp_f32 v31, v31, v249\n"
  "v_ldexp_f32 v36, v36, v249\n"
  "v_ldexp_f32 v37, v37, v249\n"
  "v_ldexp_f32 v38, v38, v249\n"
  "v_ldexp_f32 v39, v39, v249\n"
  "v_ldexp_f32 v44, v44, v249\n"
  "v_ldexp_f32 v45, v45, v249\n"
  "v_ldexp_f32 v46, v46, v249\n"
  "v_ldexp_f32 v47, v47, v249\n"
  "v_ldexp_f32 v52, v52, v249\n"
  "v_ldexp_f32 v53, v53, v249\n"
  "v_ldexp_f32 v54, v54, v249\n"
  "v_ldexp_f32 v55, v55, v249\n"
  "v_ldexp_f32 v60, v60, v249\n"
  "v_ldexp_f32 v61, v61, v249\n"
  "v_ldexp_f32 v62, v62, v249\n"
  "v_ldexp_f32 v63, v63, v249\n"
  "v_ldexp_f32 v68, v68, v249\n"
  "v_ldexp_f32 v69, v69, v249\n"
  "v_ldexp_f32 v70, v70, v249\n"
  "v_ldexp_f32 v71, v71, v249\n"
  "v_ldexp_f32 v76, v76, v249\n"
  "v_ldexp_f32 v77, v77, v249\n"
  "v_ldexp_f32 v78, v78, v249\n"
  "v_ldexp_f32 v79, v79, v249\n"
  "v_ldexp_f32 v84, v84, v249\n"
  "v_ldexp_f32 v85, v85, v249\n"
  "v_ldexp_f32 v86, v86, v249\n"
  "v_ldexp_f32 v87, v87, v249\n"
  "v_ldexp_f32 v92, v92, v249\n"
  "v_ldexp_f32 v93, v93, v249\n"
  "v_ldexp_f32 v94, v94, v249\n"
  "v_ldexp_f32 v95, v95, v249\n"
  "v_ldexp_f32 v100, v100, v249\n"
  "v_ldexp_f32 v101, v101, v249\n"
  "v_ldexp_f32 v102, v102, v249\n"
  "v_ldexp_f32 v103, v103, v249\n"
  "v_ldexp_f32 v108, v108, v249\n"
  "v_ldexp_f32 v109, v109, v249\n"
  "v_ldexp_f32 v110, v110, v249\n"
  "v_ldexp_f32 v111, v111, v249\n"
  "v_ldexp_f32 v116, v116, v249\n"
  "v_ldexp_f32 v117, v117, v249\n"
  "v_ldexp_f32 v118, v118, v249\n"
  "v_ldexp_f32 v119, v119, v249\n"
  "v_ldexp_f32 v124, v124, v249\n"
  "v_ldexp_f32 v125, v125, v249\n"
  "v_ldexp_f32 v126, v126, v249\n"
  "v_ldexp_f32 v127, v127, v249\n"
  "s_nop 3\n"
  "s_branch .Lback_lp_0_1\n"
  ".Lrare_lp_1_0:\n"
  "s_nop 15\n"
  "v_max3_f32 v248, v200, v201, v202\n"
  "v_max_f32 v248, v248, v203\n"
  "v_mov_b32 v249, v248\n"
  "s_nop 1\n"
  "v_permlane32_swap_b32 v248, v249\n"
  "v_max_f32 v248, v248, v249\n"
  "v_mov_b32 v249, v248\n"
  "s_nop 1\n"
  "v_permlane16_swap_b32 v248, v249\n"
  "v_max_f32 v248, v248, v249\n"
  "v_ceil_f32 v248, v248\n"
  "v_max_f32 v248, 0, v248\n"
  "v_cvt_i32_f32 v249, v248\n"
  "v_sub_u32 v249, 0, v249\n"
  "v_sub_f32 v200, v200, v248\n"
  "v_sub_f32 v201, v201, v248\n"
  "v_sub_f32 v202, v202, v248\n"
  "v_sub_f32 v203, v203, v248\n"
  "v_sub_f32 v208, v208, v248\n"
  "v_sub_f32 v209, v209, v248\n"
  "v_sub_f32 v210, v210, v248\n"
  "v_sub_f32 v211, v211, v248\n"
  "v_sub_f32 v216, v216, v248\n"
  "v_sub_f32 v217, v217, v248\n"
  "v_sub_f32 v218, v218, v248\n"
  "v_sub_f32 v219, v219, v248\n"
  "v_lshlrev_b32 v250, 16, v192\n"
  "v_and_b32 v251, 0xffff0000, v192\n"
  "v_ldexp_f32 v250, v250, v249\n"
  "v_ldexp_f32 v251, v251, v249\n"
  "v_cvt_pk_bf16_f32 v192, v250, v251\n"
  "v_lshlrev_b32 v250, 16, v193\n"
  "v_and_b32 v251, 0xffff0000, v193\n"
  "v_ldexp_f32 v250, v250, v249\n"
  "v_ldexp_f32 v251, v251, v249\n"
  "v_cvt_pk_bf16_f32 v193, v250, v251\n"
  "v_lshlrev_b32 v250, 16, v194\n"
  "v_and_b32 v251, 0xffff0000, v194\n"
  "v_ldexp_f32 v250, v250, v249\n"
  "v_ldexp_f32 v251, v251, v249\n"
  "v_cvt_pk_bf16_f32 v194, v250, v251\n"
  "v_lshlrev_b32 v250, 16, v195\n"
  "v_and_b32 v251, 0xffff0000, v195\n"
  "v_ldexp_f32 v250, v250, v249\n"
  "v_ldexp_f32 v251, v251, v249\n"
  "v_cvt_pk_bf16_f32 v195, v250, v251\n"
  "v_ldexp_f32 v246, v246, v249\n"
  "v_ldexp_f32 v0, v0, v249\n"
  "v_ldexp_f32 v1, v1, v249\n"
  "v_ldexp_f32 v2, v2, v249\n"
  "v_ldexp_f32 v3, v3, v249\n"
  "v_ldexp_f32 v8, v8, v249\n"
  "v_ldexp_f32 v9, v9, v249\n"
  "v_ldexp_f32 v10, v10, v249\n"
  "v_ldexp_f32 v11, v11, v249\n"
  "v_ldexp_f32 v16, v16, v249\n"
  "v_ldexp_f32 v17, v17, v249\n"
  "v_ldexp_f32 v18, v18, v249\n"
  "v_ldexp_f32 v19, v19, v249\n"
  "v_ldexp_f32 v24, v24, v249\n"
  "v_ldexp_f32 v25, v25, v249\n"
  "v_ldexp_f32 v26, v26, v249\n"
  "v_ldexp_f32 v27, v27, v249\n"
  "v_ldexp_f32 v32, v32, v249\n"
  "v_ldexp_f32 v33, v33, v249\n"
  "v_ldexp_f32 v34, v34, v249\n"
  "v_ldexp_f32 v35, v35, v249\n"
  "v_ldexp_f32 v40, v40, v249\n"
  "v_ldexp_f32 v41, v41, v249\n"
  "v_ldexp_f32 v42, v42, v249\n"
  "v_ldexp_f32 v43, v43, v249\n"
  "v_ldexp_f32 v48, v48, v249\n"
  "v_ldexp_f32 v49, v49, v249\n"
  "v_ldexp_f32 v50, v50, v249\n"
  "v_ldexp_f32 v51, v51, v249\n"
  "v_ldexp_f32 v56, v56, v249\n"
  "v_ldexp_f32 v57, v57, v249\n"
  "v_ldexp_f32 v58, v58, v249\n"
  "v_ldexp_f32 v59, v59, v249\n"
  "v_ldexp_f32 v64, v64, v249\n"
  "v_ldexp_f32 v65, v65, v249\n"
  "v_ldexp_f32 v66, v66, v249\n"
  "v_ldexp_f32 v67, v67, v249\n"
  "v_ldexp_f32 v72, v72, v249\n"
  "v_ldexp_f32 v73, v73, v249\n"
  "v_ldexp_f32 v74, v74, v249\n"
  "v_ldexp_f32 v75, v75, v249\n"
  "v_ldexp_f32 v80, v80, v249\n"
  "v_ldexp_f32 v81, v81, v249\n"
  "v_ldexp_f32 v82, v82, v249\n"
  "v_ldexp_f32 v83, v83, v249\n"
  "v_ldexp_f32 v88, v88, v249\n"
  "v_ldexp_f32 v89, v89, v249\n"
  "v_ldexp_f32 v90, v90, v249\n"
  "v_ldexp_f32 v91, v91, v249\n"
  "v_ldexp_f32 v96, v96, v249\n"
  "v_ldexp_f32 v97, v97, v249\n"
  "v_ldexp_f32 v98, v98, v249\n"
  "v_ldexp_f32 v99, v99, v249\n"
  "v_ldexp_f32 v104, v104, v249\n"
  "v_ldexp_f32 v105, v105, v249\n"
  "v_ldexp_f32 v106, v106, v249\n"
  "v_ldexp_f32 v107, v107, v249\n"
  "v_ldexp_f32 v112, v112, v249\n"
  "v_ldexp_f32 v113, v113, v249\n"
  "v_ldexp_f32 v114, v114, v249\n"
  "v_ldexp_f32 v115, v115, v249\n"
  "v_ldexp_f32 v120, v120, v249\n"
  "v_ldexp_f32 v121, v121, v249\n"
  "v_ldexp_f32 v122, v122, v249\n"
  "v_ldexp_f32 v123, v123, v249\n"
  "v_max3_f32 v248, v204, v205, v206\n"
  "v_max_f32 v248, v248, v207\n"
  "v_mov_b32 v249, v248\n"
  "s_nop 1\n"
  "v_permlane32_swap_b32 v248, v249\n"
  "v_max_f32 v248, v248, v249\n"
  "v_mov_b32 v249, v248\n"
  "s_nop 1\n"
  "v_permlane16_swap_b32 v248, v249\n"
  "v_max_f32 v248, v248, v249\n"
  "v_ceil_f32 v248, v248\n"
  "v_max_f32 v248, 0, v248\n"
  "v_cvt_i32_f32 v249, v248\n"
  "v_sub_u32 v249, 0, v249\n"
  "v_sub_f32 v204, v204, v248\n"
  "v_sub_f32 v205, v205, v248\n"
  "v_sub_f32 v206, v206, v248\n"
  "v_sub_f32 v207, v207, v248\n"
  "v_sub_f32 v212, v212, v248\n"
  "v_sub_f32 v213, v213, v248\n"
  "v_sub_f32 v214, v214, v248\n"
  "v_sub_f32 v215, v215, v248\n"
  "v_sub_f32 v220, v220, v248\n"
  "v_sub_f32 v221, v221, v248\n"
  "v_sub_f32 v222, v222, v248\n"
  "v_sub_f32 v223, v223, v248\n"
  "v_lshlrev_b32 v250, 16, v196\n"
  "v_and_b32 v251, 0xffff0000, v196\n"
  "v_ldexp_f32 v250, v250, v249\n"
  "v_ldexp_f32 v251, v251, v249\n"
  "v_cvt_pk_bf16_f32 v196, v250, v251\n"
  "v_lshlrev_b32 v250, 16, v197\n"
  "v_and_b32 v251, 0xffff0000, v197\n"
  "v_ldexp_f32 v250, v250, v249\n"
  "v_ldexp_f32 v251, v251, v249\n"
  "v_cvt_pk_bf16_f32 v197, v250, v251\n"
  "v_lshlrev_b32 v250, 16, v198\n"
  "v_and_b32 v251, 0xffff0000, v198\n"
  "v_ldexp_f32 v250, v250, v249\n"
  "v_ldexp_f32 v251, v251, v249\n"
  "v_cvt_pk_bf16_f32 v198, v250, v251\n"
  "v_lshlrev_b32 v250, 16, v199\n"
  "v_and_b32 v251, 0xffff0000, v199\n"
  "v_ldexp_f32 v250, v250, v249\n"
  "v_ldexp_f32 v251, v251, v249\n"
  "v_cvt_pk_bf16_f32 v199, v250, v251\n"
  "v_ldexp_f32 v247, v247, v249\n"
  "v_ldexp_f32 v4, v4, v249\n"
  "v_ldexp_f32 v5, v5, v249\n"
  "v_ldexp_f32 v6, v6, v249\n"
  "v_ldexp_f32 v7, v7, v249\n"
  "v_ldexp_f32 v12, v12, v249\n"
  "v_ldexp_f32 v13, v13, v249\n"
  "v_ldexp_f32 v14, v14, v249\n"
  "v_ldexp_f32 v15, v15, v249\n"
  "v_ldexp_f32 v20, v20, v249\n"
  "v_ldexp_f32 v21, v21, v249\n"
  "v_ldexp_f32 v22, v22, v249\n"
  "v_ldexp_f32 v23, v23, v249\n"
  "v_ldexp_f32 v28, v28, v249\n"
  "v_ldexp_f32 v29, v29, v249\n"
  "v_ldexp_f32 v30, v30, v249\n"
  "v_ldexp_f32 v31, v31, v249\n"
  "v_ldexp_f32 v36, v36, v249\n"
  "v_ldexp_f32 v37, v37, v249\n"
  "v_ldexp_f32 v38, v38, v249\n"
  "v_ldexp_f32 v39, v39, v249\n"
  "v_ldexp_f32 v44, v44, v249\n"
  "v_ldexp_f32 v45, v45, v249\n"
  "v_ldexp_f32 v46, v46, v249\n"
  "v_ldexp_f32 v47, v47, v249\n"
  "v_ldexp_f32 v52, v52, v249\n"
  "v_ldexp_f32 v53, v53, v249\n"
  "v_ldexp_f32 v54, v54, v249\n"
  "v_ldexp_f32 v55, v55, v249\n"
  "v_ldexp_f32 v60, v60, v249\n"
  "v_ldexp_f32 v61, v61, v249\n"
  "v_ldexp_f32 v62, v62, v249\n"
  "v_ldexp_f32 v63, v63, v249\n"
  "v_ldexp_f32 v68, v68, v249\n"
  "v_ldexp_f32 v69, v69, v249\n"
  "v_ldexp_f32 v70, v70, v249\n"
  "v_ldexp_f32 v71, v71, v249\n"
  "v_ldexp_f32 v76, v76, v249\n"
  "v_ldexp_f32 v77, v77, v249\n"
  "v_ldexp_f32 v78, v78, v249\n"
  "v_ldexp_f32 v79, v79, v249\n"
  "v_ldexp_f32 v84, v84, v249\n"
  "v_ldexp_f32 v85, v85, v249\n"
  "v_ldexp_f32 v86, v86, v249\n"
  "v_ldexp_f32 v87, v87, v249\n"
  "v_ldexp_f32 v92, v92, v249\n"
  "v_ldexp_f32 v93, v93, v249\n"
  "v_ldexp_f32 v94, v94, v249\n"
  "v_ldexp_f32 v95, v95, v249\n"
  "v_ldexp_f32 v100, v100, v249\n"
  "v_ldexp_f32 v101, v101, v249\n"
  "v_ldexp_f32 v102, v102, v249\n"
  "v_ldexp_f32 v103, v103, v249\n"
  "v_ldexp_f32 v108, v108, v249\n"
  "v_ldexp_f32 v109, v109, v249\n"
  "v_ldexp_f32 v110, v110, v249\n"
  "v_ldexp_f32 v111, v111, v249\n"
  "v_ldexp_f32 v116, v116, v249\n"
  "v_ldexp_f32 v117, v117, v249\n"
  "v_ldexp_f32 v118, v118, v249\n"
  "v_ldexp_f32 v119, v119, v249\n"
  "v_ldexp_f32 v124, v124, v249\n"
  "v_ldexp_f32 v125, v125, v249\n"
  "v_ldexp_f32 v126, v126, v249\n"
  "v_ldexp_f32 v127, v127, v249\n"
  "s_nop 3\n"
  "s_branch .Lback_lp_1_0\n"
  ".Lrare_lp_1_1:\n"
  "s_nop 15\n"
  "v_max3_f32 v248, v208, v209, v210\n"
  "v_max_f32 v248, v248, v211\n"
  "v_mov_b32 v249, v248\n"
  "s_nop 1\n"
  "v_permlane32_swap_b32 v248, v249\n"
  "v_max_f32 v248, v248, v249\n"
  "v_mov_b32 v249, v248\n"
  "s_nop 1\n"
  "v_permlane16_swap_b32 v248, v249\n"
  "v_max_f32 v248, v248, v249\n"
  "v_ceil_f32 v248, v248\n"
  "v_max_f32 v248, 0, v248\n"
  "v_cvt_i32_f32 v249, v248\n"
  "v_sub_u32 v249, 0, v249\n"
  "v_sub_f32 v208, v208, v248\n"
  "v_sub_f32 v209, v209, v248\n"
  "v_sub_f32 v210, v210, v248\n"
  "v_sub_f32 v211, v211, v248\n"
  "v_sub_f32 v216, v216, v248\n"
  "v_sub_f32 v217, v217, v248\n"
  "v_sub_f32 v218, v218, v248\n"
  "v_sub_f32 v219, v219, v248\n"
  "v_lshlrev_b32 v250, 16, v192\n"
  "v_and_b32 v251, 0xffff0000, v192\n"
  "v_ldexp_f32 v250, v250, v249\n"
  "v_ldexp_f32 v251, v251, v249\n"
  "v_cvt_pk_bf16_f32 v192, v250, v251\n"
  "v_lshlrev_b32 v250, 16, v193\n"
  "v_and_b32 v251, 0xffff0000, v193\n"
  "v_ldexp_f32 v250, v250, v249\n"
  "v_ldexp_f32 v251, v251, v249\n"
  "v_cvt_pk_bf16_f32 v193, v250, v251\n"
  "v_lshlrev_b32 v250, 16, v194\n"
  "v_and_b32 v251, 0xffff0000, v194\n"
  "v_ldexp_f32 v250, v250, v249\n"
  "v_ldexp_f32 v251, v251, v249\n"
  "v_cvt_pk_bf16_f32 v194, v250, v251\n"
  "v_lshlrev_b32 v250, 16, v195\n"
  "v_and_b32 v251, 0xffff0000, v195\n"
  "v_ldexp_f32 v250, v250, v249\n"
  "v_ldexp_f32 v251, v251, v249\n"
  "v_cvt_pk_bf16_f32 v195, v250, v251\n"
  "v_lshlrev_b32 v250, 16, v200\n"
  "v_and_b32 v251, 0xffff0000, v200\n"
  "v_ldexp_f32 v250, v250, v249\n"
  "v_ldexp_f32 v251, v251, v249\n"
  "v_cvt_pk_bf16_f32 v200, v250, v251\n"
  "v_lshlrev_b32 v250, 16, v201\n"
  "v_and_b32 v251, 0xffff0000, v201\n"
  "v_ldexp_f32 v250, v250, v249\n"
  "v_ldexp_f32 v251, v251, v249\n"
  "v_cvt_pk_bf16_f32 v201, v250, v251\n"
  "v_ldexp_f32 v246, v246, v249\n"
  "v_ldexp_f32 v0, v0, v249\n"
  "v_ldexp_f32 v1, v1, v249\n"
  "v_ldexp_f32 v2, v2, v249\n"
  "v_ldexp_f32 v3, v3, v249\n"
  "v_ldexp_f32 v8, v8, v249\n"
  "v_ldexp_f32 v9, v9, v249\n"
  "v_ldexp_f32 v10, v10, v249\n"
  "v_ldexp_f32 v11, v11, v249\n"
  "v_ldexp_f32 v16, v16, v249\n"
  "v_ldexp_f32 v17, v17, v249\n"
  "v_ldexp_f32 v18, v18, v249\n"
  "v_ldexp_f32 v19, v19, v249\n"
  "v_ldexp_f32 v24, v24, v249\n"
  "v_ldexp_f32 v25, v25, v249\n"
  "v_ldexp_f32 v26, v26, v249\n"
  "v_ldexp_f32 v27, v27, v249\n"
  "v_ldexp_f32 v32, v32, v249\n"
  "v_ldexp_f32 v33, v33, v249\n"
  "v_ldexp_f32 v34, v34, v249\n"
  "v_ldexp_f32 v35, v35, v249\n"
  "v_ldexp_f32 v40, v40, v249\n"
  "v_ldexp_f32 v41, v41, v249\n"
  "v_ldexp_f32 v42, v42, v249\n"
  "v_ldexp_f32 v43, v43, v249\n"
  "v_ldexp_f32 v48, v48, v249\n"
  "v_ldexp_f32 v49, v49, v249\n"
  "v_ldexp_f32 v50, v50, v249\n"
  "v_ldexp_f32 v51, v51, v249\n"
  "v_ldexp_f32 v56, v56, v249\n"
  "v_ldexp_f32 v57, v57, v249\n"
  "v_ldexp_f32 v58, v58, v249\n"
  "v_ldexp_f32 v59, v59, v249\n"
  "v_ldexp_f32 v64, v64, v249\n"
  "v_ldexp_f32 v65, v65, v249\n"
  "v_ldexp_f32 v66, v66, v249\n"
  "v_ldexp_f32 v67, v67, v249\n"
  "v_ldexp_f32 v72, v72, v249\n"
  "v_ldexp_f32 v73, v73, v249\n"
  "v_ldexp_f32 v74, v74, v249\n"
  "v_ldexp_f32 v75, v75, v249\n"
  "v_ldexp_f32 v80, v80, v249\n"
  "v_ldexp_f32 v81, v81, v249\n"
  "v_ldexp_f32 v82, v82, v249\n"
  "v_ldexp_f32 v83, v83, v249\n"
  "v_ldexp_f32 v88, v88, v249\n"
  "v_ldexp_f32 v89, v89, v249\n"
  "v_ldexp_f32 v90, v90, v249\n"
  "v_ldexp_f32 v91, v91, v249\n"
  "v_ldexp_f32 v96, v96, v249\n"
  "v_ldexp_f32 v97, v97, v249\n"
  "v_ldexp_f32 v98, v98, v249\n"
  "v_ldexp_f32 v99, v99, v249\n"
  "v_ldexp_f32 v104, v104, v249\n"
  "v_ldexp_f32 v105, v105, v249\n"
  "v_ldexp_f32 v106, v106, v249\n"
  "v_ldexp_f32 v107, v107, v249\n"
  "v_ldexp_f32 v112, v112, v249\n"
  "v_ldexp_f32 v113, v113, v249\n"
  "v_ldexp_f32 v114, v114, v249\n"
  "v_ldexp_f32 v115, v115, v249\n"
  "v_ldexp_f32 v120, v120, v249\n"
  "v_ldexp_f32 v121, v121, v249\n"
  "v_ldexp_f32 v122, v122, v249\n"
  "v_ldexp_f32 v123, v123, v249\n"
  "v_max3_f32 v248, v212, v213, v214\n"
  "v_max_f32 v248, v248, v215\n"
  "v_mov_b32 v249, v248\n"
  "s_nop 1\n"
  "v_permlane32_swap_b32 v248, v249\n"
  "v_max_f32 v248, v248, v249\n"
  "v_mov_b32 v249, v248\n"
  "s_nop 1\n"
  "v_permlane16_swap_b32 v248, v249\n"
  "v_max_f32 v248, v248, v249\n"
  "v_ceil_f32 v248, v248\n"
  "v_max_f32 v248, 0, v248\n"
  "v_cvt_i32_f32 v249, v248\n"
  "v_sub_u32 v249, 0, v249\n"
  "v_sub_f32 v212, v212, v248\n"
  "v_sub_f32 v213, v213, v248\n"
  "v_sub_f32 v214, v214, v248\n"
  "v_sub_f32 v215, v215, v248\n"
  "v_sub_f32 v220, v220, v248\n"
  "v_sub_f32 v221, v221, v248\n"
  "v_sub_f32 v222, v222, v248\n"
  "v_sub_f32 v223, v223, v248\n"
  "v_lshlrev_b32 v250, 16, v196\n"
  "v_and_b32 v251, 0xffff0000, v196\n"
  "v_ldexp_f32 v250, v250, v249\n"
  "v_ldexp_f32 v251, v251, v249\n"
  "v_cvt_pk_bf16_f32 v196, v250, v251\n"
  "v_lshlrev_b32 v250, 16, v197\n"
  "v_and_b32 v251, 0xffff0000, v197\n"
  "v_ldexp_f32 v250, v250, v249\n"
  "v_ldexp_f32 v251, v251, v249\n"
  "v_cvt_pk_bf16_f32 v197, v250, v251\n"
  "v_lshlrev_b32 v250, 16, v198\n"
  "v_and_b32 v251, 0xffff0000, v198\n"
  "v_ldexp_f32 v250, v250, v249\n"
  "v_ldexp_f32 v251, v251, v249\n"
  "v_cvt_pk_bf16_f32 v198, v250, v251\n"
  "v_lshlrev_b32 v250, 16, v199\n"
  "v_and_b32 v251, 0xffff0000, v199\n"
  "v_ldexp_f32 v250, v250, v249\n"
  "v_ldexp_f32 v251, v251, v249\n"
  "v_cvt_pk_bf16_f32 v199, v250, v251\n"
  "v_lshlrev_b32 v250, 16, v204\n"
  "v_and_b32 v251, 0xffff0000, v204\n"
  "v_ldexp_f32 v250, v250, v249\n"
  "v_ldexp_f32 v251, v251, v249\n"
  "v_cvt_pk_bf16_f32 v204, v250, v251\n"
  "v_lshlrev_b32 v250, 16, v205\n"
  "v_and_b32 v251, 0xffff0000, v205\n"
  "v_ldexp_f32 v250, v250, v249\n"
  "v_ldexp_f32 v251, v251, v249\n"
  "v_cvt_pk_bf16_f32 v205, v250, v251\n"
  "v_ldexp_f32 v247, v247, v249\n"
  "v_ldexp_f32 v4, v4, v249\n"
  "v_ldexp_f32 v5, v5, v249\n"
  "v_ldexp_f32 v6, v6, v249\n"
  "v_ldexp_f32 v7, v7, v249\n"
  "v_ldexp_f32 v12, v12, v249\n"
  "v_ldexp_f32 v13, v13, v249\n"
  "v_ldexp_f32 v14, v14, v249\n"
  "v_ldexp_f32 v15, v15, v249\n"
  "v_ldexp_f32 v20, v20, v249\n"
  "v_ldexp_f32 v21, v21, v249\n"
  "v_ldexp_f32 v22, v22, v249\n"
  "v_ldexp_f32 v23, v23, v249\n"
  "v_ldexp_f32 v28, v28, v249\n"
  "v_ldexp_f32 v29, v29, v249\n"
  "v_ldexp_f32 v30, v30, v249\n"
  "v_ldexp_f32 v31, v31, v249\n"
  "v_ldexp_f32 v36, v36, v249\n"
  "v_ldexp_f32 v37, v37, v249\n"
  "v_ldexp_f32 v38, v38, v249\n"
  "v_ldexp_f32 v39, v39, v249\n"
  "v_ldexp_f32 v44, v44, v249\n"
  "v_ldexp_f32 v45, v45, v249\n"
  "v_ldexp_f32 v46, v46, v249\n"
  "v_ldexp_f32 v47, v47, v249\n"
  "v_ldexp_f32 v52, v52, v249\n"
  "v_ldexp_f32 v53, v53, v249\n"
  "v_ldexp_f32 v54, v54, v249\n"
  "v_ldexp_f32 v55, v55, v249\n"
  "v_ldexp_f32 v60, v60, v249\n"
  "v_ldexp_f32 v61, v61, v249\n"
  "v_ldexp_f32 v62, v62, v249\n"
  "v_ldexp_f32 v63, v63, v249\n"
  "v_ldexp_f32 v68, v68, v249\n"
  "v_ldexp_f32 v69, v69, v249\n"
  "v_ldexp_f32 v70, v70, v249\n"
  "v_ldexp_f32 v71, v71, v249\n"
  "v_ldexp_f32 v76, v76, v249\n"
  "v_ldexp_f32 v77, v77, v249\n"
  "v_ldexp_f32 v78, v78, v249\n"
  "v_ldexp_f32 v79, v79, v249\n"
  "v_ldexp_f32 v84, v84, v249\n"
  "v_ldexp_f32 v85, v85, v249\n"
  "v_ldexp_f32 v86, v86, v249\n"
  "v_ldexp_f32 v87, v87, v249\n"
  "v_ldexp_f32 v92, v92, v249\n"
  "v_ldexp_f32 v93, v93, v249\n"
  "v_ldexp_f32 v94, v94, v249\n"
  "v_ldexp_f32 v95, v95, v249\n"
  "v_ldexp_f32 v100, v100, v249\n"
  "v_ldexp_f32 v101, v101, v249\n"
  "v_ldexp_f32 v102, v102, v249\n"
  "v_ldexp_f32 v103, v103, v249\n"
  "v_ldexp_f32 v108, v108, v249\n"
  "v_ldexp_f32 v109, v109, v249\n"
  "v_ldexp_f32 v110, v110, v249\n"
  "v_ldexp_f32 v111, v111, v249\n"
  "v_ldexp_f32 v116, v116, v249\n"
  "v_ldexp_f32 v117, v117, v249\n"
  "v_ldexp_f32 v118, v118, v249\n"
  "v_ldexp_f32 v119, v119, v249\n"
  "v_ldexp_f32 v124, v124, v249\n"
  "v_ldexp_f32 v125, v125, v249\n"
  "v_ldexp_f32 v126, v126, v249\n"
  "v_ldexp_f32 v127, v127, v249\n"
  "s_nop 3\n"
  "s_branch .Lback_lp_1_1\n"
  ".Lrare_ep_1_1:\n"
  "s_nop 15\n"
  "v_max3_f32 v248, v208, v209, v210\n"
  "v_max_f32 v248, v248, v211\n"
  "v_mov_b32 v249, v248\n"
  "s_nop 1\n"
  "v_permlane32_swap_b32 v248, v249\n"
  "v_max_f32 v248, v248, v249\n"
  "v_mov_b32 v249, v248\n"
  "s_nop 1\n"
  "v_permlane16_swap_b32 v248, v249\n"
  "v_max_f32 v248, v248, v249\n"
  "v_ceil_f32 v248, v248\n"
  "v_max_f32 v248, 0, v248\n"
  "v_cvt_i32_f32 v249, v248\n"
  "v_sub_u32 v249, 0, v249\n"
  "v_sub_f32 v208, v208, v248\n"
  "v_sub_f32 v209, v209, v248\n"
  "v_sub_f32 v210, v210, v248\n"
  "v_sub_f32 v211, v211, v248\n"
  "v_sub_f32 v216, v216, v248\n"
  "v_sub_f32 v217, v217, v248\n"
  "v_sub_f32 v218, v218, v248\n"
  "v_sub_f32 v219, v219, v248\n"
  "v_lshlrev_b32 v250, 16, v192\n"
  "v_and_b32 v251, 0xffff0000, v192\n"
  "v_ldexp_f32 v250, v250, v249\n"
  "v_ldexp_f32 v251, v251, v249\n"
  "v_cvt_pk_bf16_f32 v192, v250, v251\n"
  "v_lshlrev_b32 v250, 16, v193\n"
  "v_and_b32 v251, 0xffff0000, v193\n"
  "v_ldexp_f32 v250, v250, v249\n"
  "v_ldexp_f32 v251, v251, v249\n"
  "v_cvt_pk_bf16_f32 v193, v250, v251\n"
  "v_lshlrev_b32 v250, 16, v194\n"
  "v_and_b32 v251, 0xffff0000, v194\n"
  "v_ldexp_f32 v250, v250, v249\n"
  "v_ldexp_f32 v251, v251, v249\n"
  "v_cvt_pk_bf16_f32 v194, v250, v251\n"
  "v_lshlrev_b32 v250, 16, v195\n"
  "v_and_b32 v251, 0xffff0000, v195\n"
  "v_ldexp_f32 v250, v250, v249\n"
  "v_ldexp_f32 v251, v251, v249\n"
  "v_cvt_pk_bf16_f32 v195, v250, v251\n"
  "v_lshlrev_b32 v250, 16, v200\n"
  "v_and_b32 v251, 0xffff0000, v200\n"
  "v_ldexp_f32 v250, v250, v249\n"
  "v_ldexp_f32 v251, v251, v249\n"
  "v_cvt_pk_bf16_f32 v200, v250, v251\n"
  "v_lshlrev_b32 v250, 16, v201\n"
  "v_and_b32 v251, 0xffff0000, v201\n"
  "v_ldexp_f32 v250, v250, v249\n"
  "v_ldexp_f32 v251, v251, v249\n"
  "v_cvt_pk_bf16_f32 v201, v250, v251\n"
  "v_ldexp_f32 v246, v246, v249\n"
  "v_ldexp_f32 v0, v0, v249\n"
  "v_ldexp_f32 v1, v1, v249\n"
  "v_ldexp_f32 v2, v2, v249\n"
  "v_ldexp_f32 v3, v3, v249\n"
  "v_ldexp_f32 v8, v8, v249\n"
  "v_ldexp_f32 v9, v9, v249\n"
  "v_ldexp_f32 v10, v10, v249\n"
  "v_ldexp_f32 v11, v11, v249\n"
  "v_ldexp_f32 v16, v16, v249\n"
  "v_ldexp_f32 v17, v17, v249\n"
  "v_ldexp_f32 v18, v18, v249\n"
  "v_ldexp_f32 v19, v19, v249\n"
  "v_ldexp_f32 v24, v24, v249\n"
  "v_ldexp_f32 v25, v25, v249\n"
  "v_ldexp_f32 v26, v26, v249\n"
  "v_ldexp_f32 v27, v27, v249\n"
  "v_ldexp_f32 v32, v32, v249\n"
  "v_ldexp_f32 v33, v33, v249\n"
  "v_ldexp_f32 v34, v34, v249\n"
  "v_ldexp_f32 v35, v35, v249\n"
  "v_ldexp_f32 v40, v40, v249\n"
  "v_ldexp_f32 v41, v41, v249\n"
  "v_ldexp_f32 v42, v42, v249\n"
  "v_ldexp_f32 v43, v43, v249\n"
  "v_ldexp_f32 v48, v48, v249\n"
  "v_ldexp_f32 v49, v49, v249\n"
  "v_ldexp_f32 v50, v50, v249\n"
  "v_ldexp_f32 v51, v51, v249\n"
  "v_ldexp_f32 v56, v56, v249\n"
  "v_ldexp_f32 v57, v57, v249\n"
  "v_ldexp_f32 v58, v58, v249\n"
  "v_ldexp_f32 v59, v59, v249\n"
  "v_ldexp_f32 v64, v64, v249\n"
  "v_ldexp_f32 v65, v65, v249\n"
  "v_ldexp_f32 v66, v66, v249\n"
  "v_ldexp_f32 v67, v67, v249\n"
  "v_ldexp_f32 v72, v72, v249\n"
  "v_ldexp_f32 v73, v73, v249\n"
  "v_ldexp_f32 v74, v74, v249\n"
  "v_ldexp_f32 v75, v75, v249\n"
  "v_ldexp_f32 v80, v80, v249\n"
  "v_ldexp_f32 v81, v81, v249\n"
  "v_ldexp_f32 v82, v82, v249\n"
  "v_ldexp_f32 v83, v83, v249\n"
  "v_ldexp_f32 v88, v88, v249\n"
  "v_ldexp_f32 v89, v89, v249\n"
  "v_ldexp_f32 v90, v90, v249\n"
  "v_ldexp_f32 v91, v91, v249\n"
  "v_ldexp_f32 v96, v96, v249\n"
  "v_ldexp_f32 v97, v97, v249\n"
  "v_ldexp_f32 v98, v98, v249\n"
  "v_ldexp_f32 v99, v99, v249\n"
  "v_ldexp_f32 v104, v104, v249\n"
  "v_ldexp_f32 v105, v105, v249\n"
  "v_ldexp_f32 v106, v106, v249\n"
  "v_ldexp_f32 v107, v107, v249\n"
  "v_ldexp_f32 v112, v112, v249\n"
  "v_ldexp_f32 v113, v113, v249\n"
  "v_ldexp_f32 v114, v114, v249\n"
  "v_ldexp_f32 v115, v115, v249\n"
  "v_ldexp_f32 v120, v120, v249\n"
  "v_ldexp_f32 v121, v121, v249\n"
  "v_ldexp_f32 v122, v122, v249\n"
  "v_ldexp_f32 v123, v123, v249\n"
  "v_max3_f32 v248, v212, v213, v214\n"
  "v_max_f32 v248, v248, v215\n"
  "v_mov_b32 v249, v248\n"
  "s_nop 1\n"
  "v_permlane32_swap_b32 v248, v249\n"
  "v_max_f32 v248, v248, v249\n"
  "v_mov_b32 v249, v248\n"
  "s_nop 1\n"
  "v_permlane16_swap_b32 v248, v249\n"
  "v_max_f32 v248, v248, v249\n"
  "v_ceil_f32 v248, v248\n"
  "v_max_f32 v248, 0, v248\n"
  "v_cvt_i32_f32 v249, v248\n"
  "v_sub_u32 v249, 0, v249\n"
  "v_sub_f32 v212, v212, v248\n"
  "v_sub_f32 v213, v213, v248\n"
  "v_sub_f32 v214, v214, v248\n"
  "v_sub_f32 v215, v215, v248\n"
  "v_sub_f32 v220, v220, v248\n"
  "v_sub_f32 v221, v221, v248\n"
  "v_sub_f32 v222, v222, v248\n"
  "v_sub_f32 v223, v223, v248\n"
  "v_lshlrev_b32 v250, 16, v196\n"
  "v_and_b32 v251, 0xffff0000, v196\n"
  "v_ldexp_f32 v250, v250, v249\n"
  "v_ldexp_f32 v251, v251, v249\n"
  "v_cvt_pk_bf16_f32 v196, v250, v251\n"
  "v_lshlrev_b32 v250, 16, v197\n"
  "v_and_b32 v251, 0xffff0000, v197\n"
  "v_ldexp_f32 v250, v250, v249\n"
  "v_ldexp_f32 v251, v251, v249\n"
  "v_cvt_pk_bf16_f32 v197, v250, v251\n"
  "v_lshlrev_b32 v250, 16, v198\n"
  "v_and_b32 v251, 0xffff0000, v198\n"
  "v_ldexp_f32 v250, v250, v249\n"
  "v_ldexp_f32 v251, v251, v249\n"
  "v_cvt_pk_bf16_f32 v198, v250, v251\n"
  "v_lshlrev_b32 v250, 16, v199\n"
  "v_and_b32 v251, 0xffff0000, v199\n"
  "v_ldexp_f32 v250, v250, v249\n"
  "v_ldexp_f32 v251, v251, v249\n"
  "v_cvt_pk_bf16_f32 v199, v250, v251\n"
  "v_lshlrev_b32 v250, 16, v204\n"
  "v_and_b32 v251, 0xffff0000, v204\n"
  "v_ldexp_f32 v250, v250, v249\n"
  "v_ldexp_f32 v251, v251, v249\n"
  "v_cvt_pk_bf16_f32 v204, v250, v251\n"
  "v_lshlrev_b32 v250, 16, v205\n"
  "v_and_b32 v251, 0xffff0000, v205\n"
  "v_ldexp_f32 v250, v250, v249\n"
  "v_ldexp_f32 v251, v251, v249\n"
  "v_cvt_pk_bf16_f32 v205, v250, v251\n"
  "v_ldexp_f32 v247, v247, v249\n"
  "v_ldexp_f32 v4, v4, v249\n"
  "v_ldexp_f32 v5, v5, v249\n"
  "v_ldexp_f32 v6, v6, v249\n"
  "v_ldexp_f32 v7, v7, v249\n"
  "v_ldexp_f32 v12, v12, v249\n"
  "v_ldexp_f32 v13, v13, v249\n"
  "v_ldexp_f32 v14, v14, v249\n"
  "v_ldexp_f32 v15, v15, v249\n"
  "v_ldexp_f32 v20, v20, v249\n"
  "v_ldexp_f32 v21, v21, v249\n"
  "v_ldexp_f32 v22, v22, v249\n"
  "v_ldexp_f32 v23, v23, v249\n"
  "v_ldexp_f32 v28, v28, v249\n"
  "v_ldexp_f32 v29, v29, v249\n"
  "v_ldexp_f32 v30, v30, v249\n"
  "v_ldexp_f32 v31, v31, v249\n"
  "v_ldexp_f32 v36, v36, v249\n"
  "v_ldexp_f32 v37, v37, v249\n"
  "v_ldexp_f32 v38, v38, v249\n"
  "v_ldexp_f32 v39, v39, v249\n"
  "v_ldexp_f32 v44, v44, v249\n"
  "v_ldexp_f32 v45, v45, v249\n"
  "v_ldexp_f32 v46, v46, v249\n"
  "v_ldexp_f32 v47, v47, v249\n"
  "v_ldexp_f32 v52, v52, v249\n"
  "v_ldexp_f32 v53, v53, v249\n"
  "v_ldexp_f32 v54, v54, v249\n"
  "v_ldexp_f32 v55, v55, v249\n"
  "v_ldexp_f32 v60, v60, v249\n"
  "v_ldexp_f32 v61, v61, v249\n"
  "v_ldexp_f32 v62, v62, v249\n"
  "v_ldexp_f32 v63, v63, v249\n"
  "v_ldexp_f32 v68, v68, v249\n"
  "v_ldexp_f32 v69, v69, v249\n"
  "v_ldexp_f32 v70, v70, v249\n"
  "v_ldexp_f32 v71, v71, v249\n"
  "v_ldexp_f32 v76, v76, v249\n"
  "v_ldexp_f32 v77, v77, v249\n"
  "v_ldexp_f32 v78, v78, v249\n"
  "v_ldexp_f32 v79, v79, v249\n"
  "v_ldexp_f32 v84, v84, v249\n"
  "v_ldexp_f32 v85, v85, v249\n"
  "v_ldexp_f32 v86, v86, v249\n"
  "v_ldexp_f32 v87, v87, v249\n"
  "v_ldexp_f32 v92, v92, v249\n"
  "v_ldexp_f32 v93, v93, v249\n"
  "v_ldexp_f32 v94, v94, v249\n"
  "v_ldexp_f32 v95, v95, v249\n"
  "v_ldexp_f32 v100, v100, v249\n"
  "v_ldexp_f32 v101, v101, v249\n"
  "v_ldexp_f32 v102, v102, v249\n"
  "v_ldexp_f32 v103, v103, v249\n"
  "v_ldexp_f32 v108, v108, v249\n"
  "v_ldexp_f32 v109, v109, v249\n"
  "v_ldexp_f32 v110, v110, v249\n"
  "v_ldexp_f32 v111, v111, v249\n"
  "v_ldexp_f32 v116, v116, v249\n"
  "v_ldexp_f32 v117, v117, v249\n"
  "v_ldexp_f32 v118, v118, v249\n"
  "v_ldexp_f32 v119, v119, v249\n"
  "v_ldexp_f32 v124, v124, v249\n"
  "v_ldexp_f32 v125, v125, v249\n"
  "v_ldexp_f32 v126, v126, v249\n"
  "v_ldexp_f32 v127, v127, v249\n"
  "s_nop 3\n"
  "s_branch .Lback_ep_1_1\n"
  ".Lcons_end:\n"
      : "+{v244}"(la[0]), "+{v245}"(la[1]), "+{v246}"(la[2]), "+{v247}"(la[3]), "+{v248}"(la[4])
      : "s"(mk_srd(ws + (size_t)(rb * 8 + kv) * PART_WG, PART_WG)), "s"(wid)
      : "v0","v1","v2","v3","v4","v5","v6","v7","v8","v9","v10","v11","v12","v13","v14","v15","v16","v17","v18","v19","v20","v21","v22","v23","v24","v25","v26","v27","v28","v29","v30","v31","v32","v33","v34","v35","v36","v37","v38","v39","v40","v41","v42","v43","v44","v45","v46","v47","v48","v49","v50","v51","v52","v53","v54","v55","v56","v57","v58","v59","v60","v61","v62","v63","v64","v65","v66","v67","v68","v69","v70","v71","v72","v73","v74","v75","v76","v77","v78","v79","v80","v81","v82","v83","v84","v85","v86","v87","v88","v89","v90","v91","v92","v93","v94","v95","v96","v97","v98","v99","v100","v101","v102","v103","v104","v105","v106","v107","v108","v109","v110","v111","v112","v113","v114","v115","v116","v117","v118","v119","v120","v121","v122","v123","v124","v125","v126","v127","v128","v129","v130","v131","v132","v133","v134","v135","v136","v137","v138","v139","v140","v141","v142","v143","v144","v145","v146","v147","v148","v149","v150","v151","v152","v153","v154","v155","v156","v157","v158","v159","v160","v161","v162","v163","v164","v165","v166","v167","v168","v169","v170","v171","v172","v173","v174","v175","v176","v177","v178","v179","v180","v181","v182","v183","v184","v185","v186","v187","v188","v189","v190","v191","v192","v193","v194","v195","v196","v197","v198","v199","v200","v201","v202","v203","v204","v205","v206","v207","v208","v209","v210","v211","v212","v213","v214","v215","v216","v217","v218","v219","v220","v221","v222","v223","v224","v225","v226","v227","v228","v229","v230","v231","v232","v233","v234","v235","v236","v237","v238","v239","v240","v241","v242","v243","v249","v250","v251","v252","v253","v254","v255","s40","s41","s42","s43","s44","s45","s46","s47","s88","s89","s90","s91","vcc","scc","memory");
  } else {
  const unsigned p = wid - 4;
  __builtin_amdgcn_s_setprio(HPRIO);
  u32x2 ep;
  u32x4 xf[14];
  const srd_t so = mk_srd((const char*)wo + (size_t)kv * 512 * 896 - BIAS, 512 * 896 + BIAS);
  {
    const srd_t sk = mk_srd((const char*)kc + ((size_t)kv * 2048 + 1536) * 512 - BIAS, 512 * 512 + BIAS), sv = mk_srd((const char*)vc + ((size_t)kv * 2048 + 1536) * 512 - BIAS, 512 * 512 + BIAS);
    const unsigned ranks = j | rot << 8;
    if (p < 2) {
      asm volatile(
  "v_mbcnt_lo_u32_b32 v200, -1, 0\n"
  "v_mbcnt_hi_u32_b32 v200, -1, v200\n"
  "s_lshr_b32 s72, %6, 8\n"
  "s_mul_i32 s60, %5, 1792\n"
  "s_mul_i32 s62, %5, 2112\n"
  "s_mul_i32 s63, %5, 4360\n"
  "s_lshl_b32 s61, %5, 16\n"
  "v_lshrrev_b32 v201, 5, v200\n"
  "v_lshlrev_b32 v201, 15, v201\n"
  "v_and_b32 v220, 31, v200\n"
  "v_lshl_add_u32 v208, v220, 4, v201\n"
  "v_add_u32 v209, 7104, v208\n"
  "v_add_u32 v210, 14208, v208\n"
  "v_add_u32 v211, 21312, v208\n"
  "v_lshlrev_b32 v201, 4, v200\n"
  "s_add_u32 s72, s72, 1\n"
  "s_cmp_eq_u32 s72, 7\n"
  "s_cselect_b32 s72, 0, s72\n"
  "s_waitcnt vmcnt(4)\n"
  "s_barrier\n"
  "s_lshl_b32 s73, s72, 19\n"
  "s_add_u32 s73, s73, s61\n"
  "s_add_u32 s72, s72, 1\n"
  "s_cmp_eq_u32 s72, 7\n"
  "s_cselect_b32 s72, 0, s72\n"
  "s_add_u32 m0, s63, 69504\n"
  "s_add_u32 s68, s73, 0\n"
  "buffer_load_dwordx4 v208, %1, s68 offen offset:0  lds\n"
  "buffer_load_dwordx4 v209, %1, s68 offen offset:1088  lds\n"
  "buffer_load_dwordx4 v210, %1, s68 offen offset:2176  lds\n"
  "buffer_load_dwordx4 v211, %1, s68 offen offset:3264  lds\n"
  "s_add_u32 m0, s63, 86880\n"
  "s_add_u32 s68, s73, 512\n"
  "buffer_load_dwordx4 v208, %1, s68 offen offset:0  lds\n"
  "buffer_load_dwordx4 v209, %1, s68 offen offset:1088  lds\n"
  "buffer_load_dwordx4 v210, %1, s68 offen offset:2176  lds\n"
  "buffer_load_dwordx4 v211, %1, s68 offen offset:3264  lds\n"
  "v_add_u32 v212, 4096, v201\n"
  "v_add_u32 v213, 4064, v201\n"
  "v_add_u32 v214, 4032, v201\n"
  "v_add_u32 v215, 4000, v201\n"
  "v_and_b32 v202, 56, v200\n"
  "v_mul_u32_u24 v202, 896, v202\n"
  "v_and_b32 v201, 0x70, v201\n"
  "v_add_u32 v202, v202, v201\n"
  "v_add_u32 v216, 4096, v202\n"
  "v_add_u32 v217, 3904, v202\n"
  "v_add_u32 v218, 3712, v202\n"
  "v_add_u32 v219, 3520, v202\n"
  "s_and_b32 s69, %5, 1\n"
  "s_mul_i32 s71, s69, 3584\n"
  "s_lshr_b32 s69, %5, 1\n"
  "s_mul_i32 s69, s69, 57344\n"
  "s_add_u32 s71, s71, s69\n"
  "s_mul_i32 s66, %5, 8448\n"
  "s_mul_i32 s67, %5, 8720\n"
  "s_lshl_b32 s64, %5, 13\n"
  "s_mul_i32 s65, %5, 57344\n"
  "s_bfe_u32 s75, %6, 0x40001\n"
  "s_mul_i32 s75, s75, 28672\n"
  "s_mul_i32 s69, %5, 14336\n"
  "s_add_u32 s75, s75, s69\n"
  "s_bfe_u32 s74, %6, 0x40001\n"
  "s_lshl_b32 s74, s74, 10\n"
  "s_lshl_b32 s69, %5, 8\n"
  "s_add_u32 s74, s74, s69\n"
  "s_waitcnt vmcnt(8)\n"
  "s_barrier\n"
  "s_add_u32 m0, s63, 104256\n"
  "s_add_u32 s68, s73, 262144\n"
  "buffer_load_dwordx4 v208, %1, s68 offen offset:0  lds\n"
  "buffer_load_dwordx4 v209, %1, s68 offen offset:1088  lds\n"
  "buffer_load_dwordx4 v210, %1, s68 offen offset:2176  lds\n"
  "buffer_load_dwordx4 v211, %1, s68 offen offset:3264  lds\n"
  "s_add_u32 m0, s63, 121632\n"
  "s_add_u32 s68, s73, 262656\n"
  "buffer_load_dwordx4 v208, %1, s68 offen offset:0  lds\n"
  "buffer_load_dwordx4 v209, %1, s68 offen offset:1088  lds\n"
  "buffer_load_dwordx4 v210, %1, s68 offen offset:2176  lds\n"
  "buffer_load_dwordx4 v211, %1, s68 offen offset:3264  lds\n"
  "s_waitcnt vmcnt(8)\n"
  "s_barrier\n"
  "s_lshl_b32 s73, s72, 19\n"
  "s_add_u32 s73, s73, s61\n"
  "s_add_u32 s72, s72, 1\n"
  "s_cmp_eq_u32 s72, 7\n"
  "s_cselect_b32 s72, 0, s72\n"
  "s_add_u32 m0, s63, 0\n"
  "s_add_u32 s68, s73, 0\n"
  "buffer_load_dwordx4 v208, %1, s68 offen offset:0  lds\n"
  "buffer_load_dwordx4 v209, %1, s68 offen offset:1088  lds\n"
  "buffer_load_dwordx4 v210, %1, s68 offen offset:2176  lds\n"
  "buffer_load_dwordx4 v211, %1, s68 offen offset:3264  lds\n"
  "s_add_u32 m0, s63, 17376\n"
  "s_add_u32 s68, s73, 512\n"
  "buffer_load_dwordx4 v208, %1, s68 offen offset:0  lds\n"
  "buffer_load_dwordx4 v209, %1, s68 offen offset:1088  lds\n"
  "buffer_load_dwordx4 v210, %1, s68 offen offset:2176  lds\n"
  "buffer_load_dwordx4 v211, %1, s68 offen offset:3264  lds\n"
  "s_buffer_load_dword s70, %2, s74 offset:4096\n"
  "s_buffer_load_dword s70, %2, s74 offset:4160\n"
  "s_buffer_load_dword s70, %2, s74 offset:4224\n"
  "s_waitcnt vmcnt(8)\n"
  "s_barrier\n"
  "s_add_u32 m0, s63, 34752\n"
  "s_add_u32 s68, s73, 262144\n"
  "buffer_load_dwordx4 v208, %1, s68 offen offset:0  lds\n"
  "buffer_load_dwordx4 v209, %1, s68 offen offset:1088  lds\n"
  "buffer_load_dwordx4 v210, %1, s68 offen offset:2176  lds\n"
  "buffer_load_dwordx4 v211, %1, s68 offen offset:3264  lds\n"
  "s_add_u32 m0, s63, 52128\n"
  "s_add_u32 s68, s73, 262656\n"
  "buffer_load_dwordx4 v208, %1, s68 offen offset:0  lds\n"
  "buffer_load_dwordx4 v209, %1, s68 offen offset:1088  lds\n"
  "buffer_load_dwordx4 v210, %1, s68 offen offset:2176  lds\n"
  "buffer_load_dwordx4 v211, %1, s68 offen offset:3264  lds\n"
  "s_buffer_load_dword s70, %2, s74 offset:4288\n"
  "s_buffer_load_dword s70, %2, s74 offset:20480\n"
  "s_buffer_load_dword s70, %2, s74 offset:20544\n"
  "s_waitcnt vmcnt(8)\n"
  "s_barrier\n"
  "s_lshl_b32 s73, s72, 19\n"
  "s_add_u32 s73, s73, s61\n"
  "s_add_u32 s72, s72, 1\n"
  "s_cmp_eq_u32 s72, 7\n"
  "s_cselect_b32 s72, 0, s72\n"
  "s_add_u32 m0, s63, 69504\n"
  "s_add_u32 s68, s73, 0\n"
  "buffer_load_dwordx4 v208, %1, s68 offen offset:0  lds\n"
  "buffer_load_dwordx4 v209, %1, s68 offen offset:1088  lds\n"
  "buffer_load_dwordx4 v210, %1, s68 offen offset:2176  lds\n"
  "buffer_load_dwordx4 v211, %1, s68 offen offset:3264  lds\n"
  "s_add_u32 m0, s63, 86880\n"
  "s_add_u32 s68, s73, 512\n"
  "buffer_load_dwordx4 v208, %1, s68 offen offset:0  lds\n"
  "buffer_load_dwordx4 v209, %1, s68 offen offset:1088  lds\n"
  "buffer_load_dwordx4 v210, %1, s68 offen offset:2176  lds\n"
  "buffer_load_dwordx4 v211, %1, s68 offen offset:3264  lds\n"
  "s_buffer_load_dword s70, %2, s74 offset:20608\n"
  "s_buffer_load_dword s70, %2, s74 offset:20672\n"
  "s_buffer_load_dword s70, %3, s74 offset:4096\n"
  "s_waitcnt vmcnt(8)\n"
  "s_barrier\n"
  "s_add_u32 m0, s63, 104256\n"
  "s_add_u32 s68, s73, 262144\n"
  "buffer_load_dwordx4 v208, %1, s68 offen offset:0  lds\n"
  "buffer_load_dwordx4 v209, %1, s68 offen offset:1088  lds\n"
  "buffer_load_dwordx4 v210, %1, s68 offen offset:2176  lds\n"
  "buffer_load_dwordx4 v211, %1, s68 offen offset:3264  lds\n"
  "s_add_u32 m0, s63, 121632\n"
  "s_add_u32 s68, s73, 262656\n"
  "buffer_load_dwordx4 v208, %1, s68 offen offset:0  lds\n"
  "buffer_load_dwordx4 v209, %1, s68 offen offset:1088  lds\n"
  "buffer_load_dwordx4 v210, %1, s68 offen offset:2176  lds\n"
  "buffer_load_dwordx4 v211, %1, s68 offen offset:3264  lds\n"
  "s_buffer_load_dword s70, %3, s74 offset:4160\n"
  "s_buffer_load_dword s70, %3, s74 offset:4224\n"
  "s_buffer_load_dword s70, %3, s74 offset:4288\n"
  "s_waitcnt vmcnt(8)\n"
  "s_barrier\n"
  "s_lshl_b32 s73, s72, 19\n"
  "s_add_u32 s73, s73, s61\n"
  "s_add_u32 s72, s72, 1\n"
  "s_cmp_eq_u32 s72, 7\n"
  "s_cselect_b32 s72, 0, s72\n"
  "s_add_u32 m0, s63, 0\n"
  "s_add_u32 s68, s73, 0\n"
  "buffer_load_dwordx4 v208, %1, s68 offen offset:0  lds\n"
  "buffer_load_dwordx4 v209, %1, s68 offen offset:1088  lds\n"
  "buffer_load_dwordx4 v210, %1, s68 offen offset:2176  lds\n"
  "buffer_load_dwordx4 v211, %1, s68 offen offset:3264  lds\n"
  "s_add_u32 m0, s63, 17376\n"
  "s_add_u32 s68, s73, 512\n"
  "buffer_load_dwordx4 v208, %1, s68 offen offset:0  lds\n"
  "buffer_load_dwordx4 v209, %1, s68 offen offset:1088  lds\n"
  "buffer_load_dwordx4 v210, %1, s68 offen offset:2176  lds\n"
  "buffer_load_dwordx4 v211, %1, s68 offen offset:3264  lds\n"
  "s_buffer_load_dword s70, %2, s74 offset:36864\n"
  "s_buffer_load_dword s70, %2, s74 offset:36928\n"
  "s_buffer_load_dword s70, %2, s74 offset:36992\n"
  "s_waitcnt vmcnt(8)\n"
  "s_barrier\n"
  "s_add_u32 m0, s63, 34752\n"
  "s_add_u32 s68, s73, 262144\n"
  "buffer_load_dwordx4 v208, %1, s68 offen offset:0  lds\n"
  "buffer_load_dwordx4 v209, %1, s68 offen offset:1088  lds\n"
  "buffer_load_dwordx4 v210, %1, s68 offen offset:2176  lds\n"
  "buffer_load_dwordx4 v211, %1, s68 offen offset:3264  lds\n"
  "s_add_u32 m0, s63, 52128\n"
  "s_add_u32 s68, s73, 262656\n"
  "buffer_load_dwordx4 v208, %1, s68 offen offset:0  lds\n"
  "buffer_load_dwordx4 v209, %1, s68 offen offset:1088  lds\n"
  "buffer_load_dwordx4 v210, %1, s68 offen offset:2176  lds\n"
  "buffer_load_dwordx4 v211, %1, s68 offen offset:3264  lds\n"
  "s_buffer_load_dword s70, %2, s74 offset:37056\n"
  "s_buffer_load_dword s70, %3, s74 offset:20480\n"
  "s_buffer_load_dword s70, %3, s74 offset:20544\n"
  "s_waitcnt vmcnt(8)\n"
  "s_barrier\n"
  "s_lshl_b32 s73, s72, 19\n"
  "s_add_u32 s73, s73, s61\n"
  "s_add_u32 s72, s72, 1\n"
  "s_cmp_eq_u32 s72, 7\n"
  "s_cselect_b32 s72, 0, s72\n"
  "s_add_u32 m0, s63, 69504\n"
  "s_add_u32 s68, s73, 0\n"
  "buffer_load_dwordx4 v208, %1, s68 offen offset:0  lds\n"
  "buffer_load_dwordx4 v209, %1, s68 offen offset:1088  lds\n"
  "buffer_load_dwordx4 v210, %1, s68 offen offset:2176  lds\n"
  "buffer_load_dwordx4 v211, %1, s68 offen offset:3264  lds\n"
  "s_add_u32 m0, s63, 86880\n"
  "s_add_u32 s68, s73, 512\n"
  "buffer_load_dwordx4 v208, %1, s68 offen offset:0  lds\n"
  "buffer_load_dwordx4 v209, %1, s68 offen offset:1088  lds\n"
  "buffer_load_dwordx4 v210, %1, s68 offen offset:2176  lds\n"
  "buffer_load_dwordx4 v211, %1, s68 offen offset:3264  lds\n"
  "s_buffer_load_dword s70, %3, s74 offset:20608\n"
  "s_buffer_load_dword s70, %3, s74 offset:20672\n"
  "s_buffer_load_dword s70, %2, s74 offset:53248\n"
  "s_waitcnt vmcnt(8)\n"
  "s_barrier\n"
  "s_add_u32 m0, s63, 104256\n"
  "s_add_u32 s68, s73, 262144\n"
  "buffer_load_dwordx4 v208, %1, s68 offen offset:0  lds\n"
  "buffer_load_dwordx4 v209, %1, s68 offen offset:1088  lds\n"
  "buffer_load_dwordx4 v210, %1, s68 offen offset:2176  lds\n"
  "buffer_load_dwordx4 v211, %1, s68 offen offset:3264  lds\n"
  "s_add_u32 m0, s63, 121632\n"
  "s_add_u32 s68, s73, 262656\n"
  "buffer_load_dwordx4 v208, %1, s68 offen offset:0  lds\n"
  "buffer_load_dwordx4 v209, %1, s68 offen offset:1088  lds\n"
  "buffer_load_dwordx4 v210, %1, s68 offen offset:2176  lds\n"
  "buffer_load_dwordx4 v211, %1, s68 offen offset:3264  lds\n"
  "s_buffer_load_dword s70, %2, s74 offset:53312\n"
  "s_buffer_load_dword s70, %2, s74 offset:53376\n"
  "s_buffer_load_dword s70, %2, s74 offset:53440\n"
  "s_waitcnt vmcnt(8)\n"
  "s_barrier\n"
  "s_lshl_b32 s73, s72, 19\n"
  "s_add_u32 s73, s73, s61\n"
  "s_add_u32 s72, s72, 1\n"
  "s_cmp_eq_u32 s72, 7\n"
  "s_cselect_b32 s72, 0, s72\n"
  "s_add_u32 m0, s63, 0\n"
  "s_add_u32 s68, s73, 0\n"
  "buffer_load_dwordx4 v208, %1, s68 offen offset:0  lds\n"
  "buffer_load_dwordx4 v209, %1, s68 offen offset:1088  lds\n"
  "buffer_load_dwordx4 v210, %1, s68 offen offset:2176  lds\n"
  "buffer_load_dwordx4 v211, %1, s68 offen offset:3264  lds\n"
  "s_add_u32 m0, s63, 17376\n"
  "s_add_u32 s68, s73, 512\n"
  "buffer_load_dwordx4 v208, %1, s68 offen offset:0  lds\n"
  "buffer_load_dwordx4 v209, %1, s68 offen offset:1088  lds\n"
  "buffer_load_dwordx4 v210, %1, s68 offen offset:2176  lds\n"
  "buffer_load_dwordx4 v211, %1, s68 offen offset:3264  lds\n"
  "s_waitcnt vmcnt(8)\n"
  "s_barrier\n"
  "s_add_u32 m0, s63, 34752\n"
  "s_add_u32 s68, s73, 262144\n"
  "buffer_load_dwordx4 v208, %1, s68 offen offset:0  lds\n"
  "buffer_load_dwordx4 v209, %1, s68 offen offset:1088  lds\n"
  "buffer_load_dwordx4 v210, %1, s68 offen offset:2176  lds\n"
  "buffer_load_dwordx4 v211, %1, s68 offen offset:3264  lds\n"
  "s_add_u32 m0, s63, 52128\n"
  "s_add_u32 s68, s73, 262656\n"
  "buffer_load_dwordx4 v208, %1, s68 offen offset:0  lds\n"
  "buffer_load_dwordx4 v209, %1, s68 offen offset:1088  lds\n"
  "buffer_load_dwordx4 v210, %1, s68 offen offset:2176  lds\n"
  "buffer_load_dwordx4 v211, %1, s68 offen offset:3264  lds\n"
  "s_waitcnt vmcnt(8)\n"
  "s_barrier\n"
  "s_add_u32 m0, s66, 86880\n"
  "s_add_u32 s68, s64, 0\n"
  "buffer_load_dwordx4 v212, %2, s68 offen offset:0  lds\n"
  "buffer_load_dwordx4 v213, %2, s68 offen offset:1056  lds\n"
  "buffer_load_dwordx4 v214, %2, s68 offen offset:2112  lds\n"
  "buffer_load_dwordx4 v215, %2, s68 offen offset:3168  lds\n"
  "s_add_u32 m0, s66, 91104\n"
  "s_add_u32 s68, s64, 4096\n"
  "buffer_load_dwordx4 v212, %2, s68 offen offset:0  lds\n"
  "buffer_load_dwordx4 v213, %2, s68 offen offset:1056  lds\n"
  "buffer_load_dwordx4 v214, %2, s68 offen offset:2112  lds\n"
  "buffer_load_dwordx4 v215, %2, s68 offen offset:3168  lds\n"
  "s_waitcnt vmcnt(8)\n"
  "s_barrier\n"
  "s_add_u32 m0, s66, 104256\n"
  "s_add_u32 s68, s64, 16384\n"
  "buffer_load_dwordx4 v212, %2, s68 offen offset:0  lds\n"
  "buffer_load_dwordx4 v213, %2, s68 offen offset:1056  lds\n"
  "buffer_load_dwordx4 v214, %2, s68 offen offset:2112  lds\n"
  "buffer_load_dwordx4 v215, %2, s68 offen offset:3168  lds\n"
  "s_add_u32 m0, s66, 108480\n"
  "s_add_u32 s68, s64, 20480\n"
  "buffer_load_dwordx4 v212, %2, s68 offen offset:0  lds\n"
  "buffer_load_dwordx4 v213, %2, s68 offen offset:1056  lds\n"
  "buffer_load_dwordx4 v214, %2, s68 offen offset:2112  lds\n"
  "buffer_load_dwordx4 v215, %2, s68 offen offset:3168  lds\n"
  "s_add_u32 m0, s66, 121632\n"
  "s_add_u32 s68, s64, 0\n"
  "buffer_load_dwordx4 v212, %3, s68 offen offset:0  lds\n"
  "buffer_load_dwordx4 v213, %3, s68 offen offset:1056  lds\n"
  "buffer_load_dwordx4 v214, %3, s68 offen offset:2112  lds\n"
  "buffer_load_dwordx4 v215, %3, s68 offen offset:3168  lds\n"
  "s_waitcnt vmcnt(12)\n"
  "s_barrier\n"
  "s_add_u32 m0, s66, 125856\n"
  "s_add_u32 s68, s64, 4096\n"
  "buffer_load_dwordx4 v212, %3, s68 offen offset:0  lds\n"
  "buffer_load_dwordx4 v213, %3, s68 offen offset:1056  lds\n"
  "buffer_load_dwordx4 v214, %3, s68 offen offset:2112  lds\n"
  "buffer_load_dwordx4 v215, %3, s68 offen offset:3168  lds\n"
  "s_add_u32 m0, s66, 0\n"
  "s_add_u32 s68, s64, 32768\n"
  "buffer_load_dwordx4 v212, %2, s68 offen offset:0  lds\n"
  "buffer_load_dwordx4 v213, %2, s68 offen offset:1056  lds\n"
  "buffer_load_dwordx4 v214, %2, s68 offen offset:2112  lds\n"
  "buffer_load_dwordx4 v215, %2, s68 offen offset:3168  lds\n"
  "s_add_u32 m0, s66, 4224\n"
  "s_add_u32 s68, s64, 36864\n"
  "buffer_load_dwordx4 v212, %2, s68 offen offset:0  lds\n"
  "buffer_load_dwordx4 v213, %2, s68 offen offset:1056  lds\n"
  "buffer_load_dwordx4 v214, %2, s68 offen offset:2112  lds\n"
  "buffer_load_dwordx4 v215, %2, s68 offen offset:3168  lds\n"
  "s_add_u32 m0, s66, 17376\n"
  "s_add_u32 s68, s64, 16384\n"
  "buffer_load_dwordx4 v212, %3, s68 offen offset:0  lds\n"
  "buffer_load_dwordx4 v213, %3, s68 offen offset:1056  lds\n"
  "buffer_load_dwordx4 v214, %3, s68 offen offset:2112  lds\n"
  "buffer_load_dwordx4 v215, %3, s68 offen offset:3168  lds\n"
  "s_add_u32 m0, s66, 21600\n"
  "s_add_u32 s68, s64, 20480\n"
  "buffer_load_dwordx4 v212, %3, s68 offen offset:0  lds\n"
  "buffer_load_dwordx4 v213, %3, s68 offen offset:1056  lds\n"
  "buffer_load_dwordx4 v214, %3, s68 offen offset:2112  lds\n"
  "buffer_load_dwordx4 v215, %3, s68 offen offset:3168  lds\n"
  "s_buffer_load_dword s70, %4, s75 offset:4096\n"
  "s_buffer_load_dword s70, %4, s75 offset:4160\n"
  "s_buffer_load_dword s70, %4, s75 offset:4992\n"
  "s_buffer_load_dword s70, %4, s75 offset:5056\n"
  "s_buffer_load_dword s70, %4, s75 offset:5888\n"
  "s_buffer_load_dword s70, %4, s75 offset:5952\n"
  "s_buffer_load_dword s70, %4, s75 offset:6784\n"
  "s_buffer_load_dword s70, %4, s75 offset:6848\n"
  "s_buffer_load_dword s70, %4, s75 offset:7680\n"
  "s_buffer_load_dword s70, %4, s75 offset:7744\n"
  "s_buffer_load_dword s70, %4, s75 offset:8576\n"
  "s_buffer_load_dword s70, %4, s75 offset:8640\n"
  "s_buffer_load_dword s70, %4, s75 offset:9472\n"
  "s_buffer_load_dword s70, %4, s75 offset:9536\n"
  "s_waitcnt vmcnt(16)\n"
  "s_barrier\n"
  "s_add_u32 m0, s66, 34752\n"
  "s_add_u32 s68, s64, 49152\n"
  "buffer_load_dwordx4 v212, %2, s68 offen offset:0  lds\n"
  "buffer_load_dwordx4 v213, %2, s68 offen offset:1056  lds\n"
  "buffer_load_dwordx4 v214, %2, s68 offen offset:2112  lds\n"
  "buffer_load_dwordx4 v215, %2, s68 offen offset:3168  lds\n"
  "s_add_u32 m0, s66, 38976\n"
  "s_add_u32 s68, s64, 53248\n"
  "buffer_load_dwordx4 v212, %2, s68 offen offset:0  lds\n"
  "buffer_load_dwordx4 v213, %2, s68 offen offset:1056  lds\n"
  "buffer_load_dwordx4 v214, %2, s68 offen offset:2112  lds\n"
  "buffer_load_dwordx4 v215, %2, s68 offen offset:3168  lds\n"
  "s_add_u32 m0, s66, 52128\n"
  "s_add_u32 s68, s64, 32768\n"
  "buffer_load_dwordx4 v212, %3, s68 offen offset:0  lds\n"
  "buffer_load_dwordx4 v213, %3, s68 offen offset:1056  lds\n"
  "buffer_load_dwordx4 v214, %3, s68 offen offset:2112  lds\n"
  "buffer_load_dwordx4 v215, %3, s68 offen offset:3168  lds\n"
  "s_add_u32 m0, s66, 56352\n"
  "s_add_u32 s68, s64, 36864\n"
  "buffer_load_dwordx4 v212, %3, s68 offen offset:0  lds\n"
  "buffer_load_dwordx4 v213, %3, s68 offen offset:1056  lds\n"
  "buffer_load_dwordx4 v214, %3, s68 offen offset:2112  lds\n"
  "buffer_load_dwordx4 v215, %3, s68 offen offset:3168  lds\n"
  "s_add_u32 m0, s66, 69504\n"
  "s_add_u32 s68, s64, 65536\n"
  "buffer_load_dwordx4 v212, %2, s68 offen offset:0  lds\n"
  "buffer_load_dwordx4 v213, %2, s68 offen offset:1056  lds\n"
  "buffer_load_dwordx4 v214, %2, s68 offen offset:2112  lds\n"
  "buffer_load_dwordx4 v215, %2, s68 offen offset:3168  lds\n"
  "s_buffer_load_dword s70, %4, s75 offset:10368\n"
  "s_buffer_load_dword s70, %4, s75 offset:10432\n"
  "s_buffer_load_dword s70, %4, s75 offset:11264\n"
  "s_buffer_load_dword s70, %4, s75 offset:11328\n"
  "s_buffer_load_dword s70, %4, s75 offset:12160\n"
  "s_buffer_load_dword s70, %4, s75 offset:12224\n"
  "s_buffer_load_dword s70, %4, s75 offset:13056\n"
  "s_buffer_load_dword s70, %4, s75 offset:13120\n"
  "s_buffer_load_dword s70, %4, s75 offset:13952\n"
  "s_buffer_load_dword s70, %4, s75 offset:14016\n"
  "s_buffer_load_dword s70, %4, s75 offset:14848\n"
  "s_buffer_load_dword s70, %4, s75 offset:14912\n"
  "s_buffer_load_dword s70, %4, s75 offset:15744\n"
  "s_buffer_load_dword s70, %4, s75 offset:15808\n"
  "s_waitcnt vmcnt(20)\n"
  "s_barrier\n"
  "s_add_u32 m0, s66, 73728\n"
  "s_add_u32 s68, s64, 69632\n"
  "buffer_load_dwordx4 v212, %2, s68 offen offset:0  lds\n"
  "buffer_load_dwordx4 v213, %2, s68 offen offset:1056  lds\n"
  "buffer_load_dwordx4 v214, %2, s68 offen offset:2112  lds\n"
  "buffer_load_dwordx4 v215, %2, s68 offen offset:3168  lds\n"
  "s_add_u32 m0, s66, 86880\n"
  "s_add_u32 s68, s64, 49152\n"
  "buffer_load_dwordx4 v212, %3, s68 offen offset:0  lds\n"
  "buffer_load_dwordx4 v213, %3, s68 offen offset:1056  lds\n"
  "buffer_load_dwordx4 v214, %3, s68 offen offset:2112  lds\n"
  "buffer_load_dwordx4 v215, %3, s68 offen offset:3168  lds\n"
  "s_add_u32 m0, s66, 91104\n"
  "s_add_u32 s68, s64, 53248\n"
  "buffer_load_dwordx4 v212, %3, s68 offen offset:0  lds\n"
  "buffer_load_dwordx4 v213, %3, s68 offen offset:1056  lds\n"
  "buffer_load_dwordx4 v214, %3, s68 offen offset:2112  lds\n"
  "buffer_load_dwordx4 v215, %3, s68 offen offset:3168  lds\n"
  "s_add_u32 m0, s66, 104256\n"
  "s_add_u32 s68, s64, 81920\n"
  "buffer_load_dwordx4 v212, %2, s68 offen offset:0  lds\n"
  "buffer_load_dwordx4 v213, %2, s68 offen offset:1056  lds\n"
  "buffer_load_dwordx4 v214, %2, s68 offen offset:2112  lds\n"
  "buffer_load_dwordx4 v215, %2, s68 offen offset:3168  lds\n"
  "s_add_u32 m0, s66, 108480\n"
  "s_add_u32 s68, s64, 86016\n"
  "buffer_load_dwordx4 v212, %2, s68 offen offset:0  lds\n"
  "buffer_load_dwordx4 v213, %2, s68 offen offset:1056  lds\n"
  "buffer_load_dwordx4 v214, %2, s68 offen offset:2112  lds\n"
  "buffer_load_dwordx4 v215, %2, s68 offen offset:3168  lds\n"
  "s_buffer_load_dword s70, %4, s75 offset:16640\n"
  "s_buffer_load_dword s70, %4, s75 offset:16704\n"
  "s_buffer_load_dword s70, %4, s75 offset:17536\n"
  "s_buffer_load_dword s70, %4, s75 offset:17600\n"
  "s_buffer_load_dword s70, %4, s75 offset:4224\n"
  "s_buffer_load_dword s70, %4, s75 offset:4288\n"
  "s_buffer_load_dword s70, %4, s75 offset:5120\n"
  "s_buffer_load_dword s70, %4, s75 offset:5184\n"
  "s_buffer_load_dword s70, %4, s75 offset:6016\n"
  "s_buffer_load_dword s70, %4, s75 offset:6080\n"
  "s_buffer_load_dword s70, %4, s75 offset:6912\n"
  "s_buffer_load_dword s70, %4, s75 offset:6976\n"
  "s_buffer_load_dword s70, %4, s75 offset:7808\n"
  "s_buffer_load_dword s70, %4, s75 offset:7872\n"
  "s_waitcnt vmcnt(24)\n"
  "s_barrier\n"
  "s_add_u32 m0, s66, 121632\n"
  "s_add_u32 s68, s64, 65536\n"
  "buffer_load_dwordx4 v212, %3, s68 offen offset:0  lds\n"
  "buffer_load_dwordx4 v213, %3, s68 offen offset:1056  lds\n"
  "buffer_load_dwordx4 v214, %3, s68 offen offset:2112  lds\n"
  "buffer_load_dwordx4 v215, %3, s68 offen offset:3168  lds\n"
  "s_add_u32 m0, s66, 125856\n"
  "s_add_u32 s68, s64, 69632\n"
  "buffer_load_dwordx4 v212, %3, s68 offen offset:0  lds\n"
  "buffer_load_dwordx4 v213, %3, s68 offen offset:1056  lds\n"
  "buffer_load_dwordx4 v214, %3, s68 offen offset:2112  lds\n"
  "buffer_load_dwordx4 v215, %3, s68 offen offset:3168  lds\n"
  "s_add_u32 m0, s66, 0\n"
  "s_add_u32 s68, s64, 98304\n"
  "buffer_load_dwordx4 v212, %2, s68 offen offset:0  lds\n"
  "buffer_load_dwordx4 v213, %2, s68 offen offset:1056  lds\n"
  "buffer_load_dwordx4 v214, %2, s68 offen offset:2112  lds\n"
  "buffer_load_dwordx4 v215, %2, s68 offen offset:3168  lds\n"
  "s_add_u32 m0, s66, 4224\n"
  "s_add_u32 s68, s64, 102400\n"
  "buffer_load_dwordx4 v212, %2, s68 offen offset:0  lds\n"
  "buffer_load_dwordx4 v213, %2, s68 offen offset:1056  lds\n"
  "buffer_load_dwordx4 v214, %2, s68 offen offset:2112  lds\n"
  "buffer_load_dwordx4 v215, %2, s68 offen offset:3168  lds\n"
  "s_buffer_load_dword s70, %4, s75 offset:8704\n"
  "s_buffer_load_dword s70, %4, s75 offset:8768\n"
  "s_buffer_load_dword s70, %4, s75 offset:9600\n"
  "s_buffer_load_dword s70, %4, s75 offset:9664\n"
  "s_buffer_load_dword s70, %4, s75 offset:10496\n"
  "s_buffer_load_dword s70, %4, s75 offset:10560\n"
  "s_buffer_load_dword s70, %4, s75 offset:11392\n"
  "s_buffer_load_dword s70, %4, s75 offset:11456\n"
  "s_buffer_load_dword s70, %4, s75 offset:12288\n"
  "s_buffer_load_dword s70, %4, s75 offset:12352\n"
  "s_buffer_load_dword s70, %4, s75 offset:13184\n"
  "s_buffer_load_dword s70, %4, s75 offset:13248\n"
  "s_buffer_load_dword s70, %4, s75 offset:14080\n"
  "s_buffer_load_dword s70, %4, s75 offset:14144\n"
  "s_waitcnt vmcnt(24)\n"
  "s_barrier\n"
  "s_add_u32 m0, s66, 17376\n"
  "s_add_u32 s68, s64, 81920\n"
  "buffer_load_dwordx4 v212, %3, s68 offen offset:0  lds\n"
  "buffer_load_dwordx4 v213, %3, s68 offen offset:1056  lds\n"
  "buffer_load_dwordx4 v214, %3, s68 offen offset:2112  lds\n"
  "buffer_load_dwordx4 v215, %3, s68 offen offset:3168  lds\n"
  "s_add_u32 m0, s66, 21600\n"
  "s_add_u32 s68, s64, 86016\n"
  "buffer_load_dwordx4 v212, %3, s68 offen offset:0  lds\n"
  "buffer_load_dwordx4 v213, %3, s68 offen offset:1056  lds\n"
  "buffer_load_dwordx4 v214, %3, s68 offen offset:2112  lds\n"
  "buffer_load_dwordx4 v215, %3, s68 offen offset:3168  lds\n"
  "s_add_u32 m0, s66, 34752\n"
  "s_add_u32 s68, s64, 114688\n"
  "buffer_load_dwordx4 v212, %2, s68 offen offset:0  lds\n"
  "buffer_load_dwordx4 v213, %2, s68 offen offset:1056  lds\n"
  "buffer_load_dwordx4 v214, %2, s68 offen offset:2112  lds\n"
  "buffer_load_dwordx4 v215, %2, s68 offen offset:3168  lds\n"
  "s_add_u32 m0, s66, 38976\n"
  "s_add_u32 s68, s64, 118784\n"
  "buffer_load_dwordx4 v212, %2, s68 offen offset:0  lds\n"
  "buffer_load_dwordx4 v213, %2, s68 offen offset:1056  lds\n"
  "buffer_load_dwordx4 v214, %2, s68 offen offset:2112  lds\n"
  "buffer_load_dwordx4 v215, %2, s68 offen offset:3168  lds\n"
  "s_buffer_load_dword s70, %4, s75 offset:14976\n"
  "s_buffer_load_dword s70, %4, s75 offset:15040\n"
  "s_buffer_load_dword s70, %4, s75 offset:15872\n"
  "s_buffer_load_dword s70, %4, s75 offset:15936\n"
  "s_buffer_load_dword s70, %4, s75 offset:16768\n"
  "s_buffer_load_dword s70, %4, s75 offset:16832\n"
  "s_buffer_load_dword s70, %4, s75 offset:17664\n"
  "s_buffer_load_dword s70, %4, s75 offset:17728\n"
  "s_buffer_load_dword s70, %4, s75 offset:4352\n"
  "s_buffer_load_dword s70, %4, s75 offset:4416\n"
  "s_buffer_load_dword s70, %4, s75 offset:5248\n"
  "s_buffer_load_dword s70, %4, s75 offset:5312\n"
  "s_buffer_load_dword s70, %4, s75 offset:6144\n"
  "s_buffer_load_dword s70, %4, s75 offset:6208\n"
  "s_waitcnt vmcnt(24)\n"
  "s_barrier\n"
  "s_add_u32 m0, s66, 52128\n"
  "s_add_u32 s68, s64, 98304\n"
  "buffer_load_dwordx4 v212, %3, s68 offen offset:0  lds\n"
  "buffer_load_dwordx4 v213, %3, s68 offen offset:1056  lds\n"
  "buffer_load_dwordx4 v214, %3, s68 offen offset:2112  lds\n"
  "buffer_load_dwordx4 v215, %3, s68 offen offset:3168  lds\n"
  "s_add_u32 m0, s66, 56352\n"
  "s_add_u32 s68, s64, 102400\n"
  "buffer_load_dwordx4 v212, %3, s68 offen offset:0  lds\n"
  "buffer_load_dwordx4 v213, %3, s68 offen offset:1056  lds\n"
  "buffer_load_dwordx4 v214, %3, s68 offen offset:2112  lds\n"
  "buffer_load_dwordx4 v215, %3, s68 offen offset:3168  lds\n"
  "s_add_u32 m0, s66, 69504\n"
  "s_add_u32 s68, s64, 131072\n"
  "buffer_load_dwordx4 v212, %2, s68 offen offset:0  lds\n"
  "buffer_load_dwordx4 v213, %2, s68 offen offset:1056  lds\n"
  "buffer_load_dwordx4 v214, %2, s68 offen offset:2112  lds\n"
  "buffer_load_dwordx4 v215, %2, s68 offen offset:3168  lds\n"
  "s_add_u32 m0, s66, 73728\n"
  "s_add_u32 s68, s64, 135168\n"
  "buffer_load_dwordx4 v212, %2, s68 offen offset:0  lds\n"
  "buffer_load_dwordx4 v213, %2, s68 offen offset:1056  lds\n"
  "buffer_load_dwordx4 v214, %2, s68 offen offset:2112  lds\n"
  "buffer_load_dwordx4 v215, %2, s68 offen offset:3168  lds\n"
  "s_buffer_load_dword s70, %4, s75 offset:7040\n"
  "s_buffer_load_dword s70, %4, s75 offset:7104\n"
  "s_buffer_load_dword s70, %4, s75 offset:7936\n"
  "s_buffer_load_dword s70, %4, s75 offset:8000\n"
  "s_buffer_load_dword s70, %4, s75 offset:8832\n"
  "s_buffer_load_dword s70, %4, s75 offset:8896\n"
  "s_buffer_load_dword s70, %4, s75 offset:9728\n"
  "s_buffer_load_dword s70, %4, s75 offset:9792\n"
  "s_buffer_load_dword s70, %4, s75 offset:10624\n"
  "s_buffer_load_dword s70, %4, s75 offset:10688\n"
  "s_buffer_load_dword s70, %4, s75 offset:11520\n"
  "s_buffer_load_dword s70, %4, s75 offset:11584\n"
  "s_buffer_load_dword s70, %4, s75 offset:12416\n"
  "s_buffer_load_dword s70, %4, s75 offset:12480\n"
  "s_waitcnt vmcnt(24)\n"
  "s_barrier\n"
  "s_add_u32 m0, s66, 86880\n"
  "s_add_u32 s68, s64, 114688\n"
  "buffer_load_dwordx4 v212, %3, s68 offen offset:0  lds\n"
  "buffer_load_dwordx4 v213, %3, s68 offen offset:1056  lds\n"
  "buffer_load_dwordx4 v214, %3, s68 offen offset:2112  lds\n"
  "buffer_load_dwordx4 v215, %3, s68 offen offset:3168  lds\n"
  "s_add_u32 m0, s66, 91104\n"
  "s_add_u32 s68, s64, 118784\n"
  "buffer_load_dwordx4 v212, %3, s68 offen offset:0  lds\n"
  "buffer_load_dwordx4 v213, %3, s68 offen offset:1056  lds\n"
  "buffer_load_dwordx4 v214, %3, s68 offen offset:2112  lds\n"
  "buffer_load_dwordx4 v215, %3, s68 offen offset:3168  lds\n"
  "s_add_u32 m0, s66, 104256\n"
  "s_add_u32 s68, s64, 147456\n"
  "buffer_load_dwordx4 v212, %2, s68 offen offset:0  lds\n"
  "buffer_load_dwordx4 v213, %2, s68 offen offset:1056  lds\n"
  "buffer_load_dwordx4 v214, %2, s68 offen offset:2112  lds\n"
  "buffer_load_dwordx4 v215, %2, s68 offen offset:3168  lds\n"
  "s_add_u32 m0, s66, 108480\n"
  "s_add_u32 s68, s64, 151552\n"
  "buffer_load_dwordx4 v212, %2, s68 offen offset:0  lds\n"
  "buffer_load_dwordx4 v213, %2, s68 offen offset:1056  lds\n"
  "buffer_load_dwordx4 v214, %2, s68 offen offset:2112  lds\n"
  "buffer_load_dwordx4 v215, %2, s68 offen offset:3168  lds\n"
  "s_buffer_load_dword s70, %4, s75 offset:13312\n"
  "s_buffer_load_dword s70, %4, s75 offset:13376\n"
  "s_buffer_load_dword s70, %4, s75 offset:14208\n"
  "s_buffer_load_dword s70, %4, s75 offset:14272\n"
  "s_buffer_load_dword s70, %4, s75 offset:15104\n"
  "s_buffer_load_dword s70, %4, s75 offset:15168\n"
  "s_buffer_load_dword s70, %4, s75 offset:16000\n"
  "s_buffer_load_dword s70, %4, s75 offset:16064\n"
  "s_buffer_load_dword s70, %4, s75 offset:16896\n"
  "s_buffer_load_dword s70, %4, s75 offset:16960\n"
  "s_buffer_load_dword s70, %4, s75 offset:17792\n"
  "s_buffer_load_dword s70, %4, s75 offset:17856\n"
  "s_buffer_load_dword s70, %4, s75 offset:4480\n"
  "s_buffer_load_dword s70, %4, s75 offset:4544\n"
  "s_waitcnt vmcnt(24)\n"
  "s_barrier\n"
  "s_add_u32 m0, s66, 121632\n"
  "s_add_u32 s68, s64, 131072\n"
  "buffer_load_dwordx4 v212, %3, s68 offen offset:0  lds\n"
  "buffer_load_dwordx4 v213, %3, s68 offen offset:1056  lds\n"
  "buffer_load_dwordx4 v214, %3, s68 offen offset:2112  lds\n"
  "buffer_load_dwordx4 v215, %3, s68 offen offset:3168  lds\n"
  "s_add_u32 m0, s66, 125856\n"
  "s_add_u32 s68, s64, 135168\n"
  "buffer_load_dwordx4 v212, %3, s68 offen offset:0  lds\n"
  "buffer_load_dwordx4 v213, %3, s68 offen offset:1056  lds\n"
  "buffer_load_dwordx4 v214, %3, s68 offen offset:2112  lds\n"
  "buffer_load_dwordx4 v215, %3, s68 offen offset:3168  lds\n"
  "s_add_u32 m0, s66, 0\n"
  "s_add_u32 s68, s64, 163840\n"
  "buffer_load_dwordx4 v212, %2, s68 offen offset:0  lds\n"
  "buffer_load_dwordx4 v213, %2, s68 offen offset:1056  lds\n"
  "buffer_load_dwordx4 v214, %2, s68 offen offset:2112  lds\n"
  "buffer_load_dwordx4 v215, %2, s68 offen offset:3168  lds\n"
  "s_add_u32 m0, s66, 4224\n"
  "s_add_u32 s68, s64, 167936\n"
  "buffer_load_dwordx4 v212, %2, s68 offen offset:0  lds\n"
  "buffer_load_dwordx4 v213, %2, s68 offen offset:1056  lds\n"
  "buffer_load_dwordx4 v214, %2, s68 offen offset:2112  lds\n"
  "buffer_load_dwordx4 v215, %2, s68 offen offset:3168  lds\n"
  "s_buffer_load_dword s70, %4, s75 offset:5376\n"
  "s_buffer_load_dword s70, %4, s75 offset:5440\n"
  "s_buffer_load_dword s70, %4, s75 offset:6272\n"
  "s_buffer_load_dword s70, %4, s75 offset:6336\n"
  "s_buffer_load_dword s70, %4, s75 offset:7168\n"
  "s_buffer_load_dword s70, %4, s75 offset:7232\n"
  "s_buffer_load_dword s70, %4, s75 offset:8064\n"
  "s_buffer_load_dword s70, %4, s75 offset:8128\n"
  "s_buffer_load_dword s70, %4, s75 offset:8960\n"
  "s_buffer_load_dword s70, %4, s75 offset:9024\n"
  "s_buffer_load_dword s70, %4, s75 offset:9856\n"
  "s_buffer_load_dword s70, %4, s75 offset:9920\n"
  "s_buffer_load_dword s70, %4, s75 offset:10752\n"
  "s_buffer_load_dword s70, %4, s75 offset:10816\n"
  "s_waitcnt vmcnt(24)\n"
  "s_barrier\n"
  "s_add_u32 m0, s66, 17376\n"
  "s_add_u32 s68, s64, 147456\n"
  "buffer_load_dwordx4 v212, %3, s68 offen offset:0  lds\n"
  "buffer_load_dwordx4 v213, %3, s68 offen offset:1056  lds\n"
  "buffer_load_dwordx4 v214, %3, s68 offen offset:2112  lds\n"
  "buffer_load_dwordx4 v215, %3, s68 offen offset:3168  lds\n"
  "s_add_u32 m0, s66, 21600\n"
  "s_add_u32 s68, s64, 151552\n"
  "buffer_load_dwordx4 v212, %3, s68 offen offset:0  lds\n"
  "buffer_load_dwordx4 v213, %3, s68 offen offset:1056  lds\n"
  "buffer_load_dwordx4 v214, %3, s68 offen offset:2112  lds\n"
  "buffer_load_dwordx4 v215, %3, s68 offen offset:3168  lds\n"
  "s_add_u32 m0, s66, 34752\n"
  "s_add_u32 s68, s64, 180224\n"
  "buffer_load_dwordx4 v212, %2, s68 offen offset:0  lds\n"
  "buffer_load_dwordx4 v213, %2, s68 offen offset:1056  lds\n"
  "buffer_load_dwordx4 v214, %2, s68 offen offset:2112  lds\n"
  "buffer_load_dwordx4 v215, %2, s68 offen offset:3168  lds\n"
  "s_add_u32 m0, s66, 38976\n"
  "s_add_u32 s68, s64, 184320\n"
  "buffer_load_dwordx4 v212, %2, s68 offen offset:0  lds\n"
  "buffer_load_dwordx4 v213, %2, s68 offen offset:1056  lds\n"
  "buffer_load_dwordx4 v214, %2, s68 offen offset:2112  lds\n"
  "buffer_load_dwordx4 v215, %2, s68 offen offset:3168  lds\n"
  "s_buffer_load_dword s70, %4, s75 offset:11648\n"
  "s_buffer_load_dword s70, %4, s75 offset:11712\n"
  "s_buffer_load_dword s70, %4, s75 offset:12544\n"
  "s_buffer_load_dword s70, %4, s75 offset:12608\n"
  "s_buffer_load_dword s70, %4, s75 offset:13440\n"
  "s_buffer_load_dword s70, %4, s75 offset:13504\n"
  "s_buffer_load_dword s70, %4, s75 offset:14336\n"
  "s_buffer_load_dword s70, %4, s75 offset:14400\n"
  "s_buffer_load_dword s70, %4, s75 offset:15232\n"
  "s_buffer_load_dword s70, %4, s75 offset:15296\n"
  "s_buffer_load_dword s70, %4, s75 offset:16128\n"
  "s_buffer_load_dword s70, %4, s75 offset:16192\n"
  "s_buffer_load_dword s70, %4, s75 offset:17024\n"
  "s_buffer_load_dword s70, %4, s75 offset:17088\n"
  "s_waitcnt vmcnt(24)\n"
  "s_barrier\n"
  "s_add_u32 m0, s66, 52128\n"
  "s_add_u32 s68, s64, 163840\n"
  "buffer_load_dwordx4 v212, %3, s68 offen offset:0  lds\n"
  "buffer_load_dwordx4 v213, %3, s68 offen offset:1056  lds\n"
  "buffer_load_dwordx4 v214, %3, s68 offen offset:2112  lds\n"
  "buffer_load_dwordx4 v215, %3, s68 offen offset:3168  lds\n"
  "s_add_u32 m0, s66, 56352\n"
  "s_add_u32 s68, s64, 167936\n"
  "buffer_load_dwordx4 v212, %3, s68 offen offset:0  lds\n"
  "buffer_load_dwordx4 v213, %3, s68 offen offset:1056  lds\n"
  "buffer_load_dwordx4 v214, %3, s68 offen offset:2112  lds\n"
  "buffer_load_dwordx4 v215, %3, s68 offen offset:3168  lds\n"
  "s_add_u32 m0, s66, 69504\n"
  "s_add_u32 s68, s64, 196608\n"
  "buffer_load_dwordx4 v212, %2, s68 offen offset:0  lds\n"
  "buffer_load_dwordx4 v213, %2, s68 offen offset:1056  lds\n"
  "buffer_load_dwordx4 v214, %2, s68 offen offset:2112  lds\n"
  "buffer_load_dwordx4 v215, %2, s68 offen offset:3168  lds\n"
  "s_add_u32 m0, s66, 73728\n"
  "s_add_u32 s68, s64, 200704\n"
  "buffer_load_dwordx4 v212, %2, s68 offen offset:0  lds\n"
  "buffer_load_dwordx4 v213, %2, s68 offen offset:1056  lds\n"
  "buffer_load_dwordx4 v214, %2, s68 offen offset:2112  lds\n"
  "buffer_load_dwordx4 v215, %2, s68 offen offset:3168  lds\n"
  "s_buffer_load_dword s70, %4, s75 offset:17920\n"
  "s_buffer_load_dword s70, %4, s75 offset:17984\n"
  "s_buffer_load_dword s70, %4, s75 offset:4608\n"
  "s_buffer_load_dword s70, %4, s75 offset:4672\n"
  "s_buffer_load_dword s70, %4, s75 offset:5504\n"
  "s_buffer_load_dword s70, %4, s75 offset:5568\n"
  "s_buffer_load_dword s70, %4, s75 offset:6400\n"
  "s_buffer_load_dword s70, %4, s75 offset:6464\n"
  "s_buffer_load_dword s70, %4, s75 offset:7296\n"
  "s_buffer_load_dword s70, %4, s75 offset:7360\n"
  "s_buffer_load_dword s70, %4, s75 offset:8192\n"
  "s_buffer_load_dword s70, %4, s75 offset:8256\n"
  "s_buffer_load_dword s70, %4, s75 offset:9088\n"
  "s_buffer_load_dword s70, %4, s75 offset:9152\n"
  "s_waitcnt vmcnt(24)\n"
  "s_barrier\n"
  "s_add_u32 m0, s66, 86880\n"
  "s_add_u32 s68, s64, 180224\n"
  "buffer_load_dwordx4 v212, %3, s68 offen offset:0  lds\n"
  "buffer_load_dwordx4 v213, %3, s68 offen offset:1056  lds\n"
  "buffer_load_dwordx4 v214, %3, s68 offen offset:2112  lds\n"
  "buffer_load_dwordx4 v215, %3, s68 offen offset:3168  lds\n"
  "s_add_u32 m0, s66, 91104\n"
  "s_add_u32 s68, s64, 184320\n"
  "buffer_load_dwordx4 v212, %3, s68 offen offset:0  lds\n"
  "buffer_load_dwordx4 v213, %3, s68 offen offset:1056  lds\n"
  "buffer_load_dwordx4 v214, %3, s68 offen offset:2112  lds\n"
  "buffer_load_dwordx4 v215, %3, s68 offen offset:3168  lds\n"
  "s_add_u32 m0, s66, 104256\n"
  "s_add_u32 s68, s64, 212992\n"
  "buffer_load_dwordx4 v212, %2, s68 offen offset:0  lds\n"
  "buffer_load_dwordx4 v213, %2, s68 offen offset:1056  lds\n"
  "buffer_load_dwordx4 v214, %2, s68 offen offset:2112  lds\n"
  "buffer_load_dwordx4 v215, %2, s68 offen offset:3168  lds\n"
  "s_add_u32 m0, s66, 108480\n"
  "s_add_u32 s68, s64, 217088\n"
  "buffer_load_dwordx4 v212, %2, s68 offen offset:0  lds\n"
  "buffer_load_dwordx4 v213, %2, s68 offen offset:1056  lds\n"
  "buffer_load_dwordx4 v214, %2, s68 offen offset:2112  lds\n"
  "buffer_load_dwordx4 v215, %2, s68 offen offset:3168  lds\n"
  "s_buffer_load_dword s70, %4, s75 offset:9984\n"
  "s_buffer_load_dword s70, %4, s75 offset:10048\n"
  "s_buffer_load_dword s70, %4, s75 offset:10880\n"
  "s_buffer_load_dword s70, %4, s75 offset:10944\n"
  "s_buffer_load_dword s70, %4, s75 offset:11776\n"
  "s_buffer_load_dword s70, %4, s75 offset:11840\n"
  "s_buffer_load_dword s70, %4, s75 offset:12672\n"
  "s_buffer_load_dword s70, %4, s75 offset:12736\n"
  "s_buffer_load_dword s70, %4, s75 offset:13568\n"
  "s_buffer_load_dword s70, %4, s75 offset:13632\n"
  "s_buffer_load_dword s70, %4, s75 offset:14464\n"
  "s_buffer_load_dword s70, %4, s75 offset:14528\n"
  "s_buffer_load_dword s70, %4, s75 offset:15360\n"
  "s_buffer_load_dword s70, %4, s75 offset:15424\n"
  "s_waitcnt vmcnt(24)\n"
  "s_barrier\n"
  "s_add_u32 m0, s66, 121632\n"
  "s_add_u32 s68, s64, 196608\n"
  "buffer_load_dwordx4 v212, %3, s68 offen offset:0  lds\n"
  "buffer_load_dwordx4 v213, %3, s68 offen offset:1056  lds\n"
  "buffer_load_dwordx4 v214, %3, s68 offen offset:2112  lds\n"
  "buffer_load_dwordx4 v215, %3, s68 offen offset:3168  lds\n"
  "s_add_u32 m0, s66, 125856\n"
  "s_add_u32 s68, s64, 200704\n"
  "buffer_load_dwordx4 v212, %3, s68 offen offset:0  lds\n"
  "buffer_load_dwordx4 v213, %3, s68 offen offset:1056  lds\n"
  "buffer_load_dwordx4 v214, %3, s68 offen offset:2112  lds\n"
  "buffer_load_dwordx4 v215, %3, s68 offen offset:3168  lds\n"
  "s_add_u32 m0, s66, 0\n"
  "s_add_u32 s68, s64, 229376\n"
  "buffer_load_dwordx4 v212, %2, s68 offen offset:0  lds\n"
  "buffer_load_dwordx4 v213, %2, s68 offen offset:1056  lds\n"
  "buffer_load_dwordx4 v214, %2, s68 offen offset:2112  lds\n"
  "buffer_load_dwordx4 v215, %2, s68 offen offset:3168  lds\n"
  "s_add_u32 m0, s66, 4224\n"
  "s_add_u32 s68, s64, 233472\n"
  "buffer_load_dwordx4 v212, %2, s68 offen offset:0  lds\n"
  "buffer_load_dwordx4 v213, %2, s68 offen offset:1056  lds\n"
  "buffer_load_dwordx4 v214, %2, s68 offen offset:2112  lds\n"
  "buffer_load_dwordx4 v215, %2, s68 offen offset:3168  lds\n"
  "s_buffer_load_dword s70, %4, s75 offset:16256\n"
  "s_buffer_load_dword s70, %4, s75 offset:16320\n"
  "s_buffer_load_dword s70, %4, s75 offset:17152\n"
  "s_buffer_load_dword s70, %4, s75 offset:17216\n"
  "s_buffer_load_dword s70, %4, s75 offset:18048\n"
  "s_buffer_load_dword s70, %4, s75 offset:18112\n"
  "s_buffer_load_dword s70, %4, s75 offset:4736\n"
  "s_buffer_load_dword s70, %4, s75 offset:4800\n"
  "s_buffer_load_dword s70, %4, s75 offset:5632\n"
  "s_buffer_load_dword s70, %4, s75 offset:5696\n"
  "s_buffer_load_dword s70, %4, s75 offset:6528\n"
  "s_buffer_load_dword s70, %4, s75 offset:6592\n"
  "s_buffer_load_dword s70, %4, s75 offset:7424\n"
  "s_buffer_load_dword s70, %4, s75 offset:7488\n"
  "s_waitcnt vmcnt(24)\n"
  "s_barrier\n"
  "s_add_u32 m0, s66, 17376\n"
  "s_add_u32 s68, s64, 212992\n"
  "buffer_load_dwordx4 v212, %3, s68 offen offset:0  lds\n"
  "buffer_load_dwordx4 v213, %3, s68 offen offset:1056  lds\n"
  "buffer_load_dwordx4 v214, %3, s68 offen offset:2112  lds\n"
  "buffer_load_dwordx4 v215, %3, s68 offen offset:3168  lds\n"
  "s_add_u32 m0, s66, 21600\n"
  "s_add_u32 s68, s64, 217088\n"
  "buffer_load_dwordx4 v212, %3, s68 offen offset:0  lds\n"
  "buffer_load_dwordx4 v213, %3, s68 offen offset:1056  lds\n"
  "buffer_load_dwordx4 v214, %3, s68 offen offset:2112  lds\n"
  "buffer_load_dwordx4 v215, %3, s68 offen offset:3168  lds\n"
  "s_add_u32 m0, s66, 34752\n"
  "s_add_u32 s68, s64, 245760\n"
  "buffer_load_dwordx4 v212, %2, s68 offen offset:0  lds\n"
  "buffer_load_dwordx4 v213, %2, s68 offen offset:1056  lds\n"
  "buffer_load_dwordx4 v214, %2, s68 offen offset:2112  lds\n"
  "buffer_load_dwordx4 v215, %2, s68 offen offset:3168  lds\n"
  "s_add_u32 m0, s66, 38976\n"
  "s_add_u32 s68, s64, 249856\n"
  "buffer_load_dwordx4 v212, %2, s68 offen offset:0  lds\n"
  "buffer_load_dwordx4 v213, %2, s68 offen offset:1056  lds\n"
  "buffer_load_dwordx4 v214, %2, s68 offen offset:2112  lds\n"
  "buffer_load_dwordx4 v215, %2, s68 offen offset:3168  lds\n"
  "s_buffer_load_dword s70, %4, s75 offset:8320\n"
  "s_buffer_load_dword s70, %4, s75 offset:8384\n"
  "s_buffer_load_dword s70, %4, s75 offset:9216\n"
  "s_buffer_load_dword s70, %4, s75 offset:9280\n"
  "s_buffer_load_dword s70, %4, s75 offset:10112\n"
  "s_buffer_load_dword s70, %4, s75 offset:10176\n"
  "s_buffer_load_dword s70, %4, s75 offset:11008\n"
  "s_buffer_load_dword s70, %4, s75 offset:11072\n"
  "s_buffer_load_dword s70, %4, s75 offset:11904\n"
  "s_buffer_load_dword s70, %4, s75 offset:11968\n"
  "s_buffer_load_dword s70, %4, s75 offset:12800\n"
  "s_buffer_load_dword s70, %4, s75 offset:12864\n"
  "s_buffer_load_dword s70, %4, s75 offset:13696\n"
  "s_buffer_load_dword s70, %4, s75 offset:13760\n"
  "s_waitcnt vmcnt(24)\n"
  "s_barrier\n"
  "s_add_u32 m0, s66, 52128\n"
  "s_add_u32 s68, s64, 229376\n"
  "buffer_load_dwordx4 v212, %3, s68 offen offset:0  lds\n"
  "buffer_load_dwordx4 v213, %3, s68 offen offset:1056  lds\n"
  "buffer_load_dwordx4 v214, %3, s68 offen offset:2112  lds\n"
  "buffer_load_dwordx4 v215, %3, s68 offen offset:3168  lds\n"
  "s_add_u32 m0, s66, 56352\n"
  "s_add_u32 s68, s64, 233472\n"
  "buffer_load_dwordx4 v212, %3, s68 offen offset:0  lds\n"
  "buffer_load_dwordx4 v213, %3, s68 offen offset:1056  lds\n"
  "buffer_load_dwordx4 v214, %3, s68 offen offset:2112  lds\n"
  "buffer_load_dwordx4 v215, %3, s68 offen offset:3168  lds\n"
  "s_add_u32 m0, s66, 69504\n"
  "s_add_u32 s68, s64, 245760\n"
  "buffer_load_dwordx4 v212, %3, s68 offen offset:0  lds\n"
  "buffer_load_dwordx4 v213, %3, s68 offen offset:1056  lds\n"
  "buffer_load_dwordx4 v214, %3, s68 offen offset:2112  lds\n"
  "buffer_load_dwordx4 v215, %3, s68 offen offset:3168  lds\n"
  "s_add_u32 m0, s66, 73728\n"
  "s_add_u32 s68, s64, 249856\n"
  "buffer_load_dwordx4 v212, %3, s68 offen offset:0  lds\n"
  "buffer_load_dwordx4 v213, %3, s68 offen offset:1056  lds\n"
  "buffer_load_dwordx4 v214, %3, s68 offen offset:2112  lds\n"
  "buffer_load_dwordx4 v215, %3, s68 offen offset:3168  lds\n"
  "s_buffer_load_dword s70, %4, s75 offset:14592\n"
  "s_buffer_load_dword s70, %4, s75 offset:14656\n"
  "s_buffer_load_dword s70, %4, s75 offset:15488\n"
  "s_buffer_load_dword s70, %4, s75 offset:15552\n"
  "s_buffer_load_dword s70, %4, s75 offset:16384\n"
  "s_buffer_load_dword s70, %4, s75 offset:16448\n"
  "s_buffer_load_dword s70, %4, s75 offset:17280\n"
  "s_buffer_load_dword s70, %4, s75 offset:17344\n"
  "s_buffer_load_dword s70, %4, s75 offset:18176\n"
  "s_buffer_load_dword s70, %4, s75 offset:18240\n"
  "s_buffer_load_dword s70, %4, s75 offset:4864\n"
  "s_buffer_load_dword s70, %4, s75 offset:4928\n"
  "s_buffer_load_dword s70, %4, s75 offset:5760\n"
  "s_buffer_load_dword s70, %4, s75 offset:5824\n"
  "s_waitcnt vmcnt(24)\n"
  "s_barrier\n"
  "s_add_u32 m0, s67, 104256\n"
  "s_add_u32 s68, s65, 0\n"
  "buffer_load_dwordx4 v216, %4, s68 offen offset:0  lds\n"
  "buffer_load_dwordx4 v217, %4, s68 offen offset:1088  lds\n"
  "buffer_load_dwordx4 v218, %4, s68 offen offset:2176  lds\n"
  "buffer_load_dwordx4 v219, %4, s68 offen offset:3264  lds\n"
  "s_add_u32 m0, s67, 108616\n"
  "s_add_u32 s68, s65, 3584\n"
  "buffer_load_dwordx4 v216, %4, s68 offen offset:0  lds\n"
  "buffer_load_dwordx4 v217, %4, s68 offen offset:1088  lds\n"
  "buffer_load_dwordx4 v218, %4, s68 offen offset:2176  lds\n"
  "buffer_load_dwordx4 v219, %4, s68 offen offset:3264  lds\n"
  "s_buffer_load_dword s70, %4, s75 offset:6656\n"
  "s_buffer_load_dword s70, %4, s75 offset:6720\n"
  "s_buffer_load_dword s70, %4, s75 offset:7552\n"
  "s_buffer_load_dword s70, %4, s75 offset:7616\n"
  "s_buffer_load_dword s70, %4, s75 offset:8448\n"
  "s_buffer_load_dword s70, %4, s75 offset:8512\n"
  "s_buffer_load_dword s70, %4, s75 offset:9344\n"
  "s_buffer_load_dword s70, %4, s75 offset:9408\n"
  "s_buffer_load_dword s70, %4, s75 offset:10240\n"
  "s_buffer_load_dword s70, %4, s75 offset:10304\n"
  "s_buffer_load_dword s70, %4, s75 offset:11136\n"
  "s_buffer_load_dword s70, %4, s75 offset:11200\n"
  "s_buffer_load_dword s70, %4, s75 offset:12032\n"
  "s_buffer_load_dword s70, %4, s75 offset:12096\n"
  "s_waitcnt vmcnt(16)\n"
  "s_barrier\n"
  "s_add_u32 m0, s67, 121632\n"
  "s_add_u32 s68, s65, 229376\n"
  "buffer_load_dwordx4 v216, %4, s68 offen offset:0  lds\n"
  "buffer_load_dwordx4 v217, %4, s68 offen offset:1088  lds\n"
  "buffer_load_dwordx4 v218, %4, s68 offen offset:2176  lds\n"
  "buffer_load_dwordx4 v219, %4, s68 offen offset:3264  lds\n"
  "s_add_u32 m0, s67, 125992\n"
  "s_add_u32 s68, s65, 232960\n"
  "buffer_load_dwordx4 v216, %4, s68 offen offset:0  lds\n"
  "buffer_load_dwordx4 v217, %4, s68 offen offset:1088  lds\n"
  "buffer_load_dwordx4 v218, %4, s68 offen offset:2176  lds\n"
  "buffer_load_dwordx4 v219, %4, s68 offen offset:3264  lds\n"
  "s_add_u32 m0, s67, 0\n"
  "s_add_u32 s68, s65, 114688\n"
  "buffer_load_dwordx4 v216, %4, s68 offen offset:0  lds\n"
  "buffer_load_dwordx4 v217, %4, s68 offen offset:1088  lds\n"
  "buffer_load_dwordx4 v218, %4, s68 offen offset:2176  lds\n"
  "buffer_load_dwordx4 v219, %4, s68 offen offset:3264  lds\n"
  "s_add_u32 m0, s67, 4360\n"
  "s_add_u32 s68, s65, 118272\n"
  "buffer_load_dwordx4 v216, %4, s68 offen offset:0  lds\n"
  "buffer_load_dwordx4 v217, %4, s68 offen offset:1088  lds\n"
  "buffer_load_dwordx4 v218, %4, s68 offen offset:2176  lds\n"
  "buffer_load_dwordx4 v219, %4, s68 offen offset:3264  lds\n"
  "s_buffer_load_dword s70, %4, s75 offset:12928\n"
  "s_buffer_load_dword s70, %4, s75 offset:12992\n"
  "s_buffer_load_dword s70, %4, s75 offset:13824\n"
  "s_buffer_load_dword s70, %4, s75 offset:13888\n"
  "s_buffer_load_dword s70, %4, s75 offset:14720\n"
  "s_buffer_load_dword s70, %4, s75 offset:14784\n"
  "s_buffer_load_dword s70, %4, s75 offset:15616\n"
  "s_buffer_load_dword s70, %4, s75 offset:15680\n"
  "s_buffer_load_dword s70, %4, s75 offset:16512\n"
  "s_buffer_load_dword s70, %4, s75 offset:16576\n"
  "s_buffer_load_dword s70, %4, s75 offset:17408\n"
  "s_buffer_load_dword s70, %4, s75 offset:17472\n"
  "s_buffer_load_dword s70, %4, s75 offset:18304\n"
  "s_buffer_load_dword s70, %4, s75 offset:18368\n"
  "s_waitcnt vmcnt(24)\n"
  "s_barrier\n"
  "s_add_u32 m0, s67, 17376\n"
  "s_add_u32 s68, s65, 344064\n"
  "buffer_load_dwordx4 v216, %4, s68 offen offset:0  lds\n"
  "buffer_load_dwordx4 v217, %4, s68 offen offset:1088  lds\n"
  "buffer_load_dwordx4 v218, %4, s68 offen offset:2176  lds\n"
  "buffer_load_dwordx4 v219, %4, s68 offen offset:3264  lds\n"
  "s_add_u32 m0, s67, 21736\n"
  "s_add_u32 s68, s65, 347648\n"
  "buffer_load_dwordx4 v216, %4, s68 offen offset:0  lds\n"
  "buffer_load_dwordx4 v217, %4, s68 offen offset:1088  lds\n"
  "buffer_load_dwordx4 v218, %4, s68 offen offset:2176  lds\n"
  "buffer_load_dwordx4 v219, %4, s68 offen offset:3264  lds\n"
  "s_add_u32 m0, s67, 34752\n"
  "s_add_u32 s68, s65, 128\n"
  "buffer_load_dwordx4 v216, %4, s68 offen offset:0  lds\n"
  "buffer_load_dwordx4 v217, %4, s68 offen offset:1088  lds\n"
  "buffer_load_dwordx4 v218, %4, s68 offen offset:2176  lds\n"
  "buffer_load_dwordx4 v219, %4, s68 offen offset:3264  lds\n"
  "s_add_u32 m0, s67, 39112\n"
  "s_add_u32 s68, s65, 3712\n"
  "buffer_load_dwordx4 v216, %4, s68 offen offset:0  lds\n"
  "buffer_load_dwordx4 v217, %4, s68 offen offset:1088  lds\n"
  "buffer_load_dwordx4 v218, %4, s68 offen offset:2176  lds\n"
  "buffer_load_dwordx4 v219, %4, s68 offen offset:3264  lds\n"
  "s_waitcnt vmcnt(24)\n"
  "s_barrier\n"
  "s_add_u32 m0, s67, 52128\n"
  "s_add_u32 s68, s65, 229504\n"
  "buffer_load_dwordx4 v216, %4, s68 offen offset:0  lds\n"
  "buffer_load_dwordx4 v217, %4, s68 offen offset:1088  lds\n"
  "buffer_load_dwordx4 v218, %4, s68 offen offset:2176  lds\n"
  "buffer_load_dwordx4 v219, %4, s68 offen offset:3264  lds\n"
  "s_add_u32 m0, s67, 56488\n"
  "s_add_u32 s68, s65, 233088\n"
  "buffer_load_dwordx4 v216, %4, s68 offen offset:0  lds\n"
  "buffer_load_dwordx4 v217, %4, s68 offen offset:1088  lds\n"
  "buffer_load_dwordx4 v218, %4, s68 offen offset:2176  lds\n"
  "buffer_load_dwordx4 v219, %4, s68 offen offset:3264  lds\n"
  "s_waitcnt vmcnt(32)\n"
  "s_barrier\n"
  "s_add_u32 m0, s67, 69504\n"
  "s_add_u32 s68, s65, 114816\n"
  "buffer_load_dwordx4 v216, %4, s68 offen offset:0  lds\n"
  "buffer_load_dwordx4 v217, %4, s68 offen offset:1088  lds\n"
  "buffer_load_dwordx4 v218, %4, s68 offen offset:2176  lds\n"
  "buffer_load_dwordx4 v219, %4, s68 offen offset:3264  lds\n"
  "s_add_u32 m0, s67, 73864\n"
  "s_add_u32 s68, s65, 118400\n"
  "buffer_load_dwordx4 v216, %4, s68 offen offset:0  lds\n"
  "buffer_load_dwordx4 v217, %4, s68 offen offset:1088  lds\n"
  "buffer_load_dwordx4 v218, %4, s68 offen offset:2176  lds\n"
  "buffer_load_dwordx4 v219, %4, s68 offen offset:3264  lds\n"
  "s_waitcnt vmcnt(24)\n"
  "s_barrier\n"
  "s_add_u32 m0, s67, 104256\n"
  "s_add_u32 s68, s65, 344192\n"
  "buffer_load_dwordx4 v216, %4, s68 offen offset:0  lds\n"
  "buffer_load_dwordx4 v217, %4, s68 offen offset:1088  lds\n"
  "buffer_load_dwordx4 v218, %4, s68 offen offset:2176  lds\n"
  "buffer_load_dwordx4 v219, %4, s68 offen offset:3264  lds\n"
  "s_add_u32 m0, s67, 108616\n"
  "s_add_u32 s68, s65, 347776\n"
  "buffer_load_dwordx4 v216, %4, s68 offen offset:0  lds\n"
  "buffer_load_dwordx4 v217, %4, s68 offen offset:1088  lds\n"
  "buffer_load_dwordx4 v218, %4, s68 offen offset:2176  lds\n"
  "buffer_load_dwordx4 v219, %4, s68 offen offset:3264  lds\n"
  "s_waitcnt vmcnt(16)\n"
  "s_barrier\n"
  "s_add_u32 m0, s67, 121632\n"
  "s_add_u32 s68, s65, 256\n"
  "buffer_load_dwordx4 v216, %4, s68 offen offset:0  lds\n"
  "buffer_load_dwordx4 v217, %4, s68 offen offset:1088  lds\n"
  "buffer_load_dwordx4 v218, %4, s68 offen offset:2176  lds\n"
  "buffer_load_dwordx4 v219, %4, s68 offen offset:3264  lds\n"
  "s_add_u32 m0, s67, 125992\n"
  "s_add_u32 s68, s65, 3840\n"
  "buffer_load_dwordx4 v216, %4, s68 offen offset:0  lds\n"
  "buffer_load_dwordx4 v217, %4, s68 offen offset:1088  lds\n"
  "buffer_load_dwordx4 v218, %4, s68 offen offset:2176  lds\n"
  "buffer_load_dwordx4 v219, %4, s68 offen offset:3264  lds\n"
  "s_add_u32 m0, s67, 0\n"
  "s_add_u32 s68, s65, 229632\n"
  "buffer_load_dwordx4 v216, %4, s68 offen offset:0  lds\n"
  "buffer_load_dwordx4 v217, %4, s68 offen offset:1088  lds\n"
  "buffer_load_dwordx4 v218, %4, s68 offen offset:2176  lds\n"
  "buffer_load_dwordx4 v219, %4, s68 offen offset:3264  lds\n"
  "s_add_u32 m0, s67, 4360\n"
  "s_add_u32 s68, s65, 233216\n"
  "buffer_load_dwordx4 v216, %4, s68 offen offset:0  lds\n"
  "buffer_load_dwordx4 v217, %4, s68 offen offset:1088  lds\n"
  "buffer_load_dwordx4 v218, %4, s68 offen offset:2176  lds\n"
  "buffer_load_dwordx4 v219, %4, s68 offen offset:3264  lds\n"
  "s_add_u32 m0, s67, 86880\n"
  "s_add_u32 s68, s65, 114944\n"
  "buffer_load_dwordx4 v216, %4, s68 offen offset:0  lds\n"
  "s_waitcnt vmcnt(17)\n"
  "s_barrier\n"
  "s_add_u32 m0, s67, 86880\n"
  "s_add_u32 s68, s65, 114944\n"
  "buffer_load_dwordx4 v217, %4, s68 offen offset:1088  lds\n"
  "buffer_load_dwordx4 v218, %4, s68 offen offset:2176  lds\n"
  "buffer_load_dwordx4 v219, %4, s68 offen offset:3264  lds\n"
  "s_add_u32 m0, s67, 91240\n"
  "s_add_u32 s68, s65, 118528\n"
  "buffer_load_dwordx4 v216, %4, s68 offen offset:0  lds\n"
  "buffer_load_dwordx4 v217, %4, s68 offen offset:1088  lds\n"
  "buffer_load_dwordx4 v218, %4, s68 offen offset:2176  lds\n"
  "buffer_load_dwordx4 v219, %4, s68 offen offset:3264  lds\n"
  "s_add_u32 m0, s67, 17376\n"
  "s_add_u32 s68, s65, 344320\n"
  "buffer_load_dwordx4 v216, %4, s68 offen offset:0  lds\n"
  "buffer_load_dwordx4 v217, %4, s68 offen offset:1088  lds\n"
  "buffer_load_dwordx4 v218, %4, s68 offen offset:2176  lds\n"
  "buffer_load_dwordx4 v219, %4, s68 offen offset:3264  lds\n"
  "s_add_u32 m0, s67, 21736\n"
  "s_add_u32 s68, s65, 347904\n"
  "buffer_load_dwordx4 v216, %4, s68 offen offset:0  lds\n"
  "buffer_load_dwordx4 v217, %4, s68 offen offset:1088  lds\n"
  "buffer_load_dwordx4 v218, %4, s68 offen offset:2176  lds\n"
  "buffer_load_dwordx4 v219, %4, s68 offen offset:3264  lds\n"
  "s_add_u32 m0, s67, 34752\n"
  "s_add_u32 s68, s65, 384\n"
  "buffer_load_dwordx4 v216, %4, s68 offen offset:0  lds\n"
  "buffer_load_dwordx4 v217, %4, s68 offen offset:1088  lds\n"
  "s_waitcnt vmcnt(18)\n"
  "s_barrier\n"
  "s_add_u32 m0, s67, 34752\n"
  "s_add_u32 s68, s65, 384\n"
  "buffer_load_dwordx4 v218, %4, s68 offen offset:2176  lds\n"
  "buffer_load_dwordx4 v219, %4, s68 offen offset:3264  lds\n"
  "s_add_u32 m0, s67, 39112\n"
  "s_add_u32 s68, s65, 3968\n"
  "buffer_load_dwordx4 v216, %4, s68 offen offset:0  lds\n"
  "buffer_load_dwordx4 v217, %4, s68 offen offset:1088  lds\n"
  "buffer_load_dwordx4 v218, %4, s68 offen offset:2176  lds\n"
  "buffer_load_dwordx4 v219, %4, s68 offen offset:3264  lds\n"
  "s_add_u32 m0, s67, 52128\n"
  "s_add_u32 s68, s65, 229760\n"
  "buffer_load_dwordx4 v216, %4, s68 offen offset:0  lds\n"
  "buffer_load_dwordx4 v217, %4, s68 offen offset:1088  lds\n"
  "buffer_load_dwordx4 v218, %4, s68 offen offset:2176  lds\n"
  "buffer_load_dwordx4 v219, %4, s68 offen offset:3264  lds\n"
  "s_add_u32 m0, s67, 56488\n"
  "s_add_u32 s68, s65, 233344\n"
  "buffer_load_dwordx4 v216, %4, s68 offen offset:0  lds\n"
  "buffer_load_dwordx4 v217, %4, s68 offen offset:1088  lds\n"
  "buffer_load_dwordx4 v218, %4, s68 offen offset:2176  lds\n"
  "buffer_load_dwordx4 v219, %4, s68 offen offset:3264  lds\n"
  "s_add_u32 m0, s67, 69504\n"
  "s_add_u32 s68, s65, 115072\n"
  "buffer_load_dwordx4 v216, %4, s68 offen offset:0  lds\n"
  "buffer_load_dwordx4 v217, %4, s68 offen offset:1088  lds\n"
  "buffer_load_dwordx4 v218, %4, s68 offen offset:2176  lds\n"
  "s_waitcnt vmcnt(19)\n"
  "s_barrier\n"
  "s_add_u32 m0, s67, 69504\n"
  "s_add_u32 s68, s65, 115072\n"
  "buffer_load_dwordx4 v219, %4, s68 offen offset:3264  lds\n"
  "s_add_u32 m0, s67, 73864\n"
  "s_add_u32 s68, s65, 118656\n"
  "buffer_load_dwordx4 v216, %4, s68 offen offset:0  lds\n"
  "buffer_load_dwordx4 v217, %4, s68 offen offset:1088  lds\n"
  "buffer_load_dwordx4 v218, %4, s68 offen offset:2176  lds\n"
  "buffer_load_dwordx4 v219, %4, s68 offen offset:3264  lds\n"
  "s_add_u32 m0, s67, 104256\n"
  "s_add_u32 s68, s65, 344448\n"
  "buffer_load_dwordx4 v216, %4, s68 offen offset:0  lds\n"
  "buffer_load_dwordx4 v217, %4, s68 offen offset:1088  lds\n"
  "buffer_load_dwordx4 v218, %4, s68 offen offset:2176  lds\n"
  "buffer_load_dwordx4 v219, %4, s68 offen offset:3264  lds\n"
  "s_add_u32 m0, s67, 108616\n"
  "s_add_u32 s68, s65, 348032\n"
  "buffer_load_dwordx4 v216, %4, s68 offen offset:0  lds\n"
  "buffer_load_dwordx4 v217, %4, s68 offen offset:1088  lds\n"
  "buffer_load_dwordx4 v218, %4, s68 offen offset:2176  lds\n"
  "buffer_load_dwordx4 v219, %4, s68 offen offset:3264  lds\n"
  "s_add_u32 m0, s67, 121632\n"
  "s_add_u32 s68, s65, 512\n"
  "buffer_load_dwordx4 v216, %4, s68 offen offset:0  lds\n"
  "buffer_load_dwordx4 v217, %4, s68 offen offset:1088  lds\n"
  "buffer_load_dwordx4 v218, %4, s68 offen offset:2176  lds\n"
  "buffer_load_dwordx4 v219, %4, s68 offen offset:3264  lds\n"
  "s_waitcnt vmcnt(20)\n"
  "s_barrier\n"
  "s_add_u32 m0, s67, 125992\n"
  "s_add_u32 s68, s65, 4096\n"
  "buffer_load_dwordx4 v216, %4, s68 offen offset:0  lds\n"
  "buffer_load_dwordx4 v217, %4, s68 offen offset:1088  lds\n"
  "buffer_load_dwordx4 v218, %4, s68 offen offset:2176  lds\n"
  "buffer_load_dwordx4 v219, %4, s68 offen offset:3264  lds\n"
  "s_add_u32 m0, s67, 0\n"
  "s_add_u32 s68, s65, 229888\n"
  "buffer_load_dwordx4 v216, %4, s68 offen offset:0  lds\n"
  "buffer_load_dwordx4 v217, %4, s68 offen offset:1088  lds\n"
  "buffer_load_dwordx4 v218, %4, s68 offen offset:2176  lds\n"
  "buffer_load_dwordx4 v219, %4, s68 offen offset:3264  lds\n"
  "s_add_u32 m0, s67, 4360\n"
  "s_add_u32 s68, s65, 233472\n"
  "buffer_load_dwordx4 v216, %4, s68 offen offset:0  lds\n"
  "buffer_load_dwordx4 v217, %4, s68 offen offset:1088  lds\n"
  "buffer_load_dwordx4 v218, %4, s68 offen offset:2176  lds\n"
  "buffer_load_dwordx4 v219, %4, s68 offen offset:3264  lds\n"
  "s_add_u32 m0, s67, 86880\n"
  "s_add_u32 s68, s65, 115200\n"
  "buffer_load_dwordx4 v216, %4, s68 offen offset:0  lds\n"
  "buffer_load_dwordx4 v217, %4, s68 offen offset:1088  lds\n"
  "buffer_load_dwordx4 v218, %4, s68 offen offset:2176  lds\n"
  "buffer_load_dwordx4 v219, %4, s68 offen offset:3264  lds\n"
  "s_add_u32 m0, s67, 91240\n"
  "s_add_u32 s68, s65, 118784\n"
  "buffer_load_dwordx4 v216, %4, s68 offen offset:0  lds\n"
  "s_waitcnt vmcnt(21)\n"
  "s_barrier\n"
  "s_add_u32 m0, s67, 91240\n"
  "s_add_u32 s68, s65, 118784\n"
  "buffer_load_dwordx4 v217, %4, s68 offen offset:1088  lds\n"
  "buffer_load_dwordx4 v218, %4, s68 offen offset:2176  lds\n"
  "buffer_load_dwordx4 v219, %4, s68 offen offset:3264  lds\n"
  "s_add_u32 m0, s67, 17376\n"
  "s_add_u32 s68, s65, 344576\n"
  "buffer_load_dwordx4 v216, %4, s68 offen offset:0  lds\n"
  "buffer_load_dwordx4 v217, %4, s68 offen offset:1088  lds\n"
  "buffer_load_dwordx4 v218, %4, s68 offen offset:2176  lds\n"
  "buffer_load_dwordx4 v219, %4, s68 offen offset:3264  lds\n"
  "s_add_u32 m0, s67, 21736\n"
  "s_add_u32 s68, s65, 348160\n"
  "buffer_load_dwordx4 v216, %4, s68 offen offset:0  lds\n"
  "buffer_load_dwordx4 v217, %4, s68 offen offset:1088  lds\n"
  "buffer_load_dwordx4 v218, %4, s68 offen offset:2176  lds\n"
  "buffer_load_dwordx4 v219, %4, s68 offen offset:3264  lds\n"
  "s_add_u32 m0, s67, 34752\n"
  "s_add_u32 s68, s65, 640\n"
  "buffer_load_dwordx4 v216, %4, s68 offen offset:0  lds\n"
  "buffer_load_dwordx4 v217, %4, s68 offen offset:1088  lds\n"
  "buffer_load_dwordx4 v218, %4, s68 offen offset:2176  lds\n"
  "buffer_load_dwordx4 v219, %4, s68 offen offset:3264  lds\n"
  "s_add_u32 m0, s67, 39112\n"
  "s_add_u32 s68, s65, 4224\n"
  "buffer_load_dwordx4 v216, %4, s68 offen offset:0  lds\n"
  "buffer_load_dwordx4 v217, %4, s68 offen offset:1088  lds\n"
  "s_waitcnt vmcnt(22)\n"
  "s_barrier\n"
  "s_add_u32 m0, s67, 39112\n"
  "s_add_u32 s68, s65, 4224\n"
  "buffer_load_dwordx4 v218, %4, s68 offen offset:2176  lds\n"
  "buffer_load_dwordx4 v219, %4, s68 offen offset:3264  lds\n"
  "s_add_u32 m0, s67, 52128\n"
  "s_add_u32 s68, s65, 230016\n"
  "buffer_load_dwordx4 v216, %4, s68 offen offset:0  lds\n"
  "buffer_load_dwordx4 v217, %4, s68 offen offset:1088  lds\n"
  "buffer_load_dwordx4 v218, %4, s68 offen offset:2176  lds\n"
  "buffer_load_dwordx4 v219, %4, s68 offen offset:3264  lds\n"
  "s_add_u32 m0, s67, 56488\n"
  "s_add_u32 s68, s65, 233600\n"
  "buffer_load_dwordx4 v216, %4, s68 offen offset:0  lds\n"
  "buffer_load_dwordx4 v217, %4, s68 offen offset:1088  lds\n"
  "buffer_load_dwordx4 v218, %4, s68 offen offset:2176  lds\n"
  "buffer_load_dwordx4 v219, %4, s68 offen offset:3264  lds\n"
  "s_add_u32 m0, s67, 69504\n"
  "s_add_u32 s68, s65, 115328\n"
  "buffer_load_dwordx4 v216, %4, s68 offen offset:0  lds\n"
  "buffer_load_dwordx4 v217, %4, s68 offen offset:1088  lds\n"
  "buffer_load_dwordx4 v218, %4, s68 offen offset:2176  lds\n"
  "buffer_load_dwordx4 v219, %4, s68 offen offset:3264  lds\n"
  "s_add_u32 m0, s67, 73864\n"
  "s_add_u32 s68, s65, 118912\n"
  "buffer_load_dwordx4 v216, %4, s68 offen offset:0  lds\n"
  "buffer_load_dwordx4 v217, %4, s68 offen offset:1088  lds\n"
  "buffer_load_dwordx4 v218, %4, s68 offen offset:2176  lds\n"
  "s_waitcnt vmcnt(23)\n"
  "s_barrier\n"
  "s_add_u32 m0, s67, 73864\n"
  "s_add_u32 s68, s65, 118912\n"
  "buffer_load_dwordx4 v219, %4, s68 offen offset:3264  lds\n"
  "s_add_u32 m0, s67, 104256\n"
  "s_add_u32 s68, s65, 344704\n"
  "buffer_load_dwordx4 v216, %4, s68 offen offset:0  lds\n"
  "buffer_load_dwordx4 v217, %4, s68 offen offset:1088  lds\n"
  "buffer_load_dwordx4 v218, %4, s68 offen offset:2176  lds\n"
  "buffer_load_dwordx4 v219, %4, s68 offen offset:3264  lds\n"
  "s_add_u32 m0, s67, 108616\n"
  "s_add_u32 s68, s65, 348288\n"
  "buffer_load_dwordx4 v216, %4, s68 offen offset:0  lds\n"
  "buffer_load_dwordx4 v217, %4, s68 offen offset:1088  lds\n"
  "buffer_load_dwordx4 v218, %4, s68 offen offset:2176  lds\n"
  "buffer_load_dwordx4 v219, %4, s68 offen offset:3264  lds\n"
  "s_add_u32 m0, s67, 121632\n"
  "s_add_u32 s68, s65, 768\n"
  "buffer_load_dwordx4 v216, %4, s68 offen offset:0  lds\n"
  "buffer_load_dwordx4 v217, %4, s68 offen offset:1088  lds\n"
  "buffer_load_dwordx4 v218, %4, s68 offen offset:2176  lds\n"
  "buffer_load_dwordx4 v219, %4, s68 offen offset:3264  lds\n"
  "s_add_u32 m0, s67, 125992\n"
  "s_add_u32 s68, s65, 4352\n"
  "buffer_load_dwordx4 v216, %4, s68 offen offset:0  lds\n"
  "buffer_load_dwordx4 v217, %4, s68 offen offset:1088  lds\n"
  "buffer_load_dwordx4 v218, %4, s68 offen offset:2176  lds\n"
  "buffer_load_dwordx4 v219, %4, s68 offen offset:3264  lds\n"
  "s_waitcnt vmcnt(24)\n"
  "s_barrier\n"
  "s_add_u32 m0, s67, 0\n"
  "s_add_u32 s68, s65, 230144\n"
  "buffer_load_dwordx4 v216, %4, s68 offen offset:0  lds\n"
  "buffer_load_dwordx4 v217, %4, s68 offen offset:1088  lds\n"
  "buffer_load_dwordx4 v218, %4, s68 offen offset:2176  lds\n"
  "buffer_load_dwordx4 v219, %4, s68 offen offset:3264  lds\n"
  "s_add_u32 m0, s67, 4360\n"
  "s_add_u32 s68, s65, 233728\n"
  "buffer_load_dwordx4 v216, %4, s68 offen offset:0  lds\n"
  "buffer_load_dwordx4 v217, %4, s68 offen offset:1088  lds\n"
  "buffer_load_dwordx4 v218, %4, s68 offen offset:2176  lds\n"
  "buffer_load_dwordx4 v219, %4, s68 offen offset:3264  lds\n"
  "s_add_u32 m0, s67, 86880\n"
  "s_add_u32 s68, s65, 115456\n"
  "buffer_load_dwordx4 v216, %4, s68 offen offset:0  lds\n"
  "buffer_load_dwordx4 v217, %4, s68 offen offset:1088  lds\n"
  "buffer_load_dwordx4 v218, %4, s68 offen offset:2176  lds\n"
  "buffer_load_dwordx4 v219, %4, s68 offen offset:3264  lds\n"
  "s_add_u32 m0, s67, 91240\n"
  "s_add_u32 s68, s65, 119040\n"
  "buffer_load_dwordx4 v216, %4, s68 offen offset:0  lds\n"
  "buffer_load_dwordx4 v217, %4, s68 offen offset:1088  lds\n"
  "buffer_load_dwordx4 v218, %4, s68 offen offset:2176  lds\n"
  "buffer_load_dwordx4 v219, %4, s68 offen offset:3264  lds\n"
  "s_waitcnt vmcnt(24)\n"
  "s_barrier\n"
  "s_add_u32 m0, s67, 17376\n"
  "s_add_u32 s68, s65, 344832\n"
  "buffer_load_dwordx4 v216, %4, s68 offen offset:0  lds\n"
  "buffer_load_dwordx4 v217, %4, s68 offen offset:1088  lds\n"
  "buffer_load_dwordx4 v218, %4, s68 offen offset:2176  lds\n"
  "buffer_load_dwordx4 v219, %4, s68 offen offset:3264  lds\n"
  "s_add_u32 m0, s67, 21736\n"
  "s_add_u32 s68, s65, 348416\n"
  "buffer_load_dwordx4 v216, %4, s68 offen offset:0  lds\n"
  "buffer_load_dwordx4 v217, %4, s68 offen offset:1088  lds\n"
  "buffer_load_dwordx4 v218, %4, s68 offen offset:2176  lds\n"
  "buffer_load_dwordx4 v219, %4, s68 offen offset:3264  lds\n"
  "s_waitcnt vmcnt(16)\n"
  "s_barrier\n"
  "s_waitcnt vmcnt(0)\n"
  "s_barrier\n"
  "s_waitcnt vmcnt(0) lgkmcnt(0)\n"
        :: "s"(sw), "s"(sw), "s"(sk), "s"(sv), "s"(so), "s"(p), "s"(ranks)
        : "v200","v201","v202","v203","v204","v205","v206","v207","v208","v209","v210","v211","v212","v213","v214","v215","v216","v217","v218","v219","v220","v221","v222","v223","v224","v225","v226","v227","v228","v229","v230","v231","v232","v233","v234","v235","s60","s61","s62","s63","s64","s65","s66","s67","s68","s69","s70","s71","s72","s73","s74","s75","s76","s77","s78","s79","scc","memory");
    } else {
      asm volatile(
  "v_mbcnt_lo_u32_b32 v200, -1, 0\n"
  "v_mbcnt_hi_u32_b32 v200, -1, v200\n"
  "s_lshr_b32 s72, %21, 8\n"
  "s_mul_i32 s60, %20, 1792\n"
  "s_mul_i32 s62, %20, 2112\n"
  "s_mul_i32 s63, %20, 4360\n"
  "s_lshl_b32 s61, %20, 16\n"
  "v_lshrrev_b32 v201, 5, v200\n"
  "v_lshlrev_b32 v201, 15, v201\n"
  "v_and_b32 v220, 31, v200\n"
  "v_lshl_add_u32 v208, v220, 4, v201\n"
  "v_add_u32 v209, 7104, v208\n"
  "v_add_u32 v210, 14208, v208\n"
  "v_add_u32 v211, 21312, v208\n"
  "v_lshlrev_b32 v201, 4, v200\n"
  "s_add_u32 s72, s72, 1\n"
  "s_cmp_eq_u32 s72, 7\n"
  "s_cselect_b32 s72, 0, s72\n"
  "s_waitcnt vmcnt(4)\n"
  "s_barrier\n"
  "s_lshl_b32 s73, s72, 19\n"
  "s_add_u32 s73, s73, s61\n"
  "s_add_u32 s72, s72, 1\n"
  "s_cmp_eq_u32 s72, 7\n"
  "s_cselect_b32 s72, 0, s72\n"
  "s_add_u32 m0, s63, 69504\n"
  "s_add_u32 s68, s73, 0\n"
  "buffer_load_dwordx4 v208, %16, s68 offen offset:0  lds\n"
  "buffer_load_dwordx4 v209, %16, s68 offen offset:1088  lds\n"
  "buffer_load_dwordx4 v210, %16, s68 offen offset:2176  lds\n"
  "buffer_load_dwordx4 v211, %16, s68 offen offset:3264  lds\n"
  "s_add_u32 m0, s63, 86880\n"
  "s_add_u32 s68, s73, 512\n"
  "buffer_load_dwordx4 v208, %16, s68 offen offset:0  lds\n"
  "buffer_load_dwordx4 v209, %16, s68 offen offset:1088  lds\n"
  "buffer_load_dwordx4 v210, %16, s68 offen offset:2176  lds\n"
  "buffer_load_dwordx4 v211, %16, s68 offen offset:3264  lds\n"
  "s_bfe_u32 s75, %21, 0x40001\n"
  "s_mul_i32 s75, s75, 28672\n"
  "s_mul_i32 s69, %20, 14336\n"
  "s_add_u32 s75, s75, s69\n"
  "s_bfe_u32 s74, %21, 0x40001\n"
  "s_lshl_b32 s74, s74, 10\n"
  "s_lshl_b32 s69, %20, 8\n"
  "s_add_u32 s74, s74, s69\n"
  "v_mov_b32 v128, 0x3f803f80\n"
  "v_mov_b32 v129, 0x3f803f80\n"
  "v_mov_b32 v130, 0x3f803f80\n"
  "v_mov_b32 v131, 0x3f803f80\n"
  "s_waitcnt vmcnt(8)\n"
  "s_barrier\n"
  "s_add_u32 m0, s63, 104256\n"
  "s_add_u32 s68, s73, 262144\n"
  "buffer_load_dwordx4 v208, %16, s68 offen offset:0  lds\n"
  "buffer_load_dwordx4 v209, %16, s68 offen offset:1088  lds\n"
  "buffer_load_dwordx4 v210, %16, s68 offen offset:2176  lds\n"
  "buffer_load_dwordx4 v211, %16, s68 offen offset:3264  lds\n"
  "s_add_u32 m0, s63, 121632\n"
  "s_add_u32 s68, s73, 262656\n"
  "buffer_load_dwordx4 v208, %16, s68 offen offset:0  lds\n"
  "buffer_load_dwordx4 v209, %16, s68 offen offset:1088  lds\n"
  "buffer_load_dwordx4 v210, %16, s68 offen offset:2176  lds\n"
  "buffer_load_dwordx4 v211, %16, s68 offen offset:3264  lds\n"
  "v_mov_b32 v132, 0x3f803f80\n"
  "v_mov_b32 v133, 0x3f803f80\n"
  "v_mov_b32 v134, 0x3f803f80\n"
  "v_mov_b32 v135, 0x3f803f80\n"
  "s_waitcnt vmcnt(8)\n"
  "s_barrier\n"
  "s_lshl_b32 s73, s72, 19\n"
  "s_add_u32 s73, s73, s61\n"
  "s_add_u32 s72, s72, 1\n"
  "s_cmp_eq_u32 s72, 7\n"
  "s_cselect_b32 s72, 0, s72\n"
  "s_add_u32 m0, s63, 0\n"
  "s_add_u32 s68, s73, 0\n"
  "buffer_load_dwordx4 v208, %16, s68 offen offset:0  lds\n"
  "buffer_load_dwordx4 v209, %16, s68 offen offset:1088  lds\n"
  "buffer_load_dwordx4 v210, %16, s68 offen offset:2176  lds\n"
  "buffer_load_dwordx4 v211, %16, s68 offen offset:3264  lds\n"
  "s_add_u32 m0, s63, 17376\n"
  "s_add_u32 s68, s73, 512\n"
  "buffer_load_dwordx4 v208, %16, s68 offen offset:0  lds\n"
  "buffer_load_dwordx4 v209, %16, s68 offen offset:1088  lds\n"
  "buffer_load_dwordx4 v210, %16, s68 offen offset:2176  lds\n"
  "buffer_load_dwordx4 v211, %16, s68 offen offset:3264  lds\n"
  "v_mov_b32 v136, 0x3f803f80\n"
  "v_mov_b32 v137, 0x3f803f80\n"
  "v_mov_b32 v138, 0x3f803f80\n"
  "v_mov_b32 v139, 0x3f803f80\n"
  "s_buffer_load_dword s70, %17, s74 offset:4096\n"
  "s_buffer_load_dword s70, %17, s74 offset:4160\n"
  "s_buffer_load_dword s70, %17, s74 offset:4224\n"
  "s_waitcnt vmcnt(8)\n"
  "s_barrier\n"
  "s_add_u32 m0, s63, 34752\n"
  "s_add_u32 s68, s73, 262144\n"
  "buffer_load_dwordx4 v208, %16, s68 offen offset:0  lds\n"
  "buffer_load_dwordx4 v209, %16, s68 offen offset:1088  lds\n"
  "buffer_load_dwordx4 v210, %16, s68 offen offset:2176  lds\n"
  "buffer_load_dwordx4 v211, %16, s68 offen offset:3264  lds\n"
  "s_add_u32 m0, s63, 52128\n"
  "s_add_u32 s68, s73, 262656\n"
  "buffer_load_dwordx4 v208, %16, s68 offen offset:0  lds\n"
  "buffer_load_dwordx4 v209, %16, s68 offen offset:1088  lds\n"
  "buffer_load_dwordx4 v210, %16, s68 offen offset:2176  lds\n"
  "buffer_load_dwordx4 v211, %16, s68 offen offset:3264  lds\n"
  "v_mov_b32 v140, 0x3f803f80\n"
  "v_mov_b32 v141, 0x3f803f80\n"
  "v_mov_b32 v142, 0x3f803f80\n"
  "v_mov_b32 v143, 0x3f803f80\n"
  "s_buffer_load_dword s70, %17, s74 offset:4288\n"
  "s_buffer_load_dword s70, %17, s74 offset:20480\n"
  "s_buffer_load_dword s70, %17, s74 offset:20544\n"
  "s_waitcnt vmcnt(8)\n"
  "s_barrier\n"
  "s_lshl_b32 s73, s72, 19\n"
  "s_add_u32 s73, s73, s61\n"
  "s_add_u32 s72, s72, 1\n"
  "s_cmp_eq_u32 s72, 7\n"
  "s_cselect_b32 s72, 0, s72\n"
  "s_add_u32 m0, s63, 69504\n"
  "s_add_u32 s68, s73, 0\n"
  "buffer_load_dwordx4 v208, %16, s68 offen offset:0  lds\n"
  "buffer_load_dwordx4 v209, %16, s68 offen offset:1088  lds\n"
  "buffer_load_dwordx4 v210, %16, s68 offen offset:2176  lds\n"
  "buffer_load_dwordx4 v211, %16, s68 offen offset:3264  lds\n"
  "s_add_u32 m0, s63, 86880\n"
  "s_add_u32 s68, s73, 512\n"
  "buffer_load_dwordx4 v208, %16, s68 offen offset:0  lds\n"
  "buffer_load_dwordx4 v209, %16, s68 offen offset:1088  lds\n"
  "buffer_load_dwordx4 v210, %16, s68 offen offset:2176  lds\n"
  "buffer_load_dwordx4 v211, %16, s68 offen offset:3264  lds\n"
  "v_mov_b32 v144, 0x3f803f80\n"
  "v_mov_b32 v145, 0x3f803f80\n"
  "v_mov_b32 v146, 0x3f803f80\n"
  "v_mov_b32 v147, 0x3f803f80\n"
  "s_buffer_load_dword s70, %17, s74 offset:20608\n"
  "s_buffer_load_dword s70, %17, s74 offset:20672\n"
  "s_buffer_load_dword s70, %18, s74 offset:4096\n"
  "s_waitcnt vmcnt(8)\n"
  "s_barrier\n"
  "s_add_u32 m0, s63, 104256\n"
  "s_add_u32 s68, s73, 262144\n"
  "buffer_load_dwordx4 v208, %16, s68 offen offset:0  lds\n"
  "buffer_load_dwordx4 v209, %16, s68 offen offset:1088  lds\n"
  "buffer_load_dwordx4 v210, %16, s68 offen offset:2176  lds\n"
  "buffer_load_dwordx4 v211, %16, s68 offen offset:3264  lds\n"
  "s_add_u32 m0, s63, 121632\n"
  "s_add_u32 s68, s73, 262656\n"
  "buffer_load_dwordx4 v208, %16, s68 offen offset:0  lds\n"
  "buffer_load_dwordx4 v209, %16, s68 offen offset:1088  lds\n"
  "buffer_load_dwordx4 v210, %16, s68 offen offset:2176  lds\n"
  "buffer_load_dwordx4 v211, %16, s68 offen offset:3264  lds\n"
  "v_mov_b32 v148, 0x3f803f80\n"
  "v_mov_b32 v149, 0x3f803f80\n"
  "v_mov_b32 v150, 0x3f803f80\n"
  "v_mov_b32 v151, 0x3f803f80\n"
  "s_buffer_load_dword s70, %18, s74 offset:4160\n"
  "s_buffer_load_dword s70, %18, s74 offset:4224\n"
  "s_buffer_load_dword s70, %18, s74 offset:4288\n"
  "s_waitcnt vmcnt(8)\n"
  "s_barrier\n"
  "s_lshl_b32 s73, s72, 19\n"
  "s_add_u32 s73, s73, s61\n"
  "s_add_u32 s72, s72, 1\n"
  "s_cmp_eq_u32 s72, 7\n"
  "s_cselect_b32 s72, 0, s72\n"
  "s_add_u32 m0, s63, 0\n"
  "s_add_u32 s68, s73, 0\n"
  "buffer_load_dwordx4 v208, %16, s68 offen offset:0  lds\n"
  "buffer_load_dwordx4 v209, %16, s68 offen offset:1088  lds\n"
  "buffer_load_dwordx4 v210, %16, s68 offen offset:2176  lds\n"
  "buffer_load_dwordx4 v211, %16, s68 offen offset:3264  lds\n"
  "s_add_u32 m0, s63, 17376\n"
  "s_add_u32 s68, s73, 512\n"
  "buffer_load_dwordx4 v208, %16, s68 offen offset:0  lds\n"
  "buffer_load_dwordx4 v209, %16, s68 offen offset:1088  lds\n"
  "buffer_load_dwordx4 v210, %16, s68 offen offset:2176  lds\n"
  "buffer_load_dwordx4 v211, %16, s68 offen offset:3264  lds\n"
  "v_mov_b32 v152, 0x3f803f80\n"
  "v_mov_b32 v153, 0x3f803f80\n"
  "v_mov_b32 v154, 0x3f803f80\n"
  "v_mov_b32 v155, 0x3f803f80\n"
  "s_buffer_load_dword s70, %17, s74 offset:36864\n"
  "s_buffer_load_dword s70, %17, s74 offset:36928\n"
  "s_buffer_load_dword s70, %17, s74 offset:36992\n"
  "s_waitcnt vmcnt(8)\n"
  "s_barrier\n"
  "s_add_u32 m0, s63, 34752\n"
  "s_add_u32 s68, s73, 262144\n"
  "buffer_load_dwordx4 v208, %16, s68 offen offset:0  lds\n"
  "buffer_load_dwordx4 v209, %16, s68 offen offset:1088  lds\n"
  "buffer_load_dwordx4 v210, %16, s68 offen offset:2176  lds\n"
  "buffer_load_dwordx4 v211, %16, s68 offen offset:3264  lds\n"
  "s_add_u32 m0, s63, 52128\n"
  "s_add_u32 s68, s73, 262656\n"
  "buffer_load_dwordx4 v208, %16, s68 offen offset:0  lds\n"
  "buffer_load_dwordx4 v209, %16, s68 offen offset:1088  lds\n"
  "buffer_load_dwordx4 v210, %16, s68 offen offset:2176  lds\n"
  "buffer_load_dwordx4 v211, %16, s68 offen offset:3264  lds\n"
  "v_mov_b32 v156, 0x3f803f80\n"
  "v_mov_b32 v157, 0x3f803f80\n"
  "v_mov_b32 v158, 0x3f803f80\n"
  "v_mov_b32 v159, 0x3f803f80\n"
  "s_buffer_load_dword s70, %17, s74 offset:37056\n"
  "s_buffer_load_dword s70, %18, s74 offset:20480\n"
  "s_buffer_load_dword s70, %18, s74 offset:20544\n"
  "s_waitcnt vmcnt(8)\n"
  "s_barrier\n"
  "s_lshl_b32 s73, s72, 19\n"
  "s_add_u32 s73, s73, s61\n"
  "s_add_u32 s72, s72, 1\n"
  "s_cmp_eq_u32 s72, 7\n"
  "s_cselect_b32 s72, 0, s72\n"
  "s_add_u32 m0, s63, 69504\n"
  "s_add_u32 s68, s73, 0\n"
  "buffer_load_dwordx4 v208, %16, s68 offen offset:0  lds\n"
  "buffer_load_dwordx4 v209, %16, s68 offen offset:1088  lds\n"
  "buffer_load_dwordx4 v210, %16, s68 offen offset:2176  lds\n"
  "buffer_load_dwordx4 v211, %16, s68 offen offset:3264  lds\n"
  "s_add_u32 m0, s63, 86880\n"
  "s_add_u32 s68, s73, 512\n"
  "buffer_load_dwordx4 v208, %16, s68 offen offset:0  lds\n"
  "buffer_load_dwordx4 v209, %16, s68 offen offset:1088  lds\n"
  "buffer_load_dwordx4 v210, %16, s68 offen offset:2176  lds\n"
  "buffer_load_dwordx4 v211, %16, s68 offen offset:3264  lds\n"
  "buffer_load_dwordx2 v[184:185], off, %15, 0 sc1\n"
  "v_mov_b32 v160, 0x3f803f80\n"
  "v_mov_b32 v161, 0x3f803f80\n"
  "v_mov_b32 v162, 0x3f803f80\n"
  "v_mov_b32 v163, 0x3f803f80\n"
  "s_buffer_load_dword s70, %18, s74 offset:20608\n"
  "s_buffer_load_dword s70, %18, s74 offset:20672\n"
  "s_buffer_load_dword s70, %17, s74 offset:53248\n"
  "s_waitcnt vmcnt(9)\n"
  "s_barrier\n"
  "s_add_u32 m0, s63, 104256\n"
  "s_add_u32 s68, s73, 262144\n"
  "buffer_load_dwordx4 v208, %16, s68 offen offset:0  lds\n"
  "buffer_load_dwordx4 v209, %16, s68 offen offset:1088  lds\n"
  "buffer_load_dwordx4 v210, %16, s68 offen offset:2176  lds\n"
  "buffer_load_dwordx4 v211, %16, s68 offen offset:3264  lds\n"
  "s_add_u32 m0, s63, 121632\n"
  "s_add_u32 s68, s73, 262656\n"
  "buffer_load_dwordx4 v208, %16, s68 offen offset:0  lds\n"
  "buffer_load_dwordx4 v209, %16, s68 offen offset:1088  lds\n"
  "buffer_load_dwordx4 v210, %16, s68 offen offset:2176  lds\n"
  "buffer_load_dwordx4 v211, %16, s68 offen offset:3264  lds\n"
  "v_mov_b32 v164, 0x3f803f80\n"
  "v_mov_b32 v165, 0x3f803f80\n"
  "v_mov_b32 v166, 0x3f803f80\n"
  "v_mov_b32 v167, 0x3f803f80\n"
  "s_buffer_load_dword s70, %17, s74 offset:53312\n"
  "s_buffer_load_dword s70, %17, s74 offset:53376\n"
  "s_buffer_load_dword s70, %17, s74 offset:53440\n"
  "s_waitcnt vmcnt(9)\n"
  "s_barrier\n"
  "s_lshl_b32 s73, s72, 19\n"
  "s_add_u32 s73, s73, s61\n"
  "s_add_u32 s72, s72, 1\n"
  "s_cmp_eq_u32 s72, 7\n"
  "s_cselect_b32 s72, 0, s72\n"
  "s_add_u32 m0, s63, 0\n"
  "s_add_u32 s68, s73, 0\n"
  "buffer_load_dwordx4 v208, %16, s68 offen offset:0  lds\n"
  "buffer_load_dwordx4 v209, %16, s68 offen offset:1088  lds\n"
  "buffer_load_dwordx4 v210, %16, s68 offen offset:2176  lds\n"
  "buffer_load_dwordx4 v211, %16, s68 offen offset:3264  lds\n"
  "s_add_u32 m0, s63, 17376\n"
  "s_add_u32 s68, s73, 512\n"
  "buffer_load_dwordx4 v208, %16, s68 offen offset:0  lds\n"
  "buffer_load_dwordx4 v209, %16, s68 offen offset:1088  lds\n"
  "buffer_load_dwordx4 v210, %16, s68 offen offset:2176  lds\n"
  "buffer_load_dwordx4 v211, %16, s68 offen offset:3264  lds\n"
  "v_mov_b32 v168, 0x3f803f80\n"
  "v_mov_b32 v169, 0x3f803f80\n"
  "v_mov_b32 v170, 0x3f803f80\n"
  "v_mov_b32 v171, 0x3f803f80\n"
  "s_waitcnt vmcnt(8)\n"
  "s_barrier\n"
  "s_add_u32 m0, s63, 34752\n"
  "s_add_u32 s68, s73, 262144\n"
  "buffer_load_dwordx4 v208, %16, s68 offen offset:0  lds\n"
  "buffer_load_dwordx4 v209, %16, s68 offen offset:1088  lds\n"
  "buffer_load_dwordx4 v210, %16, s68 offen offset:2176  lds\n"
  "buffer_load_dwordx4 v211, %16, s68 offen offset:3264  lds\n"
  "s_add_u32 m0, s63, 52128\n"
  "s_add_u32 s68, s73, 262656\n"
  "buffer_load_dwordx4 v208, %16, s68 offen offset:0  lds\n"
  "buffer_load_dwordx4 v209, %16, s68 offen offset:1088  lds\n"
  "buffer_load_dwordx4 v210, %16, s68 offen offset:2176  lds\n"
  "buffer_load_dwordx4 v211, %16, s68 offen offset:3264  lds\n"
  "v_mov_b32 v172, 0x3f803f80\n"
  "v_mov_b32 v173, 0x3f803f80\n"
  "v_mov_b32 v174, 0x3f803f80\n"
  "v_mov_b32 v175, 0x3f803f80\n"
  "s_waitcnt vmcnt(8)\n"
  "s_barrier\n"
  "v_mov_b32 v176, 0x3f803f80\n"
  "v_mov_b32 v177, 0x3f803f80\n"
  "v_mov_b32 v178, 0x3f803f80\n"
  "v_mov_b32 v179, 0x3f803f80\n"
  "s_waitcnt vmcnt(0)\n"
  "s_barrier\n"
  "v_mov_b32 v180, 0x3f803f80\n"
  "v_mov_b32 v181, 0x3f803f80\n"
  "v_mov_b32 v182, 0x3f803f80\n"
  "v_mov_b32 v183, 0x3f803f80\n"
  "s_waitcnt vmcnt(0) lgkmcnt(0)\n"
        : "={v[128:131]}"(xf[0]), "={v[132:135]}"(xf[1]), "={v[136:139]}"(xf[2]), "={v[140:143]}"(xf[3]), "={v[144:147]}"(xf[4]), "={v[148:151]}"(xf[5]), "={v[152:155]}"(xf[6]), "={v[156:159]}"(xf[7]), "={v[160:163]}"(xf[8]), "={v[164:167]}"(xf[9]), "={v[168:171]}"(xf[10]), "={v[172:175]}"(xf[11]), "={v[176:179]}"(xf[12]), "={v[180:183]}"(xf[13]), "={v[184:185]}"(ep)
        : "s"(mk_srd(ws + CTL + C_EPOCH, 8)), "s"(sw), "s"(sk), "s"(sv), "s"(so), "s"(p), "s"(ranks) : "v200","v201","v202","v203","v204","v205","v206","v207","v208","v209","v210","v211","v212","v213","v214","v215","v216","v217","v218","v219","v220","v221","v222","v223","v224","v225","v226","v227","v228","v229","v230","v231","v232","v233","v234","v235","s60","s61","s62","s63","s64","s65","s66","s67","s68","s69","s70","s71","s72","s73","s74","s75","s76","s77","s78","s79","scc","memory");
    }
  }
  const unsigned lane = lane_id();
  c.ctl = mk_srd(ws + CTL, CTL_BYTES);
  const unsigned fw = (bid & 7) * 28 + j;
  const bool ffn = j < 28;
  if (p >= 2) {
    const unsigned long long T = (((unsigned long long)ep[1] << 32) | ep[0]) + 0x9E3779B97F4A7C15ull;
    c.t0 = __builtin_amdgcn_readfirstlane((unsigned)T); c.t1 = __builtin_amdgcn_readfirstlane((unsigned)(T >> 32));
  }
  if (p == 2) {
    if (lane == 0) { u32x2 tg = {c.t0, c.t1}; *(LAS u32x2*)(c.lds + TAG_OFF) = tg; u32x4 z = {0, 0, 0, 0}; *(LAS u32x4*)(c.lds + TAG_OFF + 32) = z; }
    if (pub) {
      const f32x4 st = *(LAS f32x4*)(c.lds + ST_OFF + lane * 16);
      const unsigned rbits = pack2(st[2] * 11.0903549f, 0.f) & 0xffffu;
      const unsigned ord = (rbits & 0x8000u) ? (~rbits & 0xffffu) : (rbits | 0x8000u);
      const unsigned key = (ord << 16) | (0xffffu - (rb * 64 + lane));
      const unsigned k0 = wave_max(key), k1 = wave_max(key == k0 ? 0u : key);
      const u32x4 gv = {key, c.t0, pack2(st[0], st[1]), c.t0};
      __builtin_amdgcn_raw_buffer_store_b128(gv, c.ctl, key == k0 ? C_RC + rb * 32 : key == k1 ? C_RC + rb * 32 + 16 : 0x40000000u, 0, SC1);
    }
    drain_lds();
    unsigned pfa = 0, pfb = 0;
    bars(PFG - 1);
    { PFG_LOADS }
    bar();
    if (!ffn && lane == 0) st8(c.ctl, C_YF + bid * 8, c.t0, c.t1);
    bars(14 - PFG);
    PIN(pfa); PIN(pfb);
    const unsigned u = (bid & 7) * 7 + j;
    if (j < 7) {
      bars(L2AT - 14);
      u32x2 f[4];
      #pragma unroll
      for (int i = 0; i < 4; ++i) f[i] = ld8(c.ctl, C_YF + (i * 64 + lane) * 8);
      bars(2);
      #pragma unroll 1
      for (unsigned spin = 0; spin < SPIN_CAP; ++spin) {
        bool ok = true;
        #pragma unroll
        for (int i = 0; i < 4; ++i) ok = ok && f[i][0] == c.t0 && f[i][1] == c.t1;
        if (__all(ok)) break;
        __builtin_amdgcn_s_sleep(1);
        #pragma unroll
        for (int i = 0; i < 4; ++i) f[i] = ld8(c.ctl, C_YF + (i * 64 + lane) * 8);
      }
      asm volatile("" ::: "memory");
      if (u == 0 && lane == 0) st8(c.ctl, C_EPOCH, c.t0, c.t1);
      f32x4 yp[14];
      #pragma unroll
      for (int i = 0; i < 14; ++i) {
        if (i == 7) bar();
        yp[i] = __builtin_bit_cast(f32x4, __builtin_amdgcn_raw_buffer_load_b128(c.ctl, C_YP + (lane >> 2) * 3584 + u * 64 + (lane & 3) * 16, i * 16 * 3584, SC1));
      }
      bars(2);
      float a[4];
      #pragma unroll
      for (int e = 0; e < 4; ++e) {
        if (e == 2) bar();
        PIN(yp[0]);
        a[e] = yp[0][e];
        #pragma unroll
        for (int i = 1; i < 14; ++i) a[e] = fadd1(a[e], yp[i][e]);
        PIN(a[e]);
      }
      bar();
      #pragma unroll
      for (int e = 0; e < 4; ++e) {
        PIN(a[e]);
        a[e] += __uint_as_float(dpp<0x124>(__float_as_uint(a[e]))); a[e] += __uint_as_float(dpp<0x128>(__float_as_uint(a[e])));
        a[e] = xrow_sum(a[e]);
      }
      const unsigned e = (lane >> 2) & 3;
      st8(c.ctl, lane < 16 ? C_Y2 + (u * 16 + (lane & 3) * 4 + e) * 8 : 0x40000000u, __float_as_uint(e == 0 ? a[0] : e == 1 ? a[1] : e == 2 ? a[2] : a[3]), c.t0);
      bars(NSIDE + 2 - L2AT - 7);
    } else bars(NSIDE + 2 - 14);
  } else if (p == 3) {
    const char* gp = (const char*)((lane & 1) ? wu : wg) + (size_t)(lane >> 1) * (FF * 2) + fw * 16;
    const char* dp = (const char*)wd + (size_t)fw * 7168 + lane * 16;
    #pragma unroll
    for (int pr = 0; pr < GPER; ++pr) {
      bar();
      if (ffn) {
        #pragma unroll
        for (int i = pr < GST ? 0 : (pr - GST) * 14 / (GPER - GST); i < (pr < GST ? 0 : (pr + 1 - GST) * 14 / (GPER - GST)); ++i)
          asm volatile("s_mov_b32 m0, %0\n\ts_nop 0\n\tglobal_load_lds_dwordx4 %1, off" :: "s"(GU_OFF + i * 1024), "v"(gp + (size_t)i * 32 * (FF * 2)) : "memory");
      }
      if (ffn && pr >= 1) {
        #pragma unroll
        for (int i = (pr - 1) * 7 / (GPER - 1); i < pr * 7 / (GPER - 1); ++i)
          asm volatile("s_mov_b32 m0, %0\n\ts_nop 0\n\tglobal_load_lds_dwordx4 %1, off" :: "s"(WD_OFF + i * 1024), "v"(dp + i * 1024) : "memory");
      }
    }
    u32x4 gr = __builtin_amdgcn_raw_buffer_load_b128(c.ctl, C_RC + lane * 16, 0, SC1);
    bars(2);
    #pragma unroll 1
    for (unsigned spin = 0; spin < SPIN_CAP && !__all(gr[1] == c.t0 && gr[3] == c.t0); ++spin) { __builtin_amdgcn_s_sleep(1); asm volatile("" ::: "memory"); gr = __builtin_amdgcn_raw_buffer_load_b128(c.ctl, C_RC + lane * 16, 0, SC1); }
    const unsigned key = gr[0];
    const unsigned k0 = __builtin_amdgcn_readfirstlane(wave_max(key)), k1 = __builtin_amdgcn_readfirstlane(wave_max(key == k0 ? 0 : key));
    const unsigned r0 = (0xffffu - (k0 & 0xffffu)) & 2047, r1 = (0xffffu - (k1 & 0xffffu)) & 2047;
    if (lane == 0) { u32x4 sel = {r0, r1, k0 >> 16, k1 >> 16}; *(LAS u32x4*)(c.lds + TAG_OFF + 16) = sel; }
    if (!ffn) { drain_lds(); bars(6); }
    else {
      const unsigned g = lane >> 4, q = (lane >> 2) & 3, pp = lane & 3;
      const srd_t xs = mk_srd(x, 2048 * 896);
      const unsigned xo = ((lane & 1) ? r1 : r0) * 896 + g * 16;
      if (!(lane & 2)) {
        #pragma unroll
        for (int s = 0; s < 14; ++s) xf[s] = __builtin_amdgcn_raw_buffer_load_b128(xs, xo + s * 64, 0, 0);
      }
      const unsigned l0 = __builtin_ctzll(__ballot(key == k0) | (1ull << 63)), l1 = __builtin_ctzll(__ballot(key == k1) | (1ull << 63));
      const unsigned ms0 = __builtin_amdgcn_readlane(gr[2], l0), ms1 = __builtin_amdgcn_readlane(gr[2], l1);
      drain_lds();
      bar();
      asm volatile("s_waitcnt vmcnt(14)" ::: "memory");
      bf16x4 a0[14], a1[14];
      #pragma unroll
      for (int s = 0; s < 14; ++s) {
        a0[s] = __builtin_amdgcn_ds_read_tr16_b64_v4bf16((LAS bf16x4*)(c.lds + GU_OFF + (32 * s + 8 * g + q) * 32 + pp * 8));
        a1[s] = __builtin_amdgcn_ds_read_tr16_b64_v4bf16((LAS bf16x4*)(c.lds + GU_OFF + (32 * s + 8 * g + 4 + q) * 32 + pp * 8));
      }
      bar();
      #pragma unroll
      for (int s = 0; s < 14; ++s) PIN(xf[s]);
      f32x4 acc = {0.f, 0.f, 0.f, 0.f};
      __builtin_amdgcn_s_setprio(3);
      #pragma unroll
      for (int s = 0; s < 14; ++s) acc = __builtin_amdgcn_mfma_f32_16x16x32_bf16(__builtin_shufflevector(a0[s], a1[s], 0, 1, 2, 3, 4, 5, 6, 7), __builtin_bit_cast(bf16x8, xf[s]), acc, 0, 0, 0);
      PIN(acc);
      __builtin_amdgcn_s_setprio(HPRIO);
      bf16x4 d0[7], d1[7];
      #pragma unroll
      for (int i = 0; i < 7; ++i) {
        d0[i] = __builtin_amdgcn_ds_read_tr16_b64_v4bf16((LAS bf16x4*)(c.lds + WD_OFF + q * 896 + (64 * i + 16 * g) * 2 + pp * 8));
        d1[i] = __builtin_amdgcn_ds_read_tr16_b64_v4bf16((LAS bf16x4*)(c.lds + WD_OFF + (4 + q) * 896 + (64 * i + 16 * g) * 2 + pp * 8));
      }
      bar();
      const unsigned ms = (lane & 1) ? ms1 : ms0;
      const float nm = -lo16(ms), rs = __builtin_amdgcn_rcpf(hi16(ms));
      float hv[4];
      #pragma unroll
      for (int e = 0; e < 4; ++e) {
        const float cs = __uint_as_float(dpp<0xEE>(__float_as_uint(acc[e]))), raw = __uint_as_float(dpp<0x44>(__float_as_uint(acc[e])));
        const unsigned pre = pack2(fmul1(ffma1(cs, nm, raw), rs), 0.f) << 16;
        auto sw = __builtin_amdgcn_permlane32_swap(pre, pre, false, false);
        const float gt = __uint_as_float(sw[0]), up = __uint_as_float(sw[1]);
        hv[e] = fmul1(rbf(fmul1(gt, sigm(gt))), up);
      }
      const unsigned ha = pack2(hv[0], hv[1]), hb = pack2(hv[2], hv[3]);
      auto sa = __builtin_amdgcn_permlane16_swap(ha, ha, false, false), sb = __builtin_amdgcn_permlane16_swap(hb, hb, false, false);
      const bool on = g == ((lane & 15) >> 1);
      u32x4 bh = {on ? sa[0] : 0u, on ? sb[0] : 0u, on ? sa[1] : 0u, on ? sb[1] : 0u};
      unsigned yo = (lane & 8) ? 0x40000000u : C_YP + fw * 3584 + ((lane & 1) * DM + ((lane & 7) >> 1) * 16 + g * 4) * 4;
      PIN(bh); PIN(yo);
      bar();
      f32x4 y[7];
      __builtin_amdgcn_s_setprio(3);
      #pragma unroll
      for (int i = 0; i < 7; ++i) {
        const f32x4 z = {0.f, 0.f, 0.f, 0.f};
        y[i] = __builtin_amdgcn_mfma_f32_16x16x32_bf16(__builtin_shufflevector(d0[i], d1[i], 0, 1, 2, 3, 4, 5, 6, 7), __builtin_bit_cast(bf16x8, bh), z, 0, 0, 0);
      }
      #pragma unroll
      for (int i = 0; i < 7; ++i) PIN(y[i]);
      __builtin_amdgcn_s_setprio(HPRIO);
      #pragma unroll
      for (int i = 0; i < 7; ++i) __builtin_amdgcn_raw_buffer_store_b128(__builtin_bit_cast(u32x4, y[i]), c.ctl, yo, i * 256, SC1);
      bars(2);
      drain_vm();
      if (lane == 0) st8(c.ctl, C_YF + bid * 8, c.t0, c.t1);
    }
    st8(c.ctl, lane < 32 ? C_PF + (rb * 8 + kv) * 256 + lane * 8 : 0x40000000u, ~c.t0, ~c.t1);
    u32x4 xt[7];
    {
      const srd_t xo = mk_srd(xb, 64 * 896);
      #pragma unroll
      for (int i = 0; i < 7; ++i) xt[i] = __builtin_amdgcn_raw_buffer_load_b128(xo, ((kv >> 2) * 32 + (kv & 1) * 16 + (lane >> 2)) * 896 + ((kv >> 1) & 1) * 64 + (lane & 3) * 16, i * 128, 0);
    }
    bars(21 - GPER - 8);
    #pragma unroll
    for (int i = 0; i < 7; ++i) { *(LAS u32x4*)(c.lds + GU_OFF + i * 1024 + lane * 16) = xt[i]; bar(); }
    drain_lds(); drain_vm();
    bars(NSIDE - 28);
    st8(c.ctl, lane < 8 ? C_PF + (rb * 8 + kv) * 256 + lane * 8 : 0x40000000u, c.t0, c.t1);
    bar();
    pf0 = ld8(c.ctl, lane < 16 ? C_PF + (rb * 8 + (lane & 7)) * 256 + (lane >> 3) * 128 : 0x40000000u);
    bar();
  }
  asm volatile(".Lcode_end:");
  __builtin_amdgcn_s_setprio(0);
  }
  {
    const unsigned lane = lane_id(), slab = wid < 4 ? wid + 3 : wid - 4;
    LAS unsigned* cnt = (LAS unsigned*)(c.lds + TAG_OFF + 32);
    volatile LAS unsigned* ready = cnt;
    c.ctl = mk_srd(ws + CTL, CTL_BYTES);
    const srd_t part = mk_srd(ws + (size_t)rb * 8 * PART_WG, 8 * PART_WG), oo = mk_srd((char*)out + (size_t)rb * 64 * 896, 64 * 896);
    const unsigned row = (kv >> 2) * 32 + (kv & 1) * 16 + (lane & 15), col = slab * 64 + ((kv >> 1) & 1) * 32 + (lane >> 4) * 8;
    unsigned rdy = *ready;
    u32x2 tg = *(LAS u32x2*)(c.lds + TAG_OFF);
    u32x4 sel = *(LAS u32x4*)(c.lds + TAG_OFF + 16);
    u32x4 xw = *(LAS u32x4*)(c.lds + GU_OFF + slab * 1024 + ((lane & 15) * 4 + (lane >> 4)) * 16);
    f32x2 st = *(LAS f32x2*)(c.lds + ST_OFF + row * 16);
    asm volatile("" : "+v"(rdy), "+v"(tg), "+v"(sel), "+v"(xw), "+v"(st));
    c.t0 = __builtin_amdgcn_readfirstlane(tg[0]); c.t1 = __builtin_amdgcn_readfirstlane(tg[1]);
    if (wid == 7) {
      #pragma unroll 1
      for (unsigned spin = 0; spin < SPIN_CAP; ++spin) if (watch(c, lane, rb, pf0) == 0xff) break;
      *ready = 0xff;
    } else {
      if (wid < 4) {
        drain_vm();
        unsigned last = 0;
        if (lane == 0) last = __hip_atomic_fetch_add(cnt + 1, 1u, __ATOMIC_RELAXED, __HIP_MEMORY_SCOPE_WORKGROUP);
        if (__builtin_amdgcn_readfirstlane(last) == 3) st8(c.ctl, lane < 8 ? C_PF + (rb * 8 + kv) * 256 + 128 + lane * 8 : 0x40000000u, c.t0, c.t1);
      }
      const bool mine = rb * 64 + row == sel[0] || rb * 64 + row == sel[1];
      float upd[8];
      #pragma unroll
      for (int e = 0; e < 8; ++e) upd[e] = 0.f;
      if (__any(mine)) {
        const unsigned tk = rb * 64 + row == sel[1], o = tk ? sel[3] : sel[2];
        const float sg = rbf(sigm(lo16((o & 0x8000u) ? (o & 0x7fffu) : (~o & 0xffffu))));
        u32x2 g[8];
        #pragma unroll 1
        for (unsigned spin = 0; spin < SPIN_CAP; ++spin) {
          bool ok = true;
          #pragma unroll
          for (int e = 0; e < 8; ++e) { g[e] = ld8(c.ctl, mine ? C_Y2 + (tk * DM + col + e) * 8 : 0x40000000u); ok = ok && g[e][1] == c.t0; }
          if (__all(ok || !mine)) break;
          __builtin_amdgcn_s_sleep(1);
        }
        #pragma unroll
        for (int e = 0; e < 8; ++e) upd[e] = mine ? rbf(rbf(__uint_as_float(g[e][0])) * sg) : 0.f;
      }
      const float rs = __builtin_amdgcn_rcpf(st[1]), nm = -st[0] * rs;
      float gs[8];
      #pragma unroll
      for (int e = 0; e < 8; ++e) { const float b = __builtin_fmaf((e & 1) ? hi16(xw[e >> 1]) : lo16(xw[e >> 1]), rs, nm) + upd[e]; gs[e] = b * sigm(b); PIN(gs[e]); }
      const unsigned sh = slab <= LVA ? 0 : 4;
      u32x4 v[4][2];
      unsigned done = 0, gi[4];
      #pragma unroll
      for (int st_ = 0; st_ < 4; ++st_) {
        unsigned m;
        while ((m = (__builtin_amdgcn_readfirstlane(rdy) >> sh) & 15 & ~done) == 0) { __builtin_amdgcn_s_sleep(1); rdy = *ready; }
        asm volatile("" ::: "memory");
        gi[st_] = __builtin_ctz(m);
        done |= 1u << gi[st_];
        #pragma unroll
        for (int k = 0; k < 2; ++k) v[st_][k] = __builtin_amdgcn_raw_buffer_load_b128(part, slab * 8192 + kv * 1024 + lane * 16, (gi[st_] * 2 + k) * PART_WG, SC1);
      }
      float pa[8], pb[8], s[8];
      #pragma unroll
      for (int e = 0; e < 8; ++e) { pa[e] = 0.f; pb[e] = 0.f; }
      #pragma unroll
      for (int st_ = 0; st_ < 4; ++st_) {
        const float wb = (gi[st_] >> 1) ? 1.f : 0.f, wa = 1.f - wb;
        #pragma unroll
        for (int e = 0; e < 4; ++e) {
          const float l = lo16(v[st_][0][e]) + lo16(v[st_][1][e]);
          const float u = hi16(v[st_][0][e]) + hi16(v[st_][1][e]);
          pa[2 * e] = __builtin_fmaf(l, wa, pa[2 * e]); pb[2 * e] = __builtin_fmaf(l, wb, pb[2 * e]);
          pa[2 * e + 1] = __builtin_fmaf(u, wa, pa[2 * e + 1]); pb[2 * e + 1] = __builtin_fmaf(u, wb, pb[2 * e + 1]);
        }
        #pragma unroll
        for (int e = 0; e < 8; ++e) { PIN(pa[e]); PIN(pb[e]); }
      }
      #pragma unroll
      for (int e = 0; e < 8; ++e) s[e] = pa[e] + pb[e];
      u32x4 o;
      #pragma unroll
      for (int e = 0; e < 4; ++e) o[e] = pack2(__builtin_fmaf(s[2 * e], gs[2 * e], lo16(xw[e])), __builtin_fmaf(s[2 * e + 1], gs[2 * e + 1], hi16(xw[e])));
      __builtin_amdgcn_raw_buffer_store_b128(o, oo, row * 896 + col * 2, 0, 0);
    }
  }
}

extern "C" void kernel_launch(void* const* d_in, const int* in_sizes, int n_in, void* d_out, int out_size, void* d_ws, size_t ws_size, hipStream_t stream) {
  k_main<<<256, 512, 0, stream>>>((const u16*)d_in[0], (const u16*)d_in[1], (const u16*)d_in[2], (const u16*)d_in[3], (const u16*)d_in[4], (const u16*)d_in[6],
                                  (const u16*)d_in[7], (const u16*)d_in[8], (const u16*)d_in[9], (char*)d_ws, (u16*)d_out);
}
```
